```python
import torch

def _softmax(x, axis=-1):
    return torch.softmax(x, dim=axis)
def _gelu(x):
    return torch.nn.functional.gelu(x, approximate="tanh")
def _silu(x):
    return torch.nn.functional.silu(x)
def _sigmoid(x):
    return torch.sigmoid(x)
def _layernorm(x, axis=-1, eps=1e-5):
    m = x.mean(dim=axis, keepdim=True)
    v = x.var(dim=axis, keepdim=True, unbiased=False)
    return (x - m) / torch.sqrt(v + eps)
def _scatter_add(tgt, idx, src, axis=0):
    return tgt.clone().index_add_(axis, idx, src)
def _segment_sum(src, seg, n):
    out = torch.zeros((n,) + src.shape[1:], dtype=src.dtype)
    return out.index_add_(0, seg, src)
def _pad(x, pw):
    flat = tuple(v for pair in reversed(pw) for v in pair)
    return torch.nn.functional.pad(x, flat)

kernel_name = 'sequential_preLN__ssm_conv_s4d__glu_gated_swiglu__prompt'

def setup_inputs():

    _w = (lambda a: (lambda t: t.to(torch.bfloat16) if t.is_floating_point() else t)(torch.as_tensor(a)))
    def _A_setup():
        Bsz, L, D, N = 1, 2048, 512, 128
        return {
            "u": _w((lambda *s: torch.randn(*s, dtype=torch.bfloat16) * 0.05)(Bsz, L, D)),
            "log_A_real": _w((lambda *s: torch.randn(*s, dtype=torch.bfloat16) * 6.4)(D, N) * 0.5),
            "A_imag": _w((lambda *s: torch.randn(*s, dtype=torch.bfloat16) * 0.05)(D, N) * 3.14),
            "C_real": _w((lambda *s: torch.randn(*s, dtype=torch.bfloat16) * 0.05)(D, N) / N ** 0.5),
            "C_imag": _w((lambda *s: torch.randn(*s, dtype=torch.bfloat16) * 0.05)(D, N) / N ** 0.5),
            "log_dt": _w((lambda *s: torch.randn(*s, dtype=torch.bfloat16) * 0.05)(D) * 0.5 - 4.0),
            "D_skip": _w((lambda *s: torch.randn(*s, dtype=torch.bfloat16) * 0.05)(D)),
        }
    _A_in = _A_setup()

    def _B_setup():
        B, S, D, F = 1, 2048, 512, 1792
        return {
            "x": _w((lambda *s: torch.randn(*s, dtype=torch.bfloat16) * 0.05)(B, S, D)),
            "w1": _w((lambda *s: torch.randn(*s, dtype=torch.bfloat16) * 0.4)(D, F) * (2.0 / D) ** 0.5),
            "w3": _w((lambda *s: torch.randn(*s, dtype=torch.bfloat16) * 0.4)(D, F) * (2.0 / D) ** 0.5),
            "w2": _w((lambda *s: torch.randn(*s, dtype=torch.bfloat16) * 0.4)(F, D) * (2.0 / F) ** 0.5),
        }
    _B_in = _B_setup()

    return {
        "A_u": _A_in["u"],
        "A_log_A_real": _A_in["log_A_real"],
        "A_A_imag": _A_in["A_imag"],
        "A_C_real": _A_in["C_real"],
        "A_C_imag": _A_in["C_imag"],
        "A_log_dt": _A_in["log_dt"],
        "A_D_skip": _A_in["D_skip"],
        "B_x": _B_in["x"],
        "B_w1": _B_in["w1"],
        "B_w3": _B_in["w3"],
        "B_w2": _B_in["w2"],
    }

def reference(A_u, A_log_A_real, A_A_imag, A_C_real, A_C_imag, A_log_dt, A_D_skip, B_x, B_w1, B_w3, B_w2):
    def _A_ref(u, log_A_real, A_imag, C_real, C_imag, log_dt, D_skip):
        L = u.shape[1]
        dt = torch.exp(log_dt)
        A = -torch.exp(log_A_real) + 1j * A_imag
        C = (C_real + 1j * C_imag) * dt[:, None]
        pos = (lambda x, dt: x.to(dt))(torch.arange(L), log_dt.dtype)
        P = torch.exp((A * dt[:, None])[:, :, None] * pos[None, None, :])
        K = 2.0 * torch.einsum("dn,dnl->dl", C, P).real
        L2 = 2 * L
        u_t = (lambda x, *d: x.permute(*d))(u, 0, 2, 1)
        Kf = (lambda x, *a, **k: torch.fft.rfft(x.float(), *a, **k))(K, L2)
        Uf = (lambda x, *a, **k: torch.fft.rfft(x.float(), *a, **k))(u_t, L2)
        y = (lambda x, *a, **k: torch.fft.irfft(x, *a, **k).to(torch.bfloat16))(Uf * Kf[None, :, :], L2)[:, :, :L]
        y = (lambda x, *d: x.permute(*d))(y, 0, 2, 1)
        return y + u * D_skip[None, None, :]

    def _B_ref(x, w1, w3, w2):
        gate = torch.einsum("bsd,df->bsf", x, w1)
        up = torch.einsum("bsd,df->bsf", x, w3)
        h = _silu(gate) * up
        out = torch.einsum("bsf,fd->bsd", h, w2)
        return out

    x = A_u
    h = x + _A_ref(_layernorm(x, axis=-1), A_log_A_real, A_A_imag, A_C_real, A_C_imag, A_log_dt, A_D_skip)
    h2 = h + _B_ref(_layernorm(h, axis=-1), B_w1, B_w3, B_w2)
    return h2
```

```cpp
#include <hip/hip_runtime.h>
#include <hip/hip_bf16.h>
typedef __hip_bfloat16 bf16;
constexpr int L=2048, D=512, N=128, F=1792;
__device__ __forceinline__ float bfr(float x){ return __bfloat162float(__float2bfloat16(x)); }

__global__ void k_ln(const bf16* x, bf16* u){
  int t=blockIdx.x, l=threadIdx.x; float v[8]; float s=0;
  for(int i=0;i<8;i++){ v[i]=__bfloat162float(x[t*D+l*8+i]); s+=v[i]; }
  for(int o=32;o;o>>=1) s+=__shfl_xor(s,o);
  float m=s/D; float q=0; for(int i=0;i<8;i++){ float d=v[i]-m; q+=d*d; }
  for(int o=32;o;o>>=1) q+=__shfl_xor(q,o);
  float var=bfr(q/D); float mb=bfr(m);
  float den=bfr(sqrtf(bfr(var+1e-5f)));
  for(int i=0;i<8;i++) u[t*D+l*8+i]=__float2bfloat16(bfr(v[i]-mb)/den);
}
__global__ void k_kgen(const bf16* lar,const bf16* aim,const bf16* cr,const bf16* ci,const bf16* ldt,float* K){
  int d=blockIdx.x; __shared__ float zr[N],zi[N],c0[N],c1[N];
  float dt=bfr(expf(__bfloat162float(ldt[d])));
  if(threadIdx.x<N){ int n=threadIdx.x; zr[n]=-bfr(expf(__bfloat162float(lar[d*N+n])))*dt; zi[n]=__bfloat162float(aim[d*N+n])*dt;
    c0[n]=__bfloat162float(cr[d*N+n])*dt; c1[n]=__bfloat162float(ci[d*N+n])*dt; }
  __syncthreads();
  for(int l=threadIdx.x;l<L;l+=blockDim.x){ float p=bfr((float)l); float s=0;
    for(int n=0;n<N;n++){ float e=expf(zr[n]*p); float sn,cs; sincosf(zi[n]*p,&sn,&cs); s+=c0[n]*e*cs-c1[n]*e*sn; }
    K[d*L+l]=2.f*s; }
}
__global__ void k_conv(const bf16* x,const bf16* u,const float* K,const bf16* dsk,bf16* h){
  int d=blockIdx.x; __shared__ float su[L], sk[L];
  for(int l=threadIdx.x;l<L;l+=blockDim.x){ su[l]=__bfloat162float(u[l*D+d]); sk[l]=K[d*L+l]; }
  __syncthreads();
  float ds=__bfloat162float(dsk[d]);
  for(int l=threadIdx.x;l<L;l+=blockDim.x){ float s=0; for(int j=0;j<=l;j++) s+=sk[j]*su[l-j];
    float a=bfr(bfr(s)+bfr(su[l]*ds)); h[l*D+d]=__float2bfloat16(__bfloat162float(x[l*D+d])+a); }
}
__global__ void k_g1(const bf16* a,const bf16* w1,const bf16* w3,bf16* H){
  int t=blockIdx.x; __shared__ float sa[D];
  for(int i=threadIdx.x;i<D;i+=blockDim.x) sa[i]=__bfloat162float(a[t*D+i]);
  __syncthreads();
  for(int f=threadIdx.x;f<F;f+=blockDim.x){ float g=0,u=0; for(int k=0;k<D;k++){ g+=sa[k]*__bfloat162float(w1[k*F+f]); u+=sa[k]*__bfloat162float(w3[k*F+f]); }
    g=bfr(g); u=bfr(u); float s=bfr(g/(1.f+expf(-g))); H[t*F+f]=__float2bfloat16(s*u); }
}
__global__ void k_g2(const bf16* H,const bf16* w2,const bf16* h,bf16* out){
  int t=blockIdx.x; __shared__ float sa[F];
  for(int i=threadIdx.x;i<F;i+=blockDim.x) sa[i]=__bfloat162float(H[t*F+i]);
  __syncthreads();
  for(int c=threadIdx.x;c<D;c+=blockDim.x){ float g=0; for(int k=0;k<F;k++) g+=sa[k]*__bfloat162float(w2[k*D+c]);
    out[t*D+c]=__float2bfloat16(__bfloat162float(h[t*D+c])+bfr(g)); }
}
extern "C" void kernel_launch(void* const* d_in, const int* in_sizes, int n_in, void* d_out, int out_size, void* d_ws, size_t ws_size, hipStream_t stream){
  const bf16* x=(const bf16*)d_in[0]; const bf16* lar=(const bf16*)d_in[1]; const bf16* aim=(const bf16*)d_in[2];
  const bf16* cr=(const bf16*)d_in[3]; const bf16* ci=(const bf16*)d_in[4]; const bf16* ldt=(const bf16*)d_in[5]; const bf16* dsk=(const bf16*)d_in[6];
  const bf16* w1=(const bf16*)d_in[8]; const bf16* w3=(const bf16*)d_in[9]; const bf16* w2=(const bf16*)d_in[10];
  char* ws=(char*)d_ws; bf16* u=(bf16*)ws; bf16* h=(bf16*)(ws+(4<<20)); bf16* a=(bf16*)(ws+(8<<20)); float* K=(float*)(ws+(12<<20)); bf16* H=(bf16*)(ws+(20<<20));
  k_ln<<<L,64,0,stream>>>(x,u);
  k_kgen<<<D,256,0,stream>>>(lar,aim,cr,ci,ldt,K);
  k_conv<<<D,256,0,stream>>>(x,u,K,dsk,h);
  k_ln<<<L,64,0,stream>>>(h,a);
  k_g1<<<L,256,0,stream>>>(a,w1,w3,H);
  k_g2<<<L,256,0,stream>>>(H,w2,h,(bf16*)d_out);
}
```

```cpp
#include <hip/hip_runtime.h>
#include <hip/hip_bf16.h>

struct Srd { const void* p; unsigned bytes; unsigned fmt; };
struct KArgs { Srd x, ws, lar, aim, cr, ci, w1; const void* w3; void* out; Srd w2; const void* ldt; const void* dsk; };
static_assert(sizeof(KArgs) == 160, "no padding");

__global__ __launch_bounds__(256, 1) void s4d_swiglu_fused(KArgs args) {
  __shared__ __attribute__((aligned(16))) char lds[163840];
  asm volatile(
    "s_load_dwordx8 s[8:15], %0, 0\n"
    "s_load_dwordx16 s[16:31], %0, 32\n"
    "s_load_dwordx16 s[32:47], %0, 96\n"
    "v_lshrrev_b32 v6, 6, v0\n"
    "v_and_b32 v7, 63, v0\n"
    "v_readfirstlane_b32 s5, v6\n"
    "s_getpc_b64 s[56:57]\n"
    "Lk_pcref:\n"
    "s_mov_b32 s58, Lk_tail-Lk_pcref\n"
    "s_mov_b32 s59, 0x20000\n"
    "s_mov_b32 s4, %1\n"
    "s_lshr_b32 s52, s4, 3\n"
    "s_lshl2_add_u32 s49, s52, s5\n"
    "s_lshl_b32 s49, s49, 8\n"
    "v_lshl_add_u32 v9, v7, 7, s49\n"
    "v_lshlrev_b32 v1, 4, v7\n"
    "s_mov_b64 exec, 3\n"
    "buffer_load_dword v30, v9, s[56:59], 0 offen\n"
    "s_mov_b64 exec, -1\n"
    "s_and_b32 s53, s4, 7\n"
    "s_lshl_b32 s6, s53, 5\n"
    "s_add_u32 s6, s6, s52\n"
    "s_lshl_b32 s54, s52, 16\n"
    "s_lshl4_add_u32 s53, s5, s53\n"
    "s_lshl_b32 s53, s53, 7\n"
    "s_add_u32 s54, s54, s53\n"
    "s_lshl2_add_u32 s55, s4, s5\n"
    "s_lshl2_add_u32 s49, s6, s5\n"
    "s_lshl_b32 s49, s49, 11\n"
    "v_lshlrev_b32 v6, 1, v0\n"
    "s_lshl_b32 s50, s6, 9\n"
    "v_and_b32 v3, 0xf0, v1\n"
    "v_bfe_u32 v20, v0, 4, 2\n"
    "v_mov_b32 v16, -1\n"
    "v_mov_b32 v17, -1\n"
    "v_mov_b32 v18, -1\n"
    "v_mov_b32 v19, -1\n"
    "s_mov_b32 s51, 131072\n"
    "v_mad_u32_u24 v21, s51, v20, v3\n"
    "s_lshl_b32 s60, s5, 19\n"
    "s_lshl_b32 s52, s6, 8\n"
    "s_add_u32 s60, s60, s52\n"
    "s_add_u32 s60, s60, 2621440\n"
    "s_add_u32 s61, s60, 65536\n"
    "v_and_b32 v7, 0x70, v1\n"
    "v_bfe_u32 v8, v0, 3, 3\n"
    "v_lshlrev_b32 v9, 9, v7\n"
    "v_lshl_add_u32 v109, v8, 4, v9\n"
    "s_lshl_b32 s51, s6, 2\n"
    "s_waitcnt lgkmcnt(0)\n"
    "buffer_load_ushort v120, v6, s[16:19], s50 offen\n"
    "buffer_load_ushort v121, v6, s[20:23], s50 offen\n"
    "buffer_load_ushort v122, v6, s[24:27], s50 offen\n"
    "buffer_load_ushort v123, v6, s[28:31], s50 offen\n"
    "buffer_load_dwordx4 v[112:115], v1, s[8:11], s49 offen\n"
    "buffer_load_dwordx4 v[116:119], v1, s[8:11], s49 offen offset:1024\n"
    "buffer_load_dword v110, off, s[12:15], 0 sc1\n"
    "s_load_dword s56, s[44:45], s51\n"
    "s_load_dword s58, s[46:47], s51\n"
    "buffer_store_dwordx4 v[16:19], v21, s[12:15], s60 offen sc1\n"
    "buffer_store_dwordx4 v[16:19], v21, s[12:15], s61 offen sc1\n"
    "buffer_load_dwordx4 v[100:103], v109, s[8:11], s54 offen\n"
    "buffer_load_dwordx4 v[104:107], v109, s[8:11], s54 offen offset:1024\n"
    "s_mov_b32 s49, 65536\n"
    "v_mad_u32_u24 v108, s49, v8, v7\n"
    "v_lshl_add_u32 v15, v8, 10, v7\n"
    "s_mov_b32 s48, 0xffff0000\n"
    "v_lshl_add_u32 v4, v20, 9, v3\n"
    "v_lshl_add_u32 v2, v20, 10, v3\n"
    "v_bfe_u32 v7, v0, 5, 1\n"
    "v_mov_b32 v64, 0\n"
    "v_mov_b32 v65, 0\n"
    "v_mov_b32 v66, 0\n"
    "v_mov_b32 v67, 0\n"
    "s_movk_i32 s49, 1536\n"
    "v_mad_u32_u24 v23, s49, v7, v1\n"
    "s_movk_i32 s49, 4096\n"
    "v_mad_u32_u24 v8, s49, v7, v1\n"
    "v_add_u32 v8, 16640, v8\n"
    "v_mul_u32_u24 v9, 1312, v7\n"
    "v_add_u32 v9, 25856, v9\n"
    "v_lshlrev_b32 v22, 4, v0\n"
    "v_add_u32 v22, 17152, v22\n"
    "ds_write_b128 v8, v[64:67]\n"
    "ds_write_b16 v9, v64 offset:4\n"
    "v_and_b32 v99, 48, v0\n"
    "v_and_b32 v98, 15, v0\n"
    "v_and_b32 v6, 127, v0\n"
    "v_cmp_le_u32 s[10:11], 16, v6\n"
    "v_cmp_le_u32 s[16:17], 32, v6\n"
    "v_cmp_le_u32 s[50:51], 64, v6\n"
    "v_mov_b32 v124, 0x6040200\n"
    "v_mov_b32 v125, 0xe0c0a08\n"
    "v_mov_b32 v126, 0x16141210\n"
    "v_mov_b32 v127, 0x1e1c1a18\n"
    "v_mov_b32 v128, 0x20\n"
    "v_mov_b32 v80, 0x4020202\n"
    "v_mov_b32 v81, 0x8060606\n"
    "v_mov_b32 v82, 0xc0a0a0a\n"
    "v_mov_b32 v83, 0x100e0e0e\n"
    "v_mov_b32 v84, 0x12\n"
    "v_cndmask_b32 v124, v124, v80, s[10:11]\n"
    "v_cndmask_b32 v125, v125, v81, s[10:11]\n"
    "v_cndmask_b32 v126, v126, v82, s[10:11]\n"
    "v_cndmask_b32 v127, v127, v83, s[10:11]\n"
    "v_cndmask_b32 v128, v128, v84, s[10:11]\n"
    "v_mov_b32 v80, 0x2020202\n"
    "v_mov_b32 v81, 0x6040404\n"
    "v_mov_b32 v82, 0x6060606\n"
    "v_mov_b32 v83, 0xa080808\n"
    "v_mov_b32 v84, 0xa\n"
    "v_cndmask_b32 v124, v124, v80, s[16:17]\n"
    "v_cndmask_b32 v125, v125, v81, s[16:17]\n"
    "v_cndmask_b32 v126, v126, v82, s[16:17]\n"
    "v_cndmask_b32 v127, v127, v83, s[16:17]\n"
    "v_cndmask_b32 v128, v128, v84, s[16:17]\n"
    "v_mov_b32 v80, 0x2020202\n"
    "v_mov_b32 v81, 0x4040202\n"
    "v_mov_b32 v82, 0x4040404\n"
    "v_mov_b32 v83, 0x6060604\n"
    "v_mov_b32 v84, 0x6\n"
    "v_cndmask_b32 v124, v124, v80, s[50:51]\n"
    "v_cndmask_b32 v125, v125, v81, s[50:51]\n"
    "v_cndmask_b32 v126, v126, v82, s[50:51]\n"
    "v_cndmask_b32 v127, v127, v83, s[50:51]\n"
    "v_cndmask_b32 v128, v128, v84, s[50:51]\n"
    "v_mov_b32 v7, 0\n"
    "v_addc_co_u32 v7, vcc, 0, v7, s[10:11]\n"
    "v_addc_co_u32 v7, vcc, 0, v7, s[16:17]\n"
    "v_addc_co_u32 v7, vcc, 0, v7, s[50:51]\n"
    "v_lshlrev_b32 v130, 4, v6\n"
    "v_lshrrev_b32 v129, v7, v130\n"
    "v_lshl_add_u32 v129, v7, 7, v129\n"
    "v_cmp_eq_u32 vcc, 16, v6\n"
    "v_mov_b32 v80, 0x4020200\n"
    "s_nop 0\n"
    "v_cndmask_b32 v124, v124, v80, vcc\n"
    "v_bfe_u32 v6, v0, 1, 2\n"
    "v_mul_u32_u24 v75, 4160, v6\n"
    "v_and_b32 v7, 1, v0\n"
    "v_lshl_add_u32 v75, v7, 4, v75\n"
    "v_add_u32 v75, 0, v75\n"
    "v_bfe_u32 v6, v0, 2, 2\n"
    "v_and_b32 v7, 3, v0\n"
    "v_lshlrev_b32 v74, 7, v6\n"
    "v_lshl_add_u32 v74, v99, 1, v74\n"
    "v_lshl_add_u32 v74, v7, 3, v74\n"
    "v_mul_u32_u24 v73, 544, v98\n"
    "s_lshr_b32 s8, s5, 1\n"
    "s_mul_i32 s49, s55, 9363\n"
    "s_lshr_b32 s49, s49, 20\n"
    "s_mul_i32 s50, s49, 112\n"
    "s_sub_u32 s50, s55, s50\n"
    "s_lshl_b32 s11, s50, 14\n"
    "s_lshl_b32 s52, s49, 7\n"
    "s_add_u32 s11, s11, s52\n"
    "s_lshl_b32 s44, s55, 11\n"
    "s_add_u32 s44, s44, 524288\n"
    "s_lshl_b32 s45, s5, 11\n"
    "s_add_u32 s45, s45, 155648\n"
    "s_lshl_b32 s46, s55, 5\n"
    "s_add_u32 s46, s46, 4096\n"
    "s_lshl_b32 s47, s5, 12\n"
    "s_add_u32 s47, s47, 65536\n"
    "s_lshl_b32 s54, s6, 14\n"
    "s_add_u32 s54, s54, s47\n"
    "s_add_u32 s54, s54, 5177344\n"
    "s_lshr_b32 s57, s6, 5\n"
    "s_lshl_b32 s57, s57, 7\n"
    "s_lshl_b32 s52, s5, 5\n"
    "s_add_u32 s57, s57, s52\n"
    "s_and_b32 s52, s6, 31\n"
    "s_add_u32 s57, s57, s52\n"
    "s_lshl4_add_u32 s57, s57, 65536\n"
    "s_and_b32 s62, s4, 7\n"
    "s_lshl_b32 s62, s62, 19\n"
    "s_lshr_b32 s52, s4, 5\n"
    "s_lshl_b32 s52, s52, 11\n"
    "s_add_u32 s62, s62, s52\n"
    "s_lshl_b32 s52, s5, 9\n"
    "s_add_u32 s62, s62, s52\n"
    "s_bfe_u32 s52, s4, 0x20003\n"
    "s_lshl_b32 s52, s52, 7\n"
    "s_add_u32 s62, s62, s52\n"
    "s_add_u32 s62, s62, 5242880\n"
    "s_mul_i32 s63, s8, 31008\n"
    "s_add_u32 s63, s63, 28672\n"
    "s_and_b32 s64, s5, 1\n"
    "s_lshl_b32 s64, s64, 8\n"
    "s_add_u32 s64, s64, s63\n"
    "s_waitcnt lgkmcnt(0)\n"
    "s_lshl_b32 s49, s56, 16\n"
    "s_and_b32 s50, s56, s48\n"
    "s_cmp_lt_u32 s5, 2\n"
    "s_cselect_b32 s56, s49, s50\n"
    "s_and_b32 s59, s58, s48\n"
    "s_lshl_b32 s58, s58, 16\n"
    "v_mov_b32 v144, 0x3fb8aa3b\n"
    "v_mul_f32 v144, s56, v144\n"
    "v_exp_f32 v144, v144\n"
    "v_add_u32 v74, s45, v74\n"
    "v_add3_u32 v73, v73, v99, s63\n"
    "s_and_b32 s49, s5, 1\n"
    "s_mul_i32 s49, s49, 8704\n"
    "v_add_u32 v72, s49, v73\n"
    "s_bitcmp1_b32 s5, 0\n"
    "s_cselect_b32 s6, 256, 0\n"
    "v_and_b32 v7, 0x70, v1\n"
    "v_bfe_u32 v6, v0, 4, 1\n"
    "v_bfe_u32 v9, v0, 3, 1\n"
    "v_lshl_add_u32 v6, v9, 1, v6\n"
    "v_bfe_u32 v9, v0, 5, 1\n"
    "v_lshl_add_u32 v6, v9, 2, v6\n"
    "s_movk_i32 s49, 14336\n"
    "v_mad_u32_u24 v68, v6, s49, v7\n"
    "v_add_u32 v69, 3584, v68\n"
    "v_add_u32 v70, 7168, v68\n"
    "v_add_u32 v71, 10752, v68\n"
    "s_bfe_u32 s9, s4, 0x20001\n"
    "s_mul_i32 s9, s9, 7\n"
    "s_lshr_b32 s52, s4, 5\n"
    "s_add_u32 s9, s9, s52\n"
    "s_mul_i32 s28, s5, 458752\n"
    "s_lshl_b32 s52, s9, 7\n"
    "s_add_u32 s28, s28, s52\n"
    "s_mul_i32 s30, s5, 12672\n"
    "s_add_u32 s30, s30, 16896\n"
    "s_mul_i32 s31, s5, 4224\n"
    "s_bitcmp1_b32 s4, 3\n"
    "s_cselect_b32 s20, 229376, 0\n"
    "s_cselect_b32 s22, 0, 229376\n"
    "s_add_u32 s49, s30, 4224\n"
    "s_add_u32 s50, s30, 8448\n"
    "s_bitcmp1_b32 s4, 3\n"
    "s_cselect_b32 s24, s50, s30\n"
    "s_cselect_b32 s25, s31, s49\n"
    "s_cselect_b32 s26, s30, s50\n"
    "s_cselect_b32 s27, s49, s31\n"
    "s_add_u32 s20, s20, s28\n"
    "s_add_u32 s21, s20, 114688\n"
    "s_add_u32 s22, s22, s28\n"
    "s_add_u32 s23, s22, 114688\n"
    "s_mov_b64 s[18:19], s[34:35]\n"
    "s_bitcmp1_b32 s4, 4\n"
    "s_cselect_b64 s[16:17], s[32:33], s[36:37]\n"
    "s_cselect_b64 s[32:33], s[36:37], s[32:33]\n"
    "s_cselect_b32 s10, 0, 67584\n"
    "s_cmp_ge_u32 s4, 224\n"
    "s_cselect_b32 s34, 0, s34\n"
    "s_cselect_b32 s18, 0, s18\n"
    "v_and_b32 v7, 1, v0\n"
    "v_mul_u32_u24 v24, 4160, v7\n"
    "v_bfe_u32 v6, v0, 1, 3\n"
    "v_lshl_add_u32 v24, v6, 2, v24\n"
    "v_sub_u32 v24, v24, v99\n"
    "v_lshrrev_b32 v6, 5, v0\n"
    "v_bfe_u32 v8, v0, 1, 2\n"
    "v_lshl_add_u32 v6, v6, 2, v8\n"
    "v_mul_u32_u24 v29, 272, v6\n"
    "v_bfe_u32 v6, v0, 3, 2\n"
    "v_lshl_add_u32 v29, v7, 5, v29\n"
    "v_lshl_add_u32 v29, v6, 6, v29\n"
    "v_lshl_add_u32 v28, v98, 5, v99\n"
    "s_mul_i32 s49, s8, 8320\n"
    "s_add_u32 s49, s49, 32\n"
    "s_add_u32 s49, s49, s6\n"
    "v_add3_u32 v24, s49, v24, 20\n"
    "v_add_u32 v25, 1024, v24\n"
    "v_add_u32 v26, 2048, v24\n"
    "v_add_u32 v27, 3072, v24\n"
    "s_mul_i32 s49, s8, 4608\n"
    "s_add_u32 s49, s49, 16928\n"
    "s_sub_u32 s49, s49, s6\n"
    "v_add_u32 v28, s49, v28\n"
    "s_waitcnt vmcnt(7)\n"
    "v_lshlrev_b32 v120, 16, v120\n"
    "v_lshlrev_b32 v121, 16, v121\n"
    "v_lshlrev_b32 v122, 16, v122\n"
    "v_lshlrev_b32 v123, 16, v123\n"
    "v_mul_f32 v145, 0x3fb8aa3b, v120\n"
    "v_exp_f32 v145, v145\n"
    "s_nop 0\n"
    "v_cvt_pk_bf16_f32 v147, v144, v145\n"
    "v_lshlrev_b32 v144, 16, v147\n"
    "v_and_b32 v145, s48, v147\n"
    "v_mul_f32 v145, v145, v144\n"
    "v_mul_f32 v146, v121, v144\n"
    "v_mul_f32 v145, 0xbfb8aa3b, v145\n"
    "v_mul_f32 v146, 0x3e22f983, v146\n"
    "v_add_f32 v147, v144, v144\n"
    "v_exp_f32 v158, v145\n"
    "v_fract_f32 v159, v146\n"
    "v_mul_f32 v160, 0x41000000, v145\n"
    "v_mul_f32 v161, 0x41000000, v146\n"
    "v_mul_f32 v162, 0x43000000, v145\n"
    "v_mul_f32 v163, 0x43000000, v146\n"
    "v_mul_f32 v150, v122, v147\n"
    "v_mul_f32 v151, v123, v147\n"
    "v_exp_f32 v160, v160\n"
    "v_fract_f32 v161, v161\n"
    "v_exp_f32 v162, v162\n"
    "v_fract_f32 v163, v163\n"
    "v_cos_f32 v152, v159\n"
    "v_sin_f32 v153, v159\n"
    "v_cos_f32 v154, v161\n"
    "v_sin_f32 v155, v161\n"
    "v_cos_f32 v156, v163\n"
    "v_sin_f32 v157, v163\n"
    "v_pk_mul_f32 v[152:153], v[152:153], v[158:159] op_sel:[0,0] op_sel_hi:[1,0]\n"
    "v_pk_mul_f32 v[154:155], v[154:155], v[160:161] op_sel:[0,0] op_sel_hi:[1,0]\n"
    "v_pk_mul_f32 v[156:157], v[156:157], v[162:163] op_sel:[0,0] op_sel_hi:[1,0]\n"
    "v_mov_b32 v148, 0x3f80\n"
    "v_cvt_pk_bf16_f32 v205, v154, v155\n"
    "v_cvt_pk_bf16_f32 v206, v150, -v151\n"
    "v_pk_mul_f32 v[172:173], v[150:151], v[152:153] op_sel:[0,0] op_sel_hi:[0,1]\n"
    "v_pk_mul_f32 v[158:159], v[154:155], v[154:155] op_sel:[0,0] op_sel_hi:[0,1]\n"
    "v_pk_fma_f32 v[172:173], v[150:151], v[152:153], v[172:173] op_sel:[1,1,0] op_sel_hi:[1,0,1] neg_lo:[1,0,0]\n"
    "v_pk_fma_f32 v[158:159], v[154:155], v[154:155], v[158:159] op_sel:[1,1,0] op_sel_hi:[1,0,1] neg_lo:[1,0,0]\n"
    "v_cvt_pk_bf16_f32 v207, v172, -v173\n"
    "v_cvt_pk_bf16_f32 v208, v158, v159\n"
    "v_pk_mul_f32 v[174:175], v[172:173], v[152:153] op_sel:[0,0] op_sel_hi:[0,1]\n"
    "v_pk_mul_f32 v[160:161], v[158:159], v[154:155] op_sel:[0,0] op_sel_hi:[0,1]\n"
    "v_pk_fma_f32 v[174:175], v[172:173], v[152:153], v[174:175] op_sel:[1,1,0] op_sel_hi:[1,0,1] neg_lo:[1,0,0]\n"
    "v_pk_fma_f32 v[160:161], v[158:159], v[154:155], v[160:161] op_sel:[1,1,0] op_sel_hi:[1,0,1] neg_lo:[1,0,0]\n"
    "v_cvt_pk_bf16_f32 v209, v174, -v175\n"
    "v_cvt_pk_bf16_f32 v210, v160, v161\n"
    "v_pk_mul_f32 v[176:177], v[174:175], v[152:153] op_sel:[0,0] op_sel_hi:[0,1]\n"
    "v_pk_mul_f32 v[162:163], v[158:159], v[158:159] op_sel:[0,0] op_sel_hi:[0,1]\n"
    "v_pk_fma_f32 v[176:177], v[174:175], v[152:153], v[176:177] op_sel:[1,1,0] op_sel_hi:[1,0,1] neg_lo:[1,0,0]\n"
    "v_pk_fma_f32 v[162:163], v[158:159], v[158:159], v[162:163] op_sel:[1,1,0] op_sel_hi:[1,0,1] neg_lo:[1,0,0]\n"
    "v_cvt_pk_bf16_f32 v211, v176, -v177\n"
    "s_mov_b32 m0, s64\n"
    "s_nop 0\n"
    "ds_write_addtid_b32 v148 offset:22304\n"
    "ds_write_addtid_b32 v205 offset:22848\n"
    "ds_write_addtid_b32 v206 offset:21760\n"
    "ds_write_addtid_b32 v207 offset:0\n"
    "ds_write_addtid_b32 v208 offset:23392\n"
    "ds_write_addtid_b32 v209 offset:544\n"
    "ds_write_addtid_b32 v210 offset:23936\n"
    "ds_write_addtid_b32 v211 offset:1088\n"
    "s_waitcnt vmcnt(4)\n"
    "v_readfirstlane_b32 s7, v110\n"
    "s_nop 0\n"
    "s_mul_i32 s7, s7, 1664525\n"
    "s_add_u32 s7, s7, 1013904223\n"
    "v_mov_b32 v141, s7\n"
    "v_mov_b32 v143, s7\n"
    "v_lshlrev_b32 v32, 16, v112\n"
    "v_and_b32 v33, s48, v112\n"
    "v_lshlrev_b32 v34, 16, v113\n"
    "v_and_b32 v35, s48, v113\n"
    "v_lshlrev_b32 v36, 16, v114\n"
    "v_and_b32 v37, s48, v114\n"
    "v_lshlrev_b32 v38, 16, v115\n"
    "v_and_b32 v39, s48, v115\n"
    "v_lshlrev_b32 v40, 16, v116\n"
    "v_and_b32 v41, s48, v116\n"
    "v_lshlrev_b32 v42, 16, v117\n"
    "v_and_b32 v43, s48, v117\n"
    "v_lshlrev_b32 v44, 16, v118\n"
    "v_and_b32 v45, s48, v118\n"
    "v_lshlrev_b32 v46, 16, v119\n"
    "v_and_b32 v47, s48, v119\n"
    "v_pk_mul_f32 v[136:137], v[32:33], v[32:33]\n"
    "v_pk_add_f32 v[132:133], v[32:33], v[34:35]\n"
    "v_pk_mul_f32 v[138:139], v[40:41], v[40:41]\n"
    "v_pk_add_f32 v[134:135], v[40:41], v[42:43]\n"
    "v_pk_fma_f32 v[136:137], v[34:35], v[34:35], v[136:137]\n"
    "v_pk_fma_f32 v[138:139], v[42:43], v[42:43], v[138:139]\n"
    "v_pk_fma_f32 v[136:137], v[36:37], v[36:37], v[136:137]\n"
    "v_pk_add_f32 v[132:133], v[132:133], v[36:37]\n"
    "v_pk_fma_f32 v[138:139], v[44:45], v[44:45], v[138:139]\n"
    "v_pk_add_f32 v[134:135], v[134:135], v[44:45]\n"
    "v_pk_fma_f32 v[136:137], v[38:39], v[38:39], v[136:137]\n"
    "v_pk_add_f32 v[132:133], v[132:133], v[38:39]\n"
    "v_pk_fma_f32 v[138:139], v[46:47], v[46:47], v[138:139]\n"
    "v_pk_add_f32 v[134:135], v[134:135], v[46:47]\n"
    "v_add_f32 v32, v132, v133\n"
    "v_add_f32 v33, v134, v135\n"
    "v_add_f32 v34, v136, v137\n"
    "v_add_f32 v35, v138, v139\n"
    "v_add_f32_dpp v32, v32, v32 row_ror:8 row_mask:0xf bank_mask:0xf\n"
    "v_add_f32_dpp v33, v33, v33 row_ror:8 row_mask:0xf bank_mask:0xf\n"
    "v_add_f32_dpp v34, v34, v34 row_ror:8 row_mask:0xf bank_mask:0xf\n"
    "v_add_f32_dpp v35, v35, v35 row_ror:8 row_mask:0xf bank_mask:0xf\n"
    "v_add_f32_dpp v32, v32, v32 row_ror:4 row_mask:0xf bank_mask:0xf\n"
    "v_add_f32_dpp v33, v33, v33 row_ror:4 row_mask:0xf bank_mask:0xf\n"
    "v_add_f32_dpp v34, v34, v34 row_ror:4 row_mask:0xf bank_mask:0xf\n"
    "v_add_f32_dpp v35, v35, v35 row_ror:4 row_mask:0xf bank_mask:0xf\n"
    "v_add_f32_dpp v32, v32, v32 row_ror:2 row_mask:0xf bank_mask:0xf\n"
    "v_add_f32_dpp v33, v33, v33 row_ror:2 row_mask:0xf bank_mask:0xf\n"
    "v_add_f32_dpp v34, v34, v34 row_ror:2 row_mask:0xf bank_mask:0xf\n"
    "v_add_f32_dpp v35, v35, v35 row_ror:2 row_mask:0xf bank_mask:0xf\n"
    "v_add_f32_dpp v32, v32, v32 row_ror:1 row_mask:0xf bank_mask:0xf\n"
    "v_add_f32_dpp v33, v33, v33 row_ror:1 row_mask:0xf bank_mask:0xf\n"
    "v_add_f32_dpp v34, v34, v34 row_ror:1 row_mask:0xf bank_mask:0xf\n"
    "v_add_f32_dpp v35, v35, v35 row_ror:1 row_mask:0xf bank_mask:0xf\n"
    "v_add_f32_dpp v32, v32, v32 row_bcast:15 row_mask:0xa bank_mask:0xf\n"
    "v_add_f32_dpp v33, v33, v33 row_bcast:15 row_mask:0xa bank_mask:0xf\n"
    "v_add_f32_dpp v34, v34, v34 row_bcast:15 row_mask:0xa bank_mask:0xf\n"
    "v_add_f32_dpp v35, v35, v35 row_bcast:15 row_mask:0xa bank_mask:0xf\n"
    "v_add_f32_dpp v32, v32, v32 row_bcast:31 row_mask:0xc bank_mask:0xf\n"
    "v_add_f32_dpp v33, v33, v33 row_bcast:31 row_mask:0xc bank_mask:0xf\n"
    "v_add_f32_dpp v34, v34, v34 row_bcast:31 row_mask:0xc bank_mask:0xf\n"
    "v_add_f32_dpp v35, v35, v35 row_bcast:31 row_mask:0xc bank_mask:0xf\n"
    "s_mov_b32 s49, 0x3b000000\n"
    "v_mul_f32 v32, s49, v32\n"
    "v_mul_f32 v33, s49, v33\n"
    "v_mul_f32 v36, v32, v32\n"
    "v_mul_f32 v37, v33, v33\n"
    "v_fma_f32 v34, v34, s49, -v36\n"
    "v_fma_f32 v35, v35, s49, -v37\n"
    "v_max_f32 v34, 0, v34\n"
    "v_max_f32 v35, 0, v35\n"
    "v_cvt_pk_bf16_f32 v36, v34, v35\n"
    "v_lshlrev_b32 v34, 16, v36\n"
    "v_and_b32 v35, s48, v36\n"
    "v_add_f32 v34, 0x3727c5ac, v34\n"
    "v_add_f32 v35, 0x3727c5ac, v35\n"
    "v_cvt_pk_bf16_f32 v36, v34, v35\n"
    "v_lshlrev_b32 v34, 16, v36\n"
    "v_and_b32 v35, s48, v36\n"
    "v_sqrt_f32 v34, v34\n"
    "v_sqrt_f32 v35, v35\n"
    "s_mov_b32 exec_lo, 0\n"
    "v_cvt_pk_bf16_f32 v140, v32, v34\n"
    "v_cvt_pk_bf16_f32 v142, v33, v35\n"
    "s_brev_b32 exec_hi, 1\n"
    "s_waitcnt vmcnt(2)\n"
    "buffer_store_dwordx4 v[140:143], off, s[12:15], s57 sc1\n"
    "s_mov_b64 exec, -1\n"
    "v_cvt_pk_bf16_f32 v212, v162, v163\n"
    "v_pk_mul_f32 v[178:179], v[176:177], v[152:153] op_sel:[0,0] op_sel_hi:[0,1]\n"
    "v_pk_mul_f32 v[164:165], v[162:163], v[154:155] op_sel:[0,0] op_sel_hi:[0,1]\n"
    "v_pk_fma_f32 v[178:179], v[176:177], v[152:153], v[178:179] op_sel:[1,1,0] op_sel_hi:[1,0,1] neg_lo:[1,0,0]\n"
    "v_pk_fma_f32 v[164:165], v[162:163], v[154:155], v[164:165] op_sel:[1,1,0] op_sel_hi:[1,0,1] neg_lo:[1,0,0]\n"
    "v_cvt_pk_bf16_f32 v213, v178, -v179\n"
    "v_cvt_pk_bf16_f32 v214, v164, v165\n"
    "v_pk_mul_f32 v[180:181], v[178:179], v[152:153] op_sel:[0,0] op_sel_hi:[0,1]\n"
    "v_pk_mul_f32 v[166:167], v[162:163], v[158:159] op_sel:[0,0] op_sel_hi:[0,1]\n"
    "v_pk_fma_f32 v[180:181], v[178:179], v[152:153], v[180:181] op_sel:[1,1,0] op_sel_hi:[1,0,1] neg_lo:[1,0,0]\n"
    "v_pk_fma_f32 v[166:167], v[162:163], v[158:159], v[166:167] op_sel:[1,1,0] op_sel_hi:[1,0,1] neg_lo:[1,0,0]\n"
    "v_cvt_pk_bf16_f32 v215, v180, -v181\n"
    "v_cvt_pk_bf16_f32 v216, v166, v167\n"
    "v_pk_mul_f32 v[182:183], v[180:181], v[152:153] op_sel:[0,0] op_sel_hi:[0,1]\n"
    "v_pk_mul_f32 v[168:169], v[162:163], v[160:161] op_sel:[0,0] op_sel_hi:[0,1]\n"
    "v_pk_fma_f32 v[182:183], v[180:181], v[152:153], v[182:183] op_sel:[1,1,0] op_sel_hi:[1,0,1] neg_lo:[1,0,0]\n"
    "v_pk_fma_f32 v[168:169], v[162:163], v[160:161], v[168:169] op_sel:[1,1,0] op_sel_hi:[1,0,1] neg_lo:[1,0,0]\n"
    "v_cvt_pk_bf16_f32 v217, v182, -v183\n"
    "v_cvt_pk_bf16_f32 v218, v168, v169\n"
    "v_pk_mul_f32 v[184:185], v[182:183], v[152:153] op_sel:[0,0] op_sel_hi:[0,1]\n"
    "v_pk_mul_f32 v[170:171], v[162:163], v[162:163] op_sel:[0,0] op_sel_hi:[0,1]\n"
    "v_pk_fma_f32 v[184:185], v[182:183], v[152:153], v[184:185] op_sel:[1,1,0] op_sel_hi:[1,0,1] neg_lo:[1,0,0]\n"
    "v_pk_fma_f32 v[170:171], v[162:163], v[162:163], v[170:171] op_sel:[1,1,0] op_sel_hi:[1,0,1] neg_lo:[1,0,0]\n"
    "v_cvt_pk_bf16_f32 v219, v184, -v185\n"
    "s_mov_b32 m0, s64\n"
    "s_nop 0\n"
    "ds_write_addtid_b32 v212 offset:24480\n"
    "ds_write_addtid_b32 v213 offset:1632\n"
    "ds_write_addtid_b32 v214 offset:25024\n"
    "ds_write_addtid_b32 v215 offset:2176\n"
    "ds_write_addtid_b32 v216 offset:25568\n"
    "ds_write_addtid_b32 v217 offset:2720\n"
    "ds_write_addtid_b32 v218 offset:26112\n"
    "ds_write_addtid_b32 v219 offset:3264\n"
    "v_cvt_pk_bf16_f32 v204, v170, v171\n"
    "v_pk_mul_f32 v[186:187], v[184:185], v[152:153] op_sel:[0,0] op_sel_hi:[0,1]\n"
    "v_pk_fma_f32 v[186:187], v[184:185], v[152:153], v[186:187] op_sel:[1,1,0] op_sel_hi:[1,0,1] neg_lo:[1,0,0]\n"
    "v_cvt_pk_bf16_f32 v205, v186, -v187\n"
    "v_pk_mul_f32 v[188:189], v[172:173], v[156:157] op_sel:[0,0] op_sel_hi:[0,1]\n"
    "v_pk_mul_f32 v[190:191], v[174:175], v[156:157] op_sel:[0,0] op_sel_hi:[0,1]\n"
    "v_pk_fma_f32 v[188:189], v[172:173], v[156:157], v[188:189] op_sel:[1,1,0] op_sel_hi:[1,0,1] neg_lo:[1,0,0]\n"
    "v_pk_fma_f32 v[190:191], v[174:175], v[156:157], v[190:191] op_sel:[1,1,0] op_sel_hi:[1,0,1] neg_lo:[1,0,0]\n"
    "v_cvt_pk_bf16_f32 v206, v188, -v189\n"
    "v_cvt_pk_bf16_f32 v207, v190, -v191\n"
    "v_pk_mul_f32 v[192:193], v[176:177], v[156:157] op_sel:[0,0] op_sel_hi:[0,1]\n"
    "v_pk_mul_f32 v[194:195], v[178:179], v[156:157] op_sel:[0,0] op_sel_hi:[0,1]\n"
    "v_pk_fma_f32 v[192:193], v[176:177], v[156:157], v[192:193] op_sel:[1,1,0] op_sel_hi:[1,0,1] neg_lo:[1,0,0]\n"
    "v_pk_fma_f32 v[194:195], v[178:179], v[156:157], v[194:195] op_sel:[1,1,0] op_sel_hi:[1,0,1] neg_lo:[1,0,0]\n"
    "v_cvt_pk_bf16_f32 v208, v192, -v193\n"
    "v_cvt_pk_bf16_f32 v209, v194, -v195\n"
    "v_pk_mul_f32 v[196:197], v[180:181], v[156:157] op_sel:[0,0] op_sel_hi:[0,1]\n"
    "v_pk_mul_f32 v[198:199], v[182:183], v[156:157] op_sel:[0,0] op_sel_hi:[0,1]\n"
    "v_pk_fma_f32 v[196:197], v[180:181], v[156:157], v[196:197] op_sel:[1,1,0] op_sel_hi:[1,0,1] neg_lo:[1,0,0]\n"
    "v_pk_fma_f32 v[198:199], v[182:183], v[156:157], v[198:199] op_sel:[1,1,0] op_sel_hi:[1,0,1] neg_lo:[1,0,0]\n"
    "v_cvt_pk_bf16_f32 v210, v196, -v197\n"
    "v_cvt_pk_bf16_f32 v211, v198, -v199\n"
    "s_mov_b32 m0, s64\n"
    "s_nop 0\n"
    "ds_write_addtid_b32 v204 offset:26656\n"
    "ds_write_addtid_b32 v205 offset:3808\n"
    "ds_write_addtid_b32 v206 offset:4352\n"
    "ds_write_addtid_b32 v207 offset:4896\n"
    "ds_write_addtid_b32 v208 offset:5440\n"
    "ds_write_addtid_b32 v209 offset:5984\n"
    "ds_write_addtid_b32 v210 offset:6528\n"
    "ds_write_addtid_b32 v211 offset:7072\n"
    "v_pk_mul_f32 v[200:201], v[184:185], v[156:157] op_sel:[0,0] op_sel_hi:[0,1]\n"
    "v_pk_mul_f32 v[202:203], v[186:187], v[156:157] op_sel:[0,0] op_sel_hi:[0,1]\n"
    "v_pk_fma_f32 v[200:201], v[184:185], v[156:157], v[200:201] op_sel:[1,1,0] op_sel_hi:[1,0,1] neg_lo:[1,0,0]\n"
    "v_pk_fma_f32 v[202:203], v[186:187], v[156:157], v[202:203] op_sel:[1,1,0] op_sel_hi:[1,0,1] neg_lo:[1,0,0]\n"
    "v_cvt_pk_bf16_f32 v212, v200, -v201\n"
    "v_cvt_pk_bf16_f32 v213, v202, -v203\n"
    "v_pk_mul_f32 v[174:175], v[190:191], v[156:157] op_sel:[0,0] op_sel_hi:[0,1]\n"
    "v_pk_mul_f32 v[178:179], v[194:195], v[156:157] op_sel:[0,0] op_sel_hi:[0,1]\n"
    "v_pk_fma_f32 v[174:175], v[190:191], v[156:157], v[174:175] op_sel:[1,1,0] op_sel_hi:[1,0,1] neg_lo:[1,0,0]\n"
    "v_pk_fma_f32 v[178:179], v[194:195], v[156:157], v[178:179] op_sel:[1,1,0] op_sel_hi:[1,0,1] neg_lo:[1,0,0]\n"
    "v_cvt_pk_bf16_f32 v214, v174, -v175\n"
    "v_cvt_pk_bf16_f32 v215, v178, -v179\n"
    "v_pk_mul_f32 v[182:183], v[198:199], v[156:157] op_sel:[0,0] op_sel_hi:[0,1]\n"
    "v_pk_mul_f32 v[186:187], v[202:203], v[156:157] op_sel:[0,0] op_sel_hi:[0,1]\n"
    "v_pk_fma_f32 v[182:183], v[198:199], v[156:157], v[182:183] op_sel:[1,1,0] op_sel_hi:[1,0,1] neg_lo:[1,0,0]\n"
    "v_pk_fma_f32 v[186:187], v[202:203], v[156:157], v[186:187] op_sel:[1,1,0] op_sel_hi:[1,0,1] neg_lo:[1,0,0]\n"
    "v_cvt_pk_bf16_f32 v216, v182, -v183\n"
    "v_cvt_pk_bf16_f32 v217, v186, -v187\n"
    "v_pk_mul_f32 v[190:191], v[174:175], v[156:157] op_sel:[0,0] op_sel_hi:[0,1]\n"
    "v_pk_mul_f32 v[194:195], v[178:179], v[156:157] op_sel:[0,0] op_sel_hi:[0,1]\n"
    "v_pk_fma_f32 v[190:191], v[174:175], v[156:157], v[190:191] op_sel:[1,1,0] op_sel_hi:[1,0,1] neg_lo:[1,0,0]\n"
    "v_pk_fma_f32 v[194:195], v[178:179], v[156:157], v[194:195] op_sel:[1,1,0] op_sel_hi:[1,0,1] neg_lo:[1,0,0]\n"
    "v_cvt_pk_bf16_f32 v218, v190, -v191\n"
    "v_cvt_pk_bf16_f32 v219, v194, -v195\n"
    "s_mov_b32 m0, s64\n"
    "s_nop 0\n"
    "ds_write_addtid_b32 v212 offset:7616\n"
    "ds_write_addtid_b32 v213 offset:8160\n"
    "ds_write_addtid_b32 v214 offset:8704\n"
    "ds_write_addtid_b32 v215 offset:9248\n"
    "ds_write_addtid_b32 v216 offset:9792\n"
    "ds_write_addtid_b32 v217 offset:10336\n"
    "ds_write_addtid_b32 v218 offset:10880\n"
    "ds_write_addtid_b32 v219 offset:11424\n"
    "v_mov_b32 v49, s7\n"
    "v_mov_b32 v51, s7\n"
    "v_mov_b32 v53, s7\n"
    "v_mov_b32 v55, s7\n"
    "v_mov_b32 v57, s7\n"
    "v_mov_b32 v59, s7\n"
    "v_mov_b32 v61, s7\n"
    "v_mov_b32 v63, s7\n"
    "s_waitcnt vmcnt(1)\n"
    "v_mov_b32 v48, v100\n"
    "v_mov_b32 v50, v104\n"
    "v_mov_b32 v52, v101\n"
    "v_mov_b32 v54, v105\n"
    "v_mov_b32 v56, v102\n"
    "v_mov_b32 v58, v106\n"
    "v_mov_b32 v60, v103\n"
    "v_mov_b32 v62, v107\n"
    "buffer_store_dwordx4 v[48:51], v108, s[12:15], s62 offen sc1\n"
    "s_add_u32 s62, s62, 16384\n"
    "buffer_store_dwordx4 v[52:55], v108, s[12:15], s62 offen sc1\n"
    "s_add_u32 s62, s62, 16384\n"
    "buffer_store_dwordx4 v[56:59], v108, s[12:15], s62 offen sc1\n"
    "s_add_u32 s62, s62, 16384\n"
    "buffer_store_dwordx4 v[60:63], v108, s[12:15], s62 offen sc1\n"
    "v_pk_mul_f32 v[198:199], v[182:183], v[156:157] op_sel:[0,0] op_sel_hi:[0,1]\n"
    "v_pk_mul_f32 v[202:203], v[186:187], v[156:157] op_sel:[0,0] op_sel_hi:[0,1]\n"
    "v_pk_fma_f32 v[198:199], v[182:183], v[156:157], v[198:199] op_sel:[1,1,0] op_sel_hi:[1,0,1] neg_lo:[1,0,0]\n"
    "v_pk_fma_f32 v[202:203], v[186:187], v[156:157], v[202:203] op_sel:[1,1,0] op_sel_hi:[1,0,1] neg_lo:[1,0,0]\n"
    "v_cvt_pk_bf16_f32 v204, v198, -v199\n"
    "v_cvt_pk_bf16_f32 v205, v202, -v203\n"
    "v_pk_mul_f32 v[178:179], v[194:195], v[156:157] op_sel:[0,0] op_sel_hi:[0,1]\n"
    "v_pk_mul_f32 v[186:187], v[202:203], v[156:157] op_sel:[0,0] op_sel_hi:[0,1]\n"
    "v_pk_fma_f32 v[178:179], v[194:195], v[156:157], v[178:179] op_sel:[1,1,0] op_sel_hi:[1,0,1] neg_lo:[1,0,0]\n"
    "v_pk_fma_f32 v[186:187], v[202:203], v[156:157], v[186:187] op_sel:[1,1,0] op_sel_hi:[1,0,1] neg_lo:[1,0,0]\n"
    "v_cvt_pk_bf16_f32 v206, v178, -v179\n"
    "v_cvt_pk_bf16_f32 v207, v186, -v187\n"
    "v_pk_mul_f32 v[194:195], v[178:179], v[156:157] op_sel:[0,0] op_sel_hi:[0,1]\n"
    "v_pk_mul_f32 v[202:203], v[186:187], v[156:157] op_sel:[0,0] op_sel_hi:[0,1]\n"
    "v_pk_fma_f32 v[194:195], v[178:179], v[156:157], v[194:195] op_sel:[1,1,0] op_sel_hi:[1,0,1] neg_lo:[1,0,0]\n"
    "v_pk_fma_f32 v[202:203], v[186:187], v[156:157], v[202:203] op_sel:[1,1,0] op_sel_hi:[1,0,1] neg_lo:[1,0,0]\n"
    "v_cvt_pk_bf16_f32 v208, v194, -v195\n"
    "v_cvt_pk_bf16_f32 v209, v202, -v203\n"
    "v_pk_mul_f32 v[178:179], v[194:195], v[156:157] op_sel:[0,0] op_sel_hi:[0,1]\n"
    "v_pk_mul_f32 v[186:187], v[202:203], v[156:157] op_sel:[0,0] op_sel_hi:[0,1]\n"
    "v_pk_fma_f32 v[178:179], v[194:195], v[156:157], v[178:179] op_sel:[1,1,0] op_sel_hi:[1,0,1] neg_lo:[1,0,0]\n"
    "v_pk_fma_f32 v[186:187], v[202:203], v[156:157], v[186:187] op_sel:[1,1,0] op_sel_hi:[1,0,1] neg_lo:[1,0,0]\n"
    "v_cvt_pk_bf16_f32 v210, v178, -v179\n"
    "v_cvt_pk_bf16_f32 v211, v186, -v187\n"
    "s_mov_b32 m0, s64\n"
    "s_nop 0\n"
    "ds_write_addtid_b32 v204 offset:11968\n"
    "ds_write_addtid_b32 v205 offset:12512\n"
    "ds_write_addtid_b32 v206 offset:13056\n"
    "ds_write_addtid_b32 v207 offset:13600\n"
    "ds_write_addtid_b32 v208 offset:14144\n"
    "ds_write_addtid_b32 v209 offset:14688\n"
    "ds_write_addtid_b32 v210 offset:15232\n"
    "ds_write_addtid_b32 v211 offset:15776\n"
    "s_add_u32 m0, s45, 0\n"
    "s_nop 0\n"
    "buffer_load_dwordx4 v15, s[40:43], s11 offen lds\n"
    "s_add_u32 m0, s45, 1024\n"
    "s_add_u32 s11, s11, 8192\n"
    "buffer_load_dwordx4 v15, s[40:43], s11 offen lds\n"
    "v_pk_mul_f32 v[194:195], v[178:179], v[156:157] op_sel:[0,0] op_sel_hi:[0,1]\n"
    "v_pk_mul_f32 v[202:203], v[186:187], v[156:157] op_sel:[0,0] op_sel_hi:[0,1]\n"
    "v_pk_fma_f32 v[194:195], v[178:179], v[156:157], v[194:195] op_sel:[1,1,0] op_sel_hi:[1,0,1] neg_lo:[1,0,0]\n"
    "v_pk_fma_f32 v[202:203], v[186:187], v[156:157], v[202:203] op_sel:[1,1,0] op_sel_hi:[1,0,1] neg_lo:[1,0,0]\n"
    "v_cvt_pk_bf16_f32 v212, v194, -v195\n"
    "v_cvt_pk_bf16_f32 v213, v202, -v203\n"
    "v_pk_mul_f32 v[186:187], v[202:203], v[156:157] op_sel:[0,0] op_sel_hi:[0,1]\n"
    "v_pk_mul_f32 v[174:175], v[170:171], v[154:155] op_sel:[0,0] op_sel_hi:[0,1]\n"
    "v_pk_fma_f32 v[186:187], v[202:203], v[156:157], v[186:187] op_sel:[1,1,0] op_sel_hi:[1,0,1] neg_lo:[1,0,0]\n"
    "v_pk_fma_f32 v[174:175], v[170:171], v[154:155], v[174:175] op_sel:[1,1,0] op_sel_hi:[1,0,1] neg_lo:[1,0,0]\n"
    "v_cvt_pk_bf16_f32 v214, v186, -v187\n"
    "v_cvt_pk_bf16_f32 v215, v174, v175\n"
    "v_pk_mul_f32 v[202:203], v[186:187], v[156:157] op_sel:[0,0] op_sel_hi:[0,1]\n"
    "v_pk_mul_f32 v[172:173], v[170:171], v[158:159] op_sel:[0,0] op_sel_hi:[0,1]\n"
    "v_pk_fma_f32 v[202:203], v[186:187], v[156:157], v[202:203] op_sel:[1,1,0] op_sel_hi:[1,0,1] neg_lo:[1,0,0]\n"
    "v_pk_fma_f32 v[172:173], v[170:171], v[158:159], v[172:173] op_sel:[1,1,0] op_sel_hi:[1,0,1] neg_lo:[1,0,0]\n"
    "v_cvt_pk_bf16_f32 v216, v202, -v203\n"
    "v_cvt_pk_bf16_f32 v217, v172, v173\n"
    "v_pk_mul_f32 v[186:187], v[202:203], v[156:157] op_sel:[0,0] op_sel_hi:[0,1]\n"
    "v_pk_mul_f32 v[174:175], v[170:171], v[160:161] op_sel:[0,0] op_sel_hi:[0,1]\n"
    "v_pk_fma_f32 v[186:187], v[202:203], v[156:157], v[186:187] op_sel:[1,1,0] op_sel_hi:[1,0,1] neg_lo:[1,0,0]\n"
    "v_pk_fma_f32 v[174:175], v[170:171], v[160:161], v[174:175] op_sel:[1,1,0] op_sel_hi:[1,0,1] neg_lo:[1,0,0]\n"
    "v_cvt_pk_bf16_f32 v218, v186, -v187\n"
    "v_cvt_pk_bf16_f32 v219, v174, v175\n"
    "s_mov_b32 m0, s64\n"
    "s_nop 0\n"
    "ds_write_addtid_b32 v212 offset:16320\n"
    "ds_write_addtid_b32 v213 offset:16864\n"
    "ds_write_addtid_b32 v214 offset:17408\n"
    "ds_write_addtid_b32 v215 offset:27200\n"
    "ds_write_addtid_b32 v216 offset:17952\n"
    "ds_write_addtid_b32 v217 offset:27744\n"
    "ds_write_addtid_b32 v218 offset:18496\n"
    "ds_write_addtid_b32 v219 offset:28288\n"
    "v_pk_mul_f32 v[202:203], v[186:187], v[156:157] op_sel:[0,0] op_sel_hi:[0,1]\n"
    "v_pk_mul_f32 v[172:173], v[170:171], v[162:163] op_sel:[0,0] op_sel_hi:[0,1]\n"
    "v_pk_fma_f32 v[202:203], v[186:187], v[156:157], v[202:203] op_sel:[1,1,0] op_sel_hi:[1,0,1] neg_lo:[1,0,0]\n"
    "v_pk_fma_f32 v[172:173], v[170:171], v[162:163], v[172:173] op_sel:[1,1,0] op_sel_hi:[1,0,1] neg_lo:[1,0,0]\n"
    "v_cvt_pk_bf16_f32 v204, v202, -v203\n"
    "v_cvt_pk_bf16_f32 v205, v172, v173\n"
    "v_pk_mul_f32 v[186:187], v[202:203], v[156:157] op_sel:[0,0] op_sel_hi:[0,1]\n"
    "v_pk_mul_f32 v[174:175], v[170:171], v[164:165] op_sel:[0,0] op_sel_hi:[0,1]\n"
    "v_pk_fma_f32 v[186:187], v[202:203], v[156:157], v[186:187] op_sel:[1,1,0] op_sel_hi:[1,0,1] neg_lo:[1,0,0]\n"
    "v_pk_fma_f32 v[174:175], v[170:171], v[164:165], v[174:175] op_sel:[1,1,0] op_sel_hi:[1,0,1] neg_lo:[1,0,0]\n"
    "v_cvt_pk_bf16_f32 v206, v186, -v187\n"
    "v_cvt_pk_bf16_f32 v207, v174, v175\n"
    "v_pk_mul_f32 v[202:203], v[186:187], v[156:157] op_sel:[0,0] op_sel_hi:[0,1]\n"
    "v_pk_mul_f32 v[172:173], v[170:171], v[166:167] op_sel:[0,0] op_sel_hi:[0,1]\n"
    "v_pk_fma_f32 v[202:203], v[186:187], v[156:157], v[202:203] op_sel:[1,1,0] op_sel_hi:[1,0,1] neg_lo:[1,0,0]\n"
    "v_pk_fma_f32 v[172:173], v[170:171], v[166:167], v[172:173] op_sel:[1,1,0] op_sel_hi:[1,0,1] neg_lo:[1,0,0]\n"
    "v_cvt_pk_bf16_f32 v208, v202, -v203\n"
    "v_cvt_pk_bf16_f32 v209, v172, v173\n"
    "v_pk_mul_f32 v[186:187], v[202:203], v[156:157] op_sel:[0,0] op_sel_hi:[0,1]\n"
    "v_pk_mul_f32 v[174:175], v[170:171], v[168:169] op_sel:[0,0] op_sel_hi:[0,1]\n"
    "v_pk_fma_f32 v[186:187], v[202:203], v[156:157], v[186:187] op_sel:[1,1,0] op_sel_hi:[1,0,1] neg_lo:[1,0,0]\n"
    "v_pk_fma_f32 v[174:175], v[170:171], v[168:169], v[174:175] op_sel:[1,1,0] op_sel_hi:[1,0,1] neg_lo:[1,0,0]\n"
    "v_cvt_pk_bf16_f32 v210, v186, -v187\n"
    "v_cvt_pk_bf16_f32 v211, v174, v175\n"
    "s_mov_b32 m0, s64\n"
    "s_nop 0\n"
    "ds_write_addtid_b32 v204 offset:19040\n"
    "ds_write_addtid_b32 v205 offset:28832\n"
    "ds_write_addtid_b32 v206 offset:19584\n"
    "ds_write_addtid_b32 v207 offset:29376\n"
    "ds_write_addtid_b32 v208 offset:20128\n"
    "ds_write_addtid_b32 v209 offset:29920\n"
    "ds_write_addtid_b32 v210 offset:20672\n"
    "ds_write_addtid_b32 v211 offset:30464\n"
    "v_pk_mul_f32 v[202:203], v[186:187], v[156:157] op_sel:[0,0] op_sel_hi:[0,1]\n"
    "v_pk_fma_f32 v[202:203], v[186:187], v[156:157], v[202:203] op_sel:[1,1,0] op_sel_hi:[1,0,1] neg_lo:[1,0,0]\n"
    "v_cvt_pk_bf16_f32 v212, v202, -v203\n"
    "s_mov_b32 m0, s64\n"
    "s_nop 0\n"
    "ds_write_addtid_b32 v212 offset:21216\n"
    "s_waitcnt lgkmcnt(0)\n"
    "s_barrier\n"
    "ds_read_b128 v[148:151], v73 offset:22304\n"
    "ds_read_b128 v[152:155], v72 offset:0\n"
    "ds_read_b128 v[156:159], v73 offset:17408\n"
    "ds_read_b128 v[160:163], v73 offset:22368\n"
    "ds_read_b128 v[164:167], v72 offset:64\n"
    "ds_read_b128 v[168:171], v73 offset:17472\n"
    "ds_read_b128 v[172:175], v73 offset:22432\n"
    "ds_read_b128 v[176:179], v72 offset:128\n"
    "ds_read_b128 v[180:183], v73 offset:17536\n"
    "ds_read_b128 v[184:187], v73 offset:22496\n"
    "ds_read_b128 v[188:191], v72 offset:192\n"
    "ds_read_b128 v[192:195], v73 offset:17600\n"
    "s_mul_i32 s65, s8, 1312\n"
    "s_add_u32 s65, s65, 25856\n"
    "s_add_u32 s49, s65, 8\n"
    "v_bfe_u32 v6, v0, 5, 1\n"
    "v_bfe_u32 v7, v0, 4, 1\n"
    "v_lshlrev_b32 v8, 7, v6\n"
    "v_lshl_add_u32 v8, v98, 3, v8\n"
    "v_lshl_add_u32 v76, v7, 2, v8\n"
    "v_sub_u32 v8, 2, v6\n"
    "v_lshlrev_b32 v8, v8, v98\n"
    "v_lshl_add_u32 v8, v99, 2, v8\n"
    "v_add_u32 v8, 256, v8\n"
    "s_bitcmp1_b32 s5, 0\n"
    "s_cselect_b64 vcc, -1, 0\n"
    "s_cselect_b32 s66, 30, 0\n"
    "v_cndmask_b32 v8, v76, v8, vcc\n"
    "s_waitcnt lgkmcnt(10)\n"
    "v_mfma_f32_16x16x32_bf16 v[116:119], v[152:155], v[148:151], 0\n"
    "s_waitcnt lgkmcnt(9)\n"
    "v_mfma_f32_16x16x32_bf16 v[120:123], v[156:159], v[148:151], 0\n"
    "ds_read_b128 v[196:199], v73 offset:22560\n"
    "ds_read_b128 v[200:203], v72 offset:256\n"
    "ds_read_b128 v[204:207], v73 offset:17664\n"
    "v_mad_u32_u24 v9, v6, s66, v8\n"
    "v_lshl_add_u32 v76, v8, 1, s49\n"
    "s_add_u32 s50, s49, 4\n"
    "v_lshl_add_u32 v77, v9, 1, s50\n"
    "v_cmp_eq_u32 vcc, 0, v6\n"
    "v_lshl_add_u32 v8, v99, 2, v98\n"
    "s_waitcnt lgkmcnt(10)\n"
    "v_mfma_f32_16x16x32_bf16 v[116:119], v[164:167], v[160:163], v[116:119]\n"
    "s_waitcnt lgkmcnt(9)\n"
    "v_mfma_f32_16x16x32_bf16 v[120:123], v[168:171], v[160:163], v[120:123]\n"
    "ds_read_b128 v[148:151], v73 offset:22624\n"
    "ds_read_b128 v[152:155], v72 offset:320\n"
    "ds_read_b128 v[156:159], v73 offset:17728\n"
    "s_add_u32 s50, s49, 1024\n"
    "v_lshl_add_u32 v8, v8, 1, s50\n"
    "v_mov_b32 v9, 28480\n"
    "v_cndmask_b32 v79, v9, v8, vcc\n"
    "v_cmp_eq_u32 vcc, 0x200, v1\n"
    "s_sub_u32 s49, s49, 2\n"
    "s_waitcnt lgkmcnt(10)\n"
    "v_mfma_f32_16x16x32_bf16 v[116:119], v[176:179], v[172:175], v[116:119]\n"
    "s_waitcnt lgkmcnt(9)\n"
    "v_mfma_f32_16x16x32_bf16 v[120:123], v[180:183], v[172:175], v[120:123]\n"
    "ds_read_b128 v[160:163], v73 offset:22688\n"
    "ds_read_b128 v[164:167], v72 offset:384\n"
    "ds_read_b128 v[168:171], v73 offset:17792\n"
    "v_mov_b32 v9, s49\n"
    "v_cndmask_b32 v78, v79, v9, vcc\n"
    "s_add_u32 s49, s65, 4\n"
    "v_lshl_add_u32 v129, v129, 1, s49\n"
    "v_add_u32_sdwa v80, v129, v124 dst_sel:DWORD dst_unused:UNUSED_PAD src0_sel:DWORD src1_sel:BYTE_0\n"
    "v_add_u32_sdwa v81, v129, v124 dst_sel:DWORD dst_unused:UNUSED_PAD src0_sel:DWORD src1_sel:BYTE_1\n"
    "s_waitcnt lgkmcnt(10)\n"
    "v_mfma_f32_16x16x32_bf16 v[116:119], v[188:191], v[184:187], v[116:119]\n"
    "s_waitcnt lgkmcnt(9)\n"
    "v_mfma_f32_16x16x32_bf16 v[120:123], v[192:195], v[184:187], v[120:123]\n"
    "ds_read_b128 v[172:175], v73 offset:22752\n"
    "ds_read_b128 v[176:179], v72 offset:448\n"
    "ds_read_b128 v[180:183], v73 offset:17856\n"
    "v_add_u32_sdwa v82, v129, v124 dst_sel:DWORD dst_unused:UNUSED_PAD src0_sel:DWORD src1_sel:BYTE_2\n"
    "v_add_u32_sdwa v83, v129, v124 dst_sel:DWORD dst_unused:UNUSED_PAD src0_sel:DWORD src1_sel:BYTE_3\n"
    "v_add_u32_sdwa v84, v129, v125 dst_sel:DWORD dst_unused:UNUSED_PAD src0_sel:DWORD src1_sel:BYTE_0\n"
    "v_add_u32_sdwa v85, v129, v125 dst_sel:DWORD dst_unused:UNUSED_PAD src0_sel:DWORD src1_sel:BYTE_1\n"
    "v_add_u32_sdwa v86, v129, v125 dst_sel:DWORD dst_unused:UNUSED_PAD src0_sel:DWORD src1_sel:BYTE_2\n"
    "v_add_u32_sdwa v87, v129, v125 dst_sel:DWORD dst_unused:UNUSED_PAD src0_sel:DWORD src1_sel:BYTE_3\n"
    "s_waitcnt lgkmcnt(10)\n"
    "v_mfma_f32_16x16x32_bf16 v[116:119], v[200:203], v[196:199], v[116:119]\n"
    "s_waitcnt lgkmcnt(9)\n"
    "v_mfma_f32_16x16x32_bf16 v[120:123], v[204:207], v[196:199], v[120:123]\n"
    "v_add_u32_sdwa v88, v129, v126 dst_sel:DWORD dst_unused:UNUSED_PAD src0_sel:DWORD src1_sel:BYTE_0\n"
    "v_add_u32_sdwa v89, v129, v126 dst_sel:DWORD dst_unused:UNUSED_PAD src0_sel:DWORD src1_sel:BYTE_1\n"
    "v_add_u32_sdwa v90, v129, v126 dst_sel:DWORD dst_unused:UNUSED_PAD src0_sel:DWORD src1_sel:BYTE_2\n"
    "v_add_u32_sdwa v91, v129, v126 dst_sel:DWORD dst_unused:UNUSED_PAD src0_sel:DWORD src1_sel:BYTE_3\n"
    "v_add_u32_sdwa v92, v129, v127 dst_sel:DWORD dst_unused:UNUSED_PAD src0_sel:DWORD src1_sel:BYTE_0\n"
    "v_add_u32_sdwa v93, v129, v127 dst_sel:DWORD dst_unused:UNUSED_PAD src0_sel:DWORD src1_sel:BYTE_1\n"
    "s_waitcnt lgkmcnt(7)\n"
    "v_mfma_f32_16x16x32_bf16 v[116:119], v[152:155], v[148:151], v[116:119]\n"
    "s_waitcnt lgkmcnt(6)\n"
    "v_mfma_f32_16x16x32_bf16 v[120:123], v[156:159], v[148:151], v[120:123]\n"
    "v_add_u32_sdwa v94, v129, v127 dst_sel:DWORD dst_unused:UNUSED_PAD src0_sel:DWORD src1_sel:BYTE_2\n"
    "v_add_u32_sdwa v95, v129, v127 dst_sel:DWORD dst_unused:UNUSED_PAD src0_sel:DWORD src1_sel:BYTE_3\n"
    "v_add_u32_sdwa v96, v129, v128 dst_sel:DWORD dst_unused:UNUSED_PAD src0_sel:DWORD src1_sel:BYTE_0\n"
    "s_mul_i32 s49, s8, 8320\n"
    "s_add_u32 s49, s49, 32\n"
    "v_lshl_add_u32 v97, v130, 1, s49\n"
    "s_waitcnt lgkmcnt(4)\n"
    "v_mfma_f32_16x16x32_bf16 v[116:119], v[164:167], v[160:163], v[116:119]\n"
    "s_waitcnt lgkmcnt(3)\n"
    "v_mfma_f32_16x16x32_bf16 v[120:123], v[168:171], v[160:163], v[120:123]\n"
    "s_waitcnt lgkmcnt(1)\n"
    "v_mfma_f32_16x16x32_bf16 v[116:119], v[176:179], v[172:175], v[116:119]\n"
    "s_waitcnt lgkmcnt(0)\n"
    "v_mfma_f32_16x16x32_bf16 v[120:123], v[180:183], v[172:175], v[120:123]\n"
    "s_nop 5\n"
    "v_cvt_pk_bf16_f32 v148, v116, v117\n"
    "v_cvt_pk_bf16_f32 v149, v118, v119\n"
    "v_cvt_pk_bf16_f32 v150, v120, v121\n"
    "v_cvt_pk_bf16_f32 v151, v122, v123\n"
    "ds_write_b32 v76, v148\n"
    "ds_write_b32 v77, v149\n"
    "ds_write_b16 v78, v150\n"
    "ds_write_b16_d16_hi v79, v150 offset:32\n"
    "ds_write_b16 v79, v151 offset:64\n"
    "ds_write_b16_d16_hi v79, v151 offset:96\n"
    "s_waitcnt lgkmcnt(0)\n"
    "s_barrier\n"
    "ds_read_u16 v148, v80\n"
    "ds_read_u16 v149, v81\n"
    "ds_read_u16 v150, v82\n"
    "ds_read_u16 v151, v83\n"
    "ds_read_u16 v152, v84\n"
    "ds_read_u16 v153, v85\n"
    "ds_read_u16 v154, v86\n"
    "ds_read_u16 v155, v87\n"
    "ds_read_u16 v156, v88\n"
    "ds_read_u16 v157, v89\n"
    "ds_read_u16 v158, v90\n"
    "ds_read_u16 v159, v91\n"
    "ds_read_u16 v160, v92\n"
    "ds_read_u16 v161, v93\n"
    "ds_read_u16 v162, v94\n"
    "ds_read_u16 v163, v95\n"
    "ds_read_u16 v164, v96\n"
    "ds_write_b128 v75, v[64:67]\n"
    "buffer_load_dwordx4 v[100:103], v23, s[12:15], s54 offen offset:0 sc1\n"
    "buffer_load_dwordx4 v[104:107], v23, s[12:15], s54 offen offset:512 sc1\n"
    "buffer_load_dwordx4 v[108:111], v23, s[12:15], s54 offen offset:1024 sc1\n"
    "buffer_load_dwordx4 v[112:115], v23, s[12:15], s54 offen offset:1536 sc1\n"
    "buffer_load_dwordx4 v[132:135], v23, s[12:15], s47 offen offset:0 sc1\n"
    "buffer_load_dwordx4 v[136:139], v23, s[12:15], s47 offen offset:512 sc1\n"
    "buffer_load_dwordx4 v[140:143], v23, s[12:15], s47 offen offset:1024 sc1\n"
    "buffer_load_dwordx4 v[144:147], v23, s[12:15], s47 offen offset:1536 sc1\n"
    "s_waitcnt lgkmcnt(15)\n"
    "v_lshl_or_b32 v168, v148, 16, v149\n"
    "v_lshl_or_b32 v176, v149, 16, v150\n"
    "s_waitcnt lgkmcnt(13)\n"
    "v_lshl_or_b32 v169, v150, 16, v151\n"
    "v_lshl_or_b32 v177, v151, 16, v152\n"
    "s_waitcnt lgkmcnt(11)\n"
    "v_lshl_or_b32 v170, v152, 16, v153\n"
    "v_lshl_or_b32 v178, v153, 16, v154\n"
    "s_waitcnt lgkmcnt(9)\n"
    "v_lshl_or_b32 v171, v154, 16, v155\n"
    "v_lshl_or_b32 v179, v155, 16, v156\n"
    "s_waitcnt lgkmcnt(7)\n"
    "v_lshl_or_b32 v172, v156, 16, v157\n"
    "v_lshl_or_b32 v180, v157, 16, v158\n"
    "s_waitcnt lgkmcnt(5)\n"
    "v_lshl_or_b32 v173, v158, 16, v159\n"
    "v_lshl_or_b32 v181, v159, 16, v160\n"
    "s_waitcnt lgkmcnt(3)\n"
    "v_lshl_or_b32 v174, v160, 16, v161\n"
    "v_lshl_or_b32 v182, v161, 16, v162\n"
    "s_waitcnt lgkmcnt(1)\n"
    "v_lshl_or_b32 v175, v162, 16, v163\n"
    "v_lshl_or_b32 v183, v163, 16, v164\n"
    "ds_write_b128 v97, v[168:171] offset:0\n"
    "ds_write_b128 v97, v[172:175] offset:16\n"
    "ds_write_b128 v97, v[176:179] offset:4160\n"
    "ds_write_b128 v97, v[180:183] offset:4176\n"
    "s_waitcnt vmcnt(4)\n"
    "ds_read_b64_tr_b16 a[32:33], v74 offset:0\n"
    "ds_read_b64_tr_b16 a[34:35], v74 offset:512\n"
    "ds_read_b64_tr_b16 a[36:37], v74 offset:1024\n"
    "ds_read_b64_tr_b16 a[38:39], v74 offset:1536\n"
    "v_bitop3_b32 v6, v101, v103, s7 bitop3:0x7e\n"
    "v_bitop3_b32 v7, v105, v107, s7 bitop3:0x7e\n"
    "v_bitop3_b32 v8, v109, v111, s7 bitop3:0x7e\n"
    "v_bitop3_b32 v9, v113, v115, s7 bitop3:0x7e\n"
    "v_or3_b32 v6, v6, v7, v8\n"
    "v_or_b32 v6, v6, v9\n"
    "v_cmp_ne_u32 vcc, 0, v6\n"
    "v_lshlrev_b32 v48, 16, v100\n"
    "v_and_b32 v49, s48, v100\n"
    "v_lshlrev_b32 v50, 16, v102\n"
    "v_and_b32 v51, s48, v102\n"
    "v_lshlrev_b32 v52, 16, v104\n"
    "v_and_b32 v53, s48, v104\n"
    "v_lshlrev_b32 v54, 16, v106\n"
    "v_and_b32 v55, s48, v106\n"
    "v_lshlrev_b32 v56, 16, v108\n"
    "v_and_b32 v57, s48, v108\n"
    "v_lshlrev_b32 v58, 16, v110\n"
    "v_and_b32 v59, s48, v110\n"
    "v_lshlrev_b32 v60, 16, v112\n"
    "v_and_b32 v61, s48, v112\n"
    "v_lshlrev_b32 v62, 16, v114\n"
    "v_and_b32 v63, s48, v114\n"
    "s_cbranch_vccnz Lk_xtf_1\n"
    "Lk_xtr_2:\n"
    "s_waitcnt vmcnt(0)\n"
    "v_bitop3_b32 v6, v133, v135, s7 bitop3:0x7e\n"
    "v_bitop3_b32 v7, v137, v139, s7 bitop3:0x7e\n"
    "v_bitop3_b32 v8, v141, v143, s7 bitop3:0x7e\n"
    "v_bitop3_b32 v9, v145, v147, s7 bitop3:0x7e\n"
    "v_or3_b32 v6, v6, v7, v8\n"
    "v_or_b32 v6, v6, v9\n"
    "v_cmp_ne_u32 vcc, 0, v6\n"
    "v_and_b32 v101, s48, v132\n"
    "v_lshlrev_b32 v100, 16, v132\n"
    "v_and_b32 v103, s48, v134\n"
    "v_lshlrev_b32 v102, 16, v134\n"
    "v_and_b32 v105, s48, v136\n"
    "v_lshlrev_b32 v104, 16, v136\n"
    "v_and_b32 v107, s48, v138\n"
    "v_lshlrev_b32 v106, 16, v138\n"
    "v_and_b32 v109, s48, v140\n"
    "v_lshlrev_b32 v108, 16, v140\n"
    "v_and_b32 v111, s48, v142\n"
    "v_lshlrev_b32 v110, 16, v142\n"
    "v_and_b32 v113, s48, v144\n"
    "v_lshlrev_b32 v112, 16, v144\n"
    "v_and_b32 v115, s48, v146\n"
    "v_lshlrev_b32 v114, 16, v146\n"
    "s_cbranch_vccnz Lk_stf_3\n"
    "Lk_str_4:\n"
    "v_rcp_f32 v101, v101\n"
    "v_pk_add_f32 v[32:33], v[48:49], v[100:101] op_sel:[0,0] op_sel_hi:[1,0] neg_lo:[0,1] neg_hi:[0,1]\n"
    "v_rcp_f32 v103, v103\n"
    "v_pk_add_f32 v[34:35], v[50:51], v[102:103] op_sel:[0,0] op_sel_hi:[1,0] neg_lo:[0,1] neg_hi:[0,1]\n"
    "v_rcp_f32 v105, v105\n"
    "v_pk_add_f32 v[36:37], v[52:53], v[104:105] op_sel:[0,0] op_sel_hi:[1,0] neg_lo:[0,1] neg_hi:[0,1]\n"
    "v_rcp_f32 v107, v107\n"
    "v_pk_add_f32 v[38:39], v[54:55], v[106:107] op_sel:[0,0] op_sel_hi:[1,0] neg_lo:[0,1] neg_hi:[0,1]\n"
    "v_rcp_f32 v109, v109\n"
    "v_pk_add_f32 v[40:41], v[56:57], v[108:109] op_sel:[0,0] op_sel_hi:[1,0] neg_lo:[0,1] neg_hi:[0,1]\n"
    "v_rcp_f32 v111, v111\n"
    "v_pk_add_f32 v[42:43], v[58:59], v[110:111] op_sel:[0,0] op_sel_hi:[1,0] neg_lo:[0,1] neg_hi:[0,1]\n"
    "v_rcp_f32 v113, v113\n"
    "v_pk_add_f32 v[44:45], v[60:61], v[112:113] op_sel:[0,0] op_sel_hi:[1,0] neg_lo:[0,1] neg_hi:[0,1]\n"
    "v_rcp_f32 v115, v115\n"
    "v_pk_add_f32 v[46:47], v[62:63], v[114:115] op_sel:[0,0] op_sel_hi:[1,0] neg_lo:[0,1] neg_hi:[0,1]\n"
    "v_pk_mul_f32 v[32:33], v[32:33], v[100:101] op_sel:[0,1] op_sel_hi:[1,1]\n"
    "v_pk_mul_f32 v[34:35], v[34:35], v[102:103] op_sel:[0,1] op_sel_hi:[1,1]\n"
    "v_pk_mul_f32 v[36:37], v[36:37], v[104:105] op_sel:[0,1] op_sel_hi:[1,1]\n"
    "v_pk_mul_f32 v[38:39], v[38:39], v[106:107] op_sel:[0,1] op_sel_hi:[1,1]\n"
    "v_pk_mul_f32 v[40:41], v[40:41], v[108:109] op_sel:[0,1] op_sel_hi:[1,1]\n"
    "v_pk_mul_f32 v[42:43], v[42:43], v[110:111] op_sel:[0,1] op_sel_hi:[1,1]\n"
    "v_pk_mul_f32 v[44:45], v[44:45], v[112:113] op_sel:[0,1] op_sel_hi:[1,1]\n"
    "v_pk_mul_f32 v[46:47], v[46:47], v[114:115] op_sel:[0,1] op_sel_hi:[1,1]\n"
    "v_cvt_pk_bf16_f32 v132, v32, v34\n"
    "v_cvt_pk_bf16_f32 v133, v36, v38\n"
    "v_cvt_pk_bf16_f32 v134, v40, v42\n"
    "v_cvt_pk_bf16_f32 v135, v44, v46\n"
    "v_cvt_pk_bf16_f32 v136, v33, v35\n"
    "v_cvt_pk_bf16_f32 v137, v37, v39\n"
    "v_cvt_pk_bf16_f32 v138, v41, v43\n"
    "v_cvt_pk_bf16_f32 v139, v45, v47\n"
    "ds_write_b128 v22, v[132:135] offset:0\n"
    "ds_write_b128 v22, v[136:139] offset:4608\n"
    "s_waitcnt lgkmcnt(0)\n"
    "s_barrier\n"
    "ds_read_b128 v[104:107], v28 offset:192\n"
    "ds_read2_b32 v[72:73], v24 offset0:3 offset1:2\n"
    "ds_read2_b32 v[74:75], v24 offset0:1 offset1:0\n"
    "ds_read_b128 v[108:111], v28 offset:704\n"
    "ds_read2_b32 v[76:77], v24 offset0:131 offset1:130\n"
    "ds_read2_b32 v[78:79], v24 offset0:129 offset1:128\n"
    "ds_read_b128 v[112:115], v28 offset:1216\n"
    "ds_read2_b32 v[80:81], v25 offset0:3 offset1:2\n"
    "ds_read2_b32 v[82:83], v25 offset0:1 offset1:0\n"
    "ds_read_b128 v[116:119], v28 offset:1728\n"
    "ds_read2_b32 v[84:85], v25 offset0:131 offset1:130\n"
    "ds_read2_b32 v[86:87], v25 offset0:129 offset1:128\n"
    "ds_read_b128 v[120:123], v28 offset:2240\n"
    "ds_read2_b32 v[88:89], v26 offset0:3 offset1:2\n"
    "ds_read2_b32 v[90:91], v26 offset0:1 offset1:0\n"
    "ds_read_b128 v[124:127], v28 offset:2752\n"
    "ds_read2_b32 v[92:93], v26 offset0:131 offset1:130\n"
    "ds_read2_b32 v[94:95], v26 offset0:129 offset1:128\n"
    "ds_read_b128 v[128:131], v28 offset:3264\n"
    "ds_read2_b32 v[96:97], v27 offset0:3 offset1:2\n"
    "ds_read2_b32 v[98:99], v27 offset0:1 offset1:0\n"
    "ds_read_b128 v[132:135], v28 offset:3776\n"
    "ds_read2_b32 v[100:101], v27 offset0:131 offset1:130\n"
    "ds_read2_b32 v[102:103], v27 offset0:129 offset1:128\n"
    "v_mov_b32 v15, s7\n"
    "s_cmp_eq_u32 s4, 0\n"
    "s_cbranch_scc0 Lk_noep_5\n"
    "s_mov_b64 exec, 1\n"
    "buffer_store_dword v15, off, s[12:15], 0 sc1\n"
    "s_mov_b64 exec, -1\n"
    "Lk_noep_5:\n"
    "s_waitcnt lgkmcnt(15)\n"
    "v_mfma_f32_16x16x32_bf16 v[200:203], v[104:107], v[72:75], 0\n"
    "v_mfma_f32_16x16x32_bf16 v[204:207], v[108:111], v[72:75], 0\n"
    "v_mfma_f32_16x16x32_bf16 v[204:207], v[104:107], v[76:79], v[204:207]\n"
    "v_fma_f32 v232, v32, s58, v48\n"
    "v_fma_f32 v233, v34, s58, v50\n"
    "v_mfma_f32_16x16x32_bf16 v[208:211], v[108:111], v[76:79], 0\n"
    "s_cmp_ge_u32 s55, 896\n"
    "s_cbranch_scc1 Lk_skip_6\n"
    "buffer_store_dwordx4 a[32:35], v1, s[12:15], s44 offen offset:0 sc1\n"
    "buffer_store_dwordx4 a[36:39], v1, s[12:15], s44 offen offset:1024 sc1\n"
    "Lk_skip_6:\n"
    "v_fma_f32 v234, v36, s58, v52\n"
    "v_mfma_f32_16x16x32_bf16 v[208:211], v[112:115], v[72:75], v[208:211]\n"
    "buffer_load_dword v30, off, s[12:15], 64 sc1\n"
    "v_fma_f32 v235, v38, s58, v54\n"
    "v_mfma_f32_16x16x32_bf16 v[208:211], v[104:107], v[80:83], v[208:211]\n"
    "ds_read_b128 v[168:171], v28 offset:128\n"
    "v_fma_f32 v236, v40, s58, v56\n"
    "v_mfma_f32_16x16x32_bf16 v[212:215], v[112:115], v[76:79], 0\n"
    "s_add_u32 m0, s24, 96256\n"
    "s_nop 0\n"
    "buffer_load_dwordx4 v68, s[32:35], s20 offen lds\n"
    "ds_read2_b32 v[136:137], v24 offset0:19 offset1:18\n"
    "v_mfma_f32_16x16x32_bf16 v[212:215], v[108:111], v[80:83], v[212:215]\n"
    "ds_read2_b32 v[138:139], v24 offset0:17 offset1:16\n"
    "v_fma_f32 v237, v42, s58, v58\n"
    "v_mfma_f32_16x16x32_bf16 v[216:219], v[112:115], v[80:83], 0\n"
    "ds_read_b128 v[172:175], v28 offset:640\n"
    "v_fma_f32 v238, v44, s58, v60\n"
    "s_waitcnt lgkmcnt(15)\n"
    "v_mfma_f32_16x16x32_bf16 v[212:215], v[116:119], v[72:75], v[212:215]\n"
    "ds_read2_b32 v[140:141], v24 offset0:147 offset1:146\n"
    "v_fma_f32 v239, v46, s58, v62\n"
    "v_mfma_f32_16x16x32_bf16 v[212:215], v[104:107], v[84:87], v[212:215]\n"
    "v_fma_f32 v240, v33, s59, v49\n"
    "v_fma_f32 v241, v35, s59, v51\n"
    "v_mfma_f32_16x16x32_bf16 v[216:219], v[116:119], v[76:79], v[216:219]\n"
    "ds_read2_b32 v[142:143], v24 offset0:145 offset1:144\n"
    "v_fma_f32 v242, v37, s59, v53\n"
    "v_mfma_f32_16x16x32_bf16 v[216:219], v[108:111], v[84:87], v[216:219]\n"
    "s_add_u32 m0, s24, 97312\n"
    "s_nop 0\n"
    "buffer_load_dwordx4 v69, s[32:35], s20 offen lds\n"
    "ds_read_b128 v[176:179], v28 offset:1152\n"
    "v_mfma_f32_16x16x32_bf16 v[220:223], v[116:119], v[80:83], 0\n"
    "ds_read2_b32 v[144:145], v25 offset0:19 offset1:18\n"
    "v_fma_f32 v243, v39, s59, v55\n"
    "v_mfma_f32_16x16x32_bf16 v[220:223], v[112:115], v[84:87], v[220:223]\n"
    "ds_read2_b32 v[146:147], v25 offset0:17 offset1:16\n"
    "v_fma_f32 v244, v41, s59, v57\n"
    "v_mfma_f32_16x16x32_bf16 v[224:227], v[116:119], v[84:87], 0\n"
    "ds_read_b128 v[180:183], v28 offset:1664\n"
    "v_fma_f32 v245, v43, s59, v59\n"
    "v_mfma_f32_16x16x32_bf16 v[216:219], v[120:123], v[72:75], v[216:219]\n"
    "v_fma_f32 v246, v45, s59, v61\n"
    "v_fma_f32 v247, v47, s59, v63\n"
    "s_waitcnt lgkmcnt(15)\n"
    "v_mfma_f32_16x16x32_bf16 v[216:219], v[104:107], v[88:91], v[216:219]\n"
    "ds_read2_b32 v[148:149], v25 offset0:147 offset1:146\n"
    "s_bfe_u32 s9, s4, 0x20003\n"
    "v_mfma_f32_16x16x32_bf16 v[220:223], v[120:123], v[76:79], v[220:223]\n"
    "ds_read2_b32 v[150:151], v25 offset0:145 offset1:144\n"
    "s_and_b32 s52, s4, 1\n"
    "v_mfma_f32_16x16x32_bf16 v[220:223], v[108:111], v[88:91], v[220:223]\n"
    "s_add_u32 m0, s24, 98368\n"
    "s_nop 0\n"
    "buffer_load_dwordx4 v70, s[32:35], s20 offen lds\n"
    "ds_read_b128 v[184:187], v28 offset:2176\n"
    "v_mfma_f32_16x16x32_bf16 v[224:227], v[120:123], v[80:83], v[224:227]\n"
    "ds_read2_b32 v[152:153], v26 offset0:19 offset1:18\n"
    "s_lshl2_add_u32 s9, s52, s9\n"
    "v_mfma_f32_16x16x32_bf16 v[224:227], v[112:115], v[88:91], v[224:227]\n"
    "ds_read2_b32 v[154:155], v26 offset0:17 offset1:16\n"
    "s_lshl2_add_u32 s11, s9, s5\n"
    "v_mfma_f32_16x16x32_bf16 v[228:231], v[120:123], v[84:87], 0\n"
    "s_mul_i32 s6, s11, 229376\n"
    "s_bfe_u32 s52, s4, 0x20001\n"
    "v_mfma_f32_16x16x32_bf16 v[228:231], v[116:119], v[88:91], v[228:231]\n"
    "ds_read_b128 v[188:191], v28 offset:2688\n"
    "s_mul_i32 s52, s52, 57344\n"
    "v_mfma_f32_16x16x32_bf16 v[220:223], v[124:127], v[72:75], v[220:223]\n"
    "ds_read2_b32 v[156:157], v26 offset0:147 offset1:146\n"
    "s_add_u32 s6, s6, s52\n"
    "v_mfma_f32_16x16x32_bf16 v[220:223], v[104:107], v[92:95], v[220:223]\n"
    "ds_read2_b32 v[158:159], v26 offset0:145 offset1:144\n"
    "s_lshr_b32 s52, s4, 5\n"
    "v_mfma_f32_16x16x32_bf16 v[224:227], v[124:127], v[76:79], v[224:227]\n"
    "s_add_u32 m0, s24, 99424\n"
    "s_nop 0\n"
    "buffer_load_dwordx4 v71, s[32:35], s20 offen lds\n"
    "ds_read_b128 v[192:195], v28 offset:3200\n"
    "v_mfma_f32_16x16x32_bf16 v[224:227], v[108:111], v[92:95], v[224:227]\n"
    "ds_read2_b32 v[160:161], v27 offset0:19 offset1:18\n"
    "s_lshl_b32 s52, s52, 11\n"
    "v_mfma_f32_16x16x32_bf16 v[228:231], v[124:127], v[80:83], v[228:231]\n"
    "s_add_u32 s6, s6, s52\n"
    "s_add_u32 s6, s6, 10485760\n"
    "v_mfma_f32_16x16x32_bf16 v[228:231], v[112:115], v[92:95], v[228:231]\n"
    "ds_read2_b32 v[162:163], v27 offset0:17 offset1:16\n"
    "s_cmp_ge_u32 s4, 224\n"
    "v_mfma_f32_16x16x32_bf16 v[224:227], v[128:131], v[72:75], v[224:227]\n"
    "ds_read_b128 v[196:199], v28 offset:3712\n"
    "s_cselect_b32 s6, 19922944, s6\n"
    "s_waitcnt lgkmcnt(15)\n"
    "v_mfma_f32_16x16x32_bf16 v[224:227], v[104:107], v[96:99], v[224:227]\n"
    "ds_read2_b32 v[164:165], v27 offset0:147 offset1:146\n"
    "s_lshl_b32 s47, s9, 2\n"
    "v_mfma_f32_16x16x32_bf16 v[228:231], v[128:131], v[76:79], v[228:231]\n"
    "s_add_u32 m0, s25, 96256\n"
    "s_nop 0\n"
    "buffer_load_dwordx4 v68, s[32:35], s21 offen lds\n"
    "ds_read2_b32 v[166:167], v27 offset0:145 offset1:144\n"
    "v_mfma_f32_16x16x32_bf16 v[228:231], v[108:111], v[96:99], v[228:231]\n"
    "s_bfe_u32 s52, s4, 0x20001\n"
    "s_add_u32 s47, s47, s52\n"
    "v_mfma_f32_16x16x32_bf16 v[228:231], v[132:135], v[72:75], v[228:231]\n"
    "s_lshr_b32 s54, s4, 5\n"
    "s_lshl_b32 s41, s11, 16\n"
    "v_mfma_f32_16x16x32_bf16 v[228:231], v[104:107], v[100:103], v[228:231]\n"
    "s_add_u32 s41, s41, 2621440\n"
    "s_mul_i32 s40, s54, 229376\n"
    "v_mfma_f32_16x16x32_bf16 v[200:203], v[168:171], v[136:139], v[200:203]\n"
    "ds_read_b128 v[104:107], v28 offset:64\n"
    "s_mul_i32 s52, s5, 57344\n"
    "v_mfma_f32_16x16x32_bf16 v[204:207], v[172:175], v[136:139], v[204:207]\n"
    "ds_read2_b32 v[72:73], v24 offset0:35 offset1:34\n"
    "s_add_u32 s40, s40, s52\n"
    "v_mfma_f32_16x16x32_bf16 v[204:207], v[168:171], v[140:143], v[204:207]\n"
    "ds_read2_b32 v[74:75], v24 offset0:33 offset1:32\n"
    "s_add_u32 s40, s40, 524288\n"
    "v_mfma_f32_16x16x32_bf16 v[208:211], v[172:175], v[140:143], v[208:211]\n"
    "s_add_u32 m0, s25, 97312\n"
    "s_nop 0\n"
    "buffer_load_dwordx4 v69, s[32:35], s21 offen lds\n"
    "s_mul_i32 s42, s47, 229376\n"
    "v_mfma_f32_16x16x32_bf16 v[208:211], v[176:179], v[136:139], v[208:211]\n"
    "ds_read_b128 v[108:111], v28 offset:576\n"
    "s_mul_i32 s52, s5, 57344\n"
    "s_waitcnt lgkmcnt(15)\n"
    "v_mfma_f32_16x16x32_bf16 v[208:211], v[168:171], v[144:147], v[208:211]\n"
    "ds_read2_b32 v[76:77], v24 offset0:163 offset1:162\n"
    "s_add_u32 s42, s42, s52\n"
    "v_mfma_f32_16x16x32_bf16 v[212:215], v[176:179], v[140:143], v[212:215]\n"
    "s_add_u32 s42, s42, 10485760\n"
    "s_lshl_b32 s36, s47, 16\n"
    "v_mfma_f32_16x16x32_bf16 v[212:215], v[172:175], v[144:147], v[212:215]\n"
    "ds_read2_b32 v[78:79], v24 offset0:161 offset1:160\n"
    "s_lshl_b32 s52, s54, 13\n"
    "v_mfma_f32_16x16x32_bf16 v[216:219], v[176:179], v[144:147], v[216:219]\n"
    "ds_read_b128 v[112:115], v28 offset:1088\n"
    "s_add_u32 s36, s36, s52\n"
    "v_mfma_f32_16x16x32_bf16 v[212:215], v[180:183], v[136:139], v[212:215]\n"
    "ds_read2_b32 v[80:81], v25 offset0:35 offset1:34\n"
    "s_add_u32 s36, s36, 2621440\n"
    "v_mfma_f32_16x16x32_bf16 v[212:215], v[168:171], v[148:151], v[212:215]\n"
    "s_add_u32 m0, s25, 98368\n"
    "s_nop 0\n"
    "buffer_load_dwordx4 v70, s[32:35], s21 offen lds\n"
    "s_add_u32 s37, s36, 4096\n"
    "v_mfma_f32_16x16x32_bf16 v[216:219], v[180:183], v[140:143], v[216:219]\n"
    "ds_read2_b32 v[82:83], v25 offset0:33 offset1:32\n"
    "s_lshr_b32 s43, s47, 2\n"
    "v_mfma_f32_16x16x32_bf16 v[216:219], v[172:175], v[148:151], v[216:219]\n"
    "ds_read_b128 v[116:119], v28 offset:1600\n"
    "s_lshl_b32 s43, s43, 18\n"
    "v_mfma_f32_16x16x32_bf16 v[220:223], v[180:183], v[144:147], v[220:223]\n"
    "s_bfe_u32 s52, s47, 0x10001\n"
    "s_lshl_b32 s52, s52, 17\n"
    "v_mfma_f32_16x16x32_bf16 v[220:223], v[176:179], v[148:151], v[220:223]\n"
    "ds_read2_b32 v[84:85], v25 offset0:163 offset1:162\n"
    "s_add_u32 s43, s43, s52\n"
    "v_mfma_f32_16x16x32_bf16 v[224:227], v[180:183], v[148:151], v[224:227]\n"
    "ds_read2_b32 v[86:87], v25 offset0:161 offset1:160\n"
    "s_and_b32 s52, s47, 1\n"
    "v_mfma_f32_16x16x32_bf16 v[216:219], v[184:187], v[136:139], v[216:219]\n"
    "s_lshl_b32 s52, s52, 12\n"
    "s_add_u32 s43, s43, s52\n"
    "s_waitcnt lgkmcnt(15)\n"
    "v_mfma_f32_16x16x32_bf16 v[216:219], v[168:171], v[152:155], v[216:219]\n"
    "s_add_u32 m0, s25, 99424\n"
    "s_nop 0\n"
    "buffer_load_dwordx4 v71, s[32:35], s21 offen lds\n"
    "ds_read_b128 v[120:123], v28 offset:2112\n"
    "v_mfma_f32_16x16x32_bf16 v[220:223], v[184:187], v[140:143], v[220:223]\n"
    "ds_read2_b32 v[88:89], v26 offset0:35 offset1:34\n"
    "s_lshl_b32 s52, s54, 7\n"
    "v_mfma_f32_16x16x32_bf16 v[220:223], v[172:175], v[152:155], v[220:223]\n"
    "ds_read2_b32 v[90:91], v26 offset0:33 offset1:32\n"
    "s_add_u32 s43, s43, s52\n"
    "v_mfma_f32_16x16x32_bf16 v[224:227], v[184:187], v[144:147], v[224:227]\n"
    "s_lshl_b32 s56, s5, 9\n"
    "s_lshl_b32 s45, s11, 10\n"
    "v_mfma_f32_16x16x32_bf16 v[224:227], v[176:179], v[152:155], v[224:227]\n"
    "ds_read_b128 v[124:127], v28 offset:2624\n"
    "s_add_u32 s45, s45, 131072\n"
    "v_mfma_f32_16x16x32_bf16 v[228:231], v[184:187], v[148:151], v[228:231]\n"
    "ds_read2_b32 v[92:93], v26 offset0:163 offset1:162\n"
    "s_lshl_b32 s57, s47, 7\n"
    "v_mfma_f32_16x16x32_bf16 v[228:231], v[180:183], v[152:155], v[228:231]\n"
    "s_add_u32 m0, s26, 96256\n"
    "s_nop 0\n"
    "buffer_load_dwordx4 v68, s[32:35], s22 offen lds\n"
    "s_add_u32 s57, s57, 49152\n"
    "v_mfma_f32_16x16x32_bf16 v[220:223], v[188:191], v[136:139], v[220:223]\n"
    "ds_read2_b32 v[94:95], v26 offset0:161 offset1:160\n"
    "s_lshl_b32 s62, s11, 7\n"
    "v_mfma_f32_16x16x32_bf16 v[220:223], v[168:171], v[156:159], v[220:223]\n"
    "ds_read_b128 v[128:131], v28 offset:3136\n"
    "s_add_u32 s62, s62, 40960\n"
    "v_mfma_f32_16x16x32_bf16 v[224:227], v[188:191], v[140:143], v[224:227]\n"
    "ds_read2_b32 v[96:97], v27 offset0:35 offset1:34\n"
    "v_bfe_u32 v6, v0, 2, 2\n"
    "v_mfma_f32_16x16x32_bf16 v[224:227], v[172:175], v[156:159], v[224:227]\n"
    "v_mul_u32_u24 v6, 1056, v6\n"
    "v_and_b32 v7, 3, v0\n"
    "v_mfma_f32_16x16x32_bf16 v[228:231], v[188:191], v[144:147], v[228:231]\n"
    "ds_read2_b32 v[98:99], v27 offset0:33 offset1:32\n"
    "v_lshl_add_u32 v6, v7, 3, v6\n"
    "v_mfma_f32_16x16x32_bf16 v[228:231], v[176:179], v[156:159], v[228:231]\n"
    "ds_read_b128 v[132:135], v28 offset:3648\n"
    "v_bfe_u32 v7, v0, 5, 1\n"
    "v_mfma_f32_16x16x32_bf16 v[224:227], v[192:195], v[136:139], v[224:227]\n"
    "s_add_u32 m0, s26, 97312\n"
    "s_nop 0\n"
    "buffer_load_dwordx4 v69, s[32:35], s22 offen lds\n"
    "v_bfe_u32 v8, v0, 4, 1\n"
    "v_mfma_f32_16x16x32_bf16 v[224:227], v[168:171], v[160:163], v[224:227]\n"
    "ds_read2_b32 v[100:101], v27 offset0:163 offset1:162\n"
    "v_lshl_add_u32 v6, v7, 9, v6\n"
    "v_mfma_f32_16x16x32_bf16 v[228:231], v[192:195], v[140:143], v[228:231]\n"
    "ds_read2_b32 v[102:103], v27 offset0:161 offset1:160\n"
    "v_lshl_add_u32 v6, v8, 7, v6\n"
    "v_mfma_f32_16x16x32_bf16 v[228:231], v[172:175], v[160:163], v[228:231]\n"
    "v_add_u32 v6, 28672, v6\n"
    "s_sub_u32 s49, 67584, s10\n"
    "s_waitcnt lgkmcnt(15)\n"
    "v_mfma_f32_16x16x32_bf16 v[228:231], v[196:199], v[136:139], v[228:231]\n"
    "v_add_u32 v12, s10, v6\n"
    "v_add_u32 v5, s49, v6\n"
    "v_mfma_f32_16x16x32_bf16 v[228:231], v[168:171], v[164:167], v[228:231]\n"
    "v_lshl_add_u32 v8, v8, 10, v3\n"
    "v_lshl_add_u32 v13, v7, 13, v8\n"
    "v_mfma_f32_16x16x32_bf16 v[200:203], v[104:107], v[72:75], v[200:203]\n"
    "ds_read_b128 v[168:171], v28 offset:0\n"
    "v_xor_b32 v7, 1, v7\n"
    "v_mfma_f32_16x16x32_bf16 v[204:207], v[108:111], v[72:75], v[204:207]\n"
    "s_add_u32 m0, s26, 98368\n"
    "s_nop 0\n"
    "buffer_load_dwordx4 v70, s[32:35], s22 offen lds\n"
    "ds_read2_b32 v[136:137], v24 offset0:51 offset1:50\n"
    "v_mfma_f32_16x16x32_bf16 v[204:207], v[104:107], v[76:79], v[204:207]\n"
    "ds_read2_b32 v[138:139], v24 offset0:49 offset1:48\n"
    "s_mov_b32 s49, 47104\n"
    "v_mfma_f32_16x16x32_bf16 v[208:211], v[108:111], v[76:79], v[208:211]\n"
    "v_mad_u32_u24 v14, v7, s49, v8\n"
    "v_lshlrev_b32 v10, 2, v3\n"
    "v_mfma_f32_16x16x32_bf16 v[208:211], v[112:115], v[72:75], v[208:211]\n"
    "ds_read_b128 v[172:175], v28 offset:512\n"
    "v_and_b32 v6, 48, v0\n"
    "v_mfma_f32_16x16x32_bf16 v[208:211], v[104:107], v[80:83], v[208:211]\n"
    "ds_read2_b32 v[140:141], v24 offset0:179 offset1:178\n"
    "v_add_u32 v11, s56, v6\n"
    "v_mfma_f32_16x16x32_bf16 v[212:215], v[112:115], v[76:79], v[212:215]\n"
    "v_mfma_f32_16x16x32_bf16 v[212:215], v[108:111], v[80:83], v[212:215]\n"
    "s_add_u32 m0, s26, 99424\n"
    "s_nop 0\n"
    "buffer_load_dwordx4 v71, s[32:35], s22 offen lds\n"
    "ds_read2_b32 v[142:143], v24 offset0:177 offset1:176\n"
    "v_mfma_f32_16x16x32_bf16 v[216:219], v[112:115], v[80:83], v[216:219]\n"
    "ds_read_b128 v[176:179], v28 offset:1024\n"
    "s_waitcnt lgkmcnt(15)\n"
    "v_mfma_f32_16x16x32_bf16 v[212:215], v[116:119], v[72:75], v[212:215]\n"
    "ds_read2_b32 v[144:145], v25 offset0:51 offset1:50\n"
    "v_mfma_f32_16x16x32_bf16 v[212:215], v[104:107], v[84:87], v[212:215]\n"
    "v_mfma_f32_16x16x32_bf16 v[216:219], v[116:119], v[76:79], v[216:219]\n"
    "ds_read2_b32 v[146:147], v25 offset0:49 offset1:48\n"
    "v_mfma_f32_16x16x32_bf16 v[216:219], v[108:111], v[84:87], v[216:219]\n"
    "ds_read_b128 v[180:183], v28 offset:1536\n"
    "v_mfma_f32_16x16x32_bf16 v[220:223], v[116:119], v[80:83], v[220:223]\n"
    "v_mfma_f32_16x16x32_bf16 v[220:223], v[112:115], v[84:87], v[220:223]\n"
    "s_add_u32 m0, s27, 96256\n"
    "s_nop 0\n"
    "buffer_load_dwordx4 v68, s[32:35], s23 offen lds\n"
    "ds_read2_b32 v[148:149], v25 offset0:179 offset1:178\n"
    "v_mfma_f32_16x16x32_bf16 v[224:227], v[116:119], v[84:87], v[224:227]\n"
    "ds_read2_b32 v[150:151], v25 offset0:177 offset1:176\n"
    "v_mfma_f32_16x16x32_bf16 v[216:219], v[120:123], v[72:75], v[216:219]\n"
    "v_mfma_f32_16x16x32_bf16 v[216:219], v[104:107], v[88:91], v[216:219]\n"
    "ds_read_b128 v[184:187], v28 offset:2048\n"
    "v_mfma_f32_16x16x32_bf16 v[220:223], v[120:123], v[76:79], v[220:223]\n"
    "ds_read2_b32 v[152:153], v26 offset0:51 offset1:50\n"
    "v_mfma_f32_16x16x32_bf16 v[220:223], v[108:111], v[88:91], v[220:223]\n"
    "ds_read2_b32 v[154:155], v26 offset0:49 offset1:48\n"
    "v_mfma_f32_16x16x32_bf16 v[224:227], v[120:123], v[80:83], v[224:227]\n"
    "v_mfma_f32_16x16x32_bf16 v[224:227], v[112:115], v[88:91], v[224:227]\n"
    "s_add_u32 m0, s27, 97312\n"
    "s_nop 0\n"
    "buffer_load_dwordx4 v69, s[32:35], s23 offen lds\n"
    "ds_read_b128 v[188:191], v28 offset:2560\n"
    "v_mfma_f32_16x16x32_bf16 v[228:231], v[120:123], v[84:87], v[228:231]\n"
    "ds_read2_b32 v[156:157], v26 offset0:179 offset1:178\n"
    "v_mfma_f32_16x16x32_bf16 v[228:231], v[116:119], v[88:91], v[228:231]\n"
    "v_mfma_f32_16x16x32_bf16 v[220:223], v[124:127], v[72:75], v[220:223]\n"
    "ds_read2_b32 v[158:159], v26 offset0:177 offset1:176\n"
    "s_waitcnt lgkmcnt(15)\n"
    "v_mfma_f32_16x16x32_bf16 v[220:223], v[104:107], v[92:95], v[220:223]\n"
    "ds_read_b128 v[192:195], v28 offset:3072\n"
    "v_mfma_f32_16x16x32_bf16 v[224:227], v[124:127], v[76:79], v[224:227]\n"
    "ds_read2_b32 v[160:161], v27 offset0:51 offset1:50\n"
    "v_mfma_f32_16x16x32_bf16 v[224:227], v[108:111], v[92:95], v[224:227]\n"
    "v_mfma_f32_16x16x32_bf16 v[228:231], v[124:127], v[80:83], v[228:231]\n"
    "s_add_u32 m0, s27, 98368\n"
    "s_nop 0\n"
    "buffer_load_dwordx4 v70, s[32:35], s23 offen lds\n"
    "ds_read2_b32 v[162:163], v27 offset0:49 offset1:48\n"
    "v_mfma_f32_16x16x32_bf16 v[228:231], v[112:115], v[92:95], v[228:231]\n"
    "ds_read_b128 v[196:199], v28 offset:3584\n"
    "v_mfma_f32_16x16x32_bf16 v[224:227], v[128:131], v[72:75], v[224:227]\n"
    "v_mfma_f32_16x16x32_bf16 v[224:227], v[104:107], v[96:99], v[224:227]\n"
    "ds_read2_b32 v[164:165], v27 offset0:179 offset1:178\n"
    "v_mfma_f32_16x16x32_bf16 v[228:231], v[128:131], v[76:79], v[228:231]\n"
    "ds_read2_b32 v[166:167], v27 offset0:177 offset1:176\n"
    "v_mfma_f32_16x16x32_bf16 v[228:231], v[108:111], v[96:99], v[228:231]\n"
    "v_mfma_f32_16x16x32_bf16 v[228:231], v[132:135], v[72:75], v[228:231]\n"
    "v_mfma_f32_16x16x32_bf16 v[228:231], v[104:107], v[100:103], v[228:231]\n"
    "v_mfma_f32_16x16x32_bf16 v[200:203], v[168:171], v[136:139], v[200:203]\n"
    "s_waitcnt lgkmcnt(15)\n"
    "v_mfma_f32_16x16x32_bf16 v[204:207], v[172:175], v[136:139], v[204:207]\n"
    "v_mfma_f32_16x16x32_bf16 v[204:207], v[168:171], v[140:143], v[204:207]\n"
    "v_mfma_f32_16x16x32_bf16 v[208:211], v[172:175], v[140:143], v[208:211]\n"
    "v_mfma_f32_16x16x32_bf16 v[208:211], v[176:179], v[136:139], v[208:211]\n"
    "v_mfma_f32_16x16x32_bf16 v[208:211], v[168:171], v[144:147], v[208:211]\n"
    "v_mfma_f32_16x16x32_bf16 v[212:215], v[176:179], v[140:143], v[212:215]\n"
    "s_add_u32 m0, s27, 99424\n"
    "s_nop 0\n"
    "buffer_load_dwordx4 v71, s[32:35], s23 offen lds\n"
    "v_mfma_f32_16x16x32_bf16 v[212:215], v[172:175], v[144:147], v[212:215]\n"
    "v_mfma_f32_16x16x32_bf16 v[216:219], v[176:179], v[144:147], v[216:219]\n"
    "s_waitcnt lgkmcnt(14)\n"
    "v_mfma_f32_16x16x32_bf16 v[212:215], v[180:183], v[136:139], v[212:215]\n"
    "s_waitcnt lgkmcnt(12)\n"
    "v_mfma_f32_16x16x32_bf16 v[212:215], v[168:171], v[148:151], v[212:215]\n"
    "v_mfma_f32_16x16x32_bf16 v[216:219], v[180:183], v[140:143], v[216:219]\n"
    "v_mfma_f32_16x16x32_bf16 v[216:219], v[172:175], v[148:151], v[216:219]\n"
    "s_waitcnt vmcnt(16)\n"
    "s_mov_b64 exec, 1\n"
    "buffer_store_dword v15, off, s[12:15], s46 sc1\n"
    "s_mov_b64 exec, -1\n"
    "v_mfma_f32_16x16x32_bf16 v[220:223], v[180:183], v[144:147], v[220:223]\n"
    "v_mfma_f32_16x16x32_bf16 v[220:223], v[176:179], v[148:151], v[220:223]\n"
    "v_mfma_f32_16x16x32_bf16 v[224:227], v[180:183], v[148:151], v[224:227]\n"
    "s_waitcnt lgkmcnt(11)\n"
    "v_mfma_f32_16x16x32_bf16 v[216:219], v[184:187], v[136:139], v[216:219]\n"
    "s_waitcnt lgkmcnt(9)\n"
    "v_mfma_f32_16x16x32_bf16 v[216:219], v[168:171], v[152:155], v[216:219]\n"
    "v_mfma_f32_16x16x32_bf16 v[220:223], v[184:187], v[140:143], v[220:223]\n"
    "v_mfma_f32_16x16x32_bf16 v[220:223], v[172:175], v[152:155], v[220:223]\n"
    "v_mfma_f32_16x16x32_bf16 v[224:227], v[184:187], v[144:147], v[224:227]\n"
    "s_add_u32 m0, s30, 28672\n"
    "s_nop 0\n"
    "buffer_load_dwordx4 v68, s[16:19], s28 offen lds\n"
    "v_mfma_f32_16x16x32_bf16 v[224:227], v[176:179], v[152:155], v[224:227]\n"
    "v_mfma_f32_16x16x32_bf16 v[228:231], v[184:187], v[148:151], v[228:231]\n"
    "v_mfma_f32_16x16x32_bf16 v[228:231], v[180:183], v[152:155], v[228:231]\n"
    "s_waitcnt lgkmcnt(8)\n"
    "v_mfma_f32_16x16x32_bf16 v[220:223], v[188:191], v[136:139], v[220:223]\n"
    "s_waitcnt lgkmcnt(6)\n"
    "v_mfma_f32_16x16x32_bf16 v[220:223], v[168:171], v[156:159], v[220:223]\n"
    "v_mfma_f32_16x16x32_bf16 v[224:227], v[188:191], v[140:143], v[224:227]\n"
    "v_mfma_f32_16x16x32_bf16 v[224:227], v[172:175], v[156:159], v[224:227]\n"
    "v_mfma_f32_16x16x32_bf16 v[228:231], v[188:191], v[144:147], v[228:231]\n"
    "v_mfma_f32_16x16x32_bf16 v[228:231], v[176:179], v[156:159], v[228:231]\n"
    "s_waitcnt lgkmcnt(5)\n"
    "v_mfma_f32_16x16x32_bf16 v[224:227], v[192:195], v[136:139], v[224:227]\n"
    "s_waitcnt lgkmcnt(3)\n"
    "v_mfma_f32_16x16x32_bf16 v[224:227], v[168:171], v[160:163], v[224:227]\n"
    "v_mfma_f32_16x16x32_bf16 v[228:231], v[192:195], v[140:143], v[228:231]\n"
    "v_mfma_f32_16x16x32_bf16 v[228:231], v[172:175], v[160:163], v[228:231]\n"
    "s_waitcnt lgkmcnt(2)\n"
    "v_mfma_f32_16x16x32_bf16 v[228:231], v[196:199], v[136:139], v[228:231]\n"
    "s_waitcnt lgkmcnt(0)\n"
    "v_mfma_f32_16x16x32_bf16 v[228:231], v[168:171], v[164:167], v[228:231]\n"
    "s_add_u32 m0, s30, 29728\n"
    "s_nop 0\n"
    "buffer_load_dwordx4 v69, s[16:19], s28 offen lds\n"
    "s_mul_i32 m0, s5, 8704\n"
    "s_barrier\n"
    "ds_write_addtid_b32 v200 offset:0\n"
    "ds_write_addtid_b32 v201 offset:272\n"
    "ds_write_addtid_b32 v202 offset:544\n"
    "ds_write_addtid_b32 v203 offset:816\n"
    "ds_write_addtid_b32 v204 offset:1088\n"
    "ds_write_addtid_b32 v205 offset:1360\n"
    "ds_write_addtid_b32 v206 offset:1632\n"
    "ds_write_addtid_b32 v207 offset:1904\n"
    "ds_write_addtid_b32 v208 offset:2176\n"
    "ds_write_addtid_b32 v209 offset:2448\n"
    "ds_write_addtid_b32 v210 offset:2720\n"
    "ds_write_addtid_b32 v211 offset:2992\n"
    "ds_write_addtid_b32 v212 offset:3264\n"
    "ds_write_addtid_b32 v213 offset:3536\n"
    "ds_write_addtid_b32 v214 offset:3808\n"
    "ds_write_addtid_b32 v215 offset:4080\n"
    "ds_write_addtid_b32 v216 offset:4352\n"
    "ds_write_addtid_b32 v217 offset:4624\n"
    "ds_write_addtid_b32 v218 offset:4896\n"
    "ds_write_addtid_b32 v219 offset:5168\n"
    "ds_write_addtid_b32 v220 offset:5440\n"
    "ds_write_addtid_b32 v221 offset:5712\n"
    "ds_write_addtid_b32 v222 offset:5984\n"
    "ds_write_addtid_b32 v223 offset:6256\n"
    "ds_write_addtid_b32 v224 offset:6528\n"
    "ds_write_addtid_b32 v225 offset:6800\n"
    "ds_write_addtid_b32 v226 offset:7072\n"
    "ds_write_addtid_b32 v227 offset:7344\n"
    "ds_write_addtid_b32 v228 offset:7616\n"
    "ds_write_addtid_b32 v229 offset:7888\n"
    "ds_write_addtid_b32 v230 offset:8160\n"
    "ds_write_addtid_b32 v231 offset:8432\n"
    "s_add_u32 m0, s30, 30784\n"
    "s_nop 0\n"
    "buffer_load_dwordx4 v70, s[16:19], s28 offen lds\n"
    "s_add_u32 m0, s30, 31840\n"
    "s_nop 0\n"
    "buffer_load_dwordx4 v71, s[16:19], s28 offen lds\n"
    "s_waitcnt lgkmcnt(0)\n"
    "s_barrier\n"
    "ds_read_b128 v[72:75], v29 offset:0\n"
    "ds_read_b128 v[76:79], v29 offset:16\n"
    "ds_read_b128 v[80:83], v29 offset:8704\n"
    "ds_read_b128 v[84:87], v29 offset:8720\n"
    "ds_read_b128 v[88:91], v29 offset:17408\n"
    "ds_read_b128 v[92:95], v29 offset:17424\n"
    "ds_read_b128 v[96:99], v29 offset:26112\n"
    "ds_read_b128 v[100:103], v29 offset:26128\n"
    "s_add_u32 m0, s30, 32896\n"
    "s_add_u32 s29, s28, 114688\n"
    "buffer_load_dwordx4 v68, s[16:19], s29 offen lds\n"
    "s_add_u32 m0, s30, 33952\n"
    "s_nop 0\n"
    "buffer_load_dwordx4 v69, s[16:19], s29 offen lds\n"
    "s_waitcnt lgkmcnt(7)\n"
    "v_pk_add_f32 v[232:233], v[232:233], v[72:73]\n"
    "v_pk_add_f32 v[234:235], v[234:235], v[74:75]\n"
    "s_waitcnt lgkmcnt(6)\n"
    "v_pk_add_f32 v[236:237], v[236:237], v[76:77]\n"
    "v_pk_add_f32 v[238:239], v[238:239], v[78:79]\n"
    "s_waitcnt lgkmcnt(5)\n"
    "v_pk_add_f32 v[232:233], v[232:233], v[80:81]\n"
    "v_pk_add_f32 v[234:235], v[234:235], v[82:83]\n"
    "s_waitcnt lgkmcnt(4)\n"
    "v_pk_add_f32 v[236:237], v[236:237], v[84:85]\n"
    "v_pk_add_f32 v[238:239], v[238:239], v[86:87]\n"
    "s_waitcnt lgkmcnt(3)\n"
    "v_pk_add_f32 v[240:241], v[240:241], v[88:89]\n"
    "v_pk_add_f32 v[242:243], v[242:243], v[90:91]\n"
    "s_waitcnt lgkmcnt(2)\n"
    "v_pk_add_f32 v[244:245], v[244:245], v[92:93]\n"
    "v_pk_add_f32 v[246:247], v[246:247], v[94:95]\n"
    "s_waitcnt lgkmcnt(1)\n"
    "v_pk_add_f32 v[240:241], v[240:241], v[96:97]\n"
    "v_pk_add_f32 v[242:243], v[242:243], v[98:99]\n"
    "s_waitcnt lgkmcnt(0)\n"
    "v_pk_add_f32 v[244:245], v[244:245], v[100:101]\n"
    "v_pk_add_f32 v[246:247], v[246:247], v[102:103]\n"
    "v_cvt_pk_bf16_f32 v104, v232, v240\n"
    "v_cvt_pk_bf16_f32 v105, v233, v241\n"
    "v_cvt_pk_bf16_f32 v106, v234, v242\n"
    "v_cvt_pk_bf16_f32 v107, v235, v243\n"
    "v_cvt_pk_bf16_f32 v108, v236, v244\n"
    "v_cvt_pk_bf16_f32 v109, v237, v245\n"
    "v_cvt_pk_bf16_f32 v110, v238, v246\n"
    "v_cvt_pk_bf16_f32 v111, v239, v247\n"
    "buffer_store_dwordx4 v[104:107], v21, s[12:15], s60 offen sc1\n"
    "buffer_store_dwordx4 v[108:111], v21, s[12:15], s61 offen sc1\n"
    "s_barrier\n"
    "s_add_u32 m0, s30, 35008\n"
    "s_nop 0\n"
    "buffer_load_dwordx4 v70, s[16:19], s29 offen lds\n"
    "s_add_u32 m0, s30, 36064\n"
    "s_nop 0\n"
    "buffer_load_dwordx4 v71, s[16:19], s29 offen lds\n"
    "s_mul_i32 s51, s54, 3584\n"
    "s_mul_i32 s52, s5, 896\n"
    "s_add_u32 s51, s51, s52\n"
    "s_add_u32 s51, s51, 4096\n"
    "v_min_u32 v8, 432, v1\n"
    "v_lshl_add_u32 v245, v8, 1, s51\n"
    "buffer_load_dword v244, v245, s[12:15], 0 offen sc1\n"
    "s_lshr_b32 s49, s4, 5\n"
    "s_bfe_u32 s52, s4, 0x20001\n"
    "s_lshl3_add_u32 s49, s52, s49\n"
    "s_and_b32 s49, s49, 3\n"
    "s_lshl_b32 s49, s49, 8\n"
    "v_and_b32 v249, 1008, v1\n"
    "v_lshl_add_u32 v249, v249, 6, s49\n"
    "v_lshrrev_b32 v246, 2, v1\n"
    "v_add_u32 v246, 0, v246\n"
    "v_mov_b32 v212, 0x3f803f80\n"
    "v_mov_b32 v213, 0x3f803f80\n"
    "v_mov_b32 v214, 0x3f803f80\n"
    "v_mov_b32 v215, 0x3f803f80\n"
    "s_cmp_ge_u32 s4, 224\n"
    "s_cbranch_scc1 Lk_helper\n"
    "s_waitcnt vmcnt(12)\n"
    "v_max_u32 v7, v12, v5\n"
    "v_min_u32 v9, v12, v5\n"
    "v_add_u32 v6, s30, v7\n"
    "v_add_u32 v7, s31, v7\n"
    "v_add_u32 v8, s30, v9\n"
    "v_add_u32 v9, s31, v9\n"
    "ds_read_b64_tr_b16 v[84:85], v6 offset:0\n"
    "ds_read_b64_tr_b16 v[86:87], v6 offset:256\n"
    "ds_read_b64_tr_b16 v[88:89], v6 offset:32\n"
    "ds_read_b64_tr_b16 v[90:91], v6 offset:288\n"
    "ds_read_b64_tr_b16 v[92:93], v6 offset:64\n"
    "ds_read_b64_tr_b16 v[94:95], v6 offset:320\n"
    "ds_read_b64_tr_b16 v[96:97], v6 offset:96\n"
    "ds_read_b64_tr_b16 v[98:99], v6 offset:352\n"
    "ds_read_b64_tr_b16 v[100:101], v6 offset:4224\n"
    "ds_read_b64_tr_b16 v[102:103], v6 offset:4480\n"
    "ds_read_b64_tr_b16 v[104:105], v6 offset:4256\n"
    "ds_read_b64_tr_b16 v[106:107], v6 offset:4512\n"
    "s_waitcnt lgkmcnt(10)\n"
    "v_mfma_f32_16x16x32_bf16 v[20:23], v[84:87], v[212:215], 0\n"
    "ds_read_b64_tr_b16 v[108:109], v6 offset:4288\n"
    "ds_read_b64_tr_b16 v[110:111], v6 offset:4544\n"
    "s_add_u32 m0, s30, 37120\n"
    "s_add_u32 s29, s28, 229376\n"
    "buffer_load_dwordx4 v68, s[16:19], s29 offen lds\n"
    "s_waitcnt lgkmcnt(10)\n"
    "v_mfma_f32_16x16x32_bf16 v[24:27], v[88:91], v[212:215], 0\n"
    "ds_read_b64_tr_b16 v[112:113], v6 offset:4320\n"
    "ds_read_b64_tr_b16 v[114:115], v6 offset:4576\n"
    "s_add_u32 m0, s30, 38176\n"
    "s_nop 0\n"
    "buffer_load_dwordx4 v69, s[16:19], s29 offen lds\n"
    "s_waitcnt lgkmcnt(10)\n"
    "v_mfma_f32_16x16x32_bf16 v[28:31], v[92:95], v[212:215], 0\n"
    "ds_read_b64_tr_b16 v[116:117], v6 offset:8448\n"
    "ds_read_b64_tr_b16 v[118:119], v6 offset:8704\n"
    "s_add_u32 m0, s30, 39232\n"
    "s_nop 0\n"
    "buffer_load_dwordx4 v70, s[16:19], s29 offen lds\n"
    "s_waitcnt lgkmcnt(10)\n"
    "v_mfma_f32_16x16x32_bf16 v[32:35], v[96:99], v[212:215], 0\n"
    "ds_read_b64_tr_b16 v[120:121], v6 offset:8480\n"
    "ds_read_b64_tr_b16 v[122:123], v6 offset:8736\n"
    "s_add_u32 m0, s30, 40288\n"
    "s_nop 0\n"
    "buffer_load_dwordx4 v71, s[16:19], s29 offen lds\n"
    "s_waitcnt lgkmcnt(10)\n"
    "v_mfma_f32_16x16x32_bf16 v[20:23], v[100:103], v[212:215], v[20:23]\n"
    "ds_read_b64_tr_b16 v[124:125], v6 offset:8512\n"
    "ds_read_b64_tr_b16 v[126:127], v6 offset:8768\n"
    "s_add_u32 m0, s31, 28672\n"
    "s_add_u32 s29, s28, 344064\n"
    "buffer_load_dwordx4 v68, s[16:19], s29 offen lds\n"
    "s_waitcnt lgkmcnt(10)\n"
    "v_mfma_f32_16x16x32_bf16 v[24:27], v[104:107], v[212:215], v[24:27]\n"
    "ds_read_b64_tr_b16 v[128:129], v6 offset:8544\n"
    "ds_read_b64_tr_b16 v[130:131], v6 offset:8800\n"
    "s_add_u32 m0, s31, 29728\n"
    "s_nop 0\n"
    "buffer_load_dwordx4 v69, s[16:19], s29 offen lds\n"
    "s_waitcnt lgkmcnt(10)\n"
    "v_mfma_f32_16x16x32_bf16 v[28:31], v[108:111], v[212:215], v[28:31]\n"
    "ds_read_b64_tr_b16 v[132:133], v7 offset:0\n"
    "ds_read_b64_tr_b16 v[134:135], v7 offset:256\n"
    "s_add_u32 m0, s31, 30784\n"
    "s_nop 0\n"
    "buffer_load_dwordx4 v70, s[16:19], s29 offen lds\n"
    "s_waitcnt lgkmcnt(10)\n"
    "v_mfma_f32_16x16x32_bf16 v[32:35], v[112:115], v[212:215], v[32:35]\n"
    "ds_read_b64_tr_b16 v[136:137], v7 offset:32\n"
    "ds_read_b64_tr_b16 v[138:139], v7 offset:288\n"
    "s_add_u32 m0, s31, 31840\n"
    "s_nop 0\n"
    "buffer_load_dwordx4 v71, s[16:19], s29 offen lds\n"
    "s_waitcnt lgkmcnt(10)\n"
    "v_mfma_f32_16x16x32_bf16 v[20:23], v[116:119], v[212:215], v[20:23]\n"
    "ds_read_b64_tr_b16 v[140:141], v7 offset:64\n"
    "ds_read_b64_tr_b16 v[142:143], v7 offset:320\n"
    "s_waitcnt lgkmcnt(10)\n"
    "v_mfma_f32_16x16x32_bf16 v[24:27], v[120:123], v[212:215], v[24:27]\n"
    "ds_read_b64_tr_b16 v[144:145], v7 offset:96\n"
    "ds_read_b64_tr_b16 v[146:147], v7 offset:352\n"
    "s_waitcnt lgkmcnt(10)\n"
    "v_mfma_f32_16x16x32_bf16 v[28:31], v[124:127], v[212:215], v[28:31]\n"
    "s_waitcnt vmcnt(15)\n"
    "ds_read_b64_tr_b16 v[84:85], v8 offset:0\n"
    "ds_read_b64_tr_b16 v[86:87], v8 offset:256\n"
    "s_waitcnt lgkmcnt(10)\n"
    "v_mfma_f32_16x16x32_bf16 v[32:35], v[128:131], v[212:215], v[32:35]\n"
    "ds_read_b64_tr_b16 v[88:89], v8 offset:32\n"
    "ds_read_b64_tr_b16 v[90:91], v8 offset:288\n"
    "s_waitcnt lgkmcnt(10)\n"
    "v_mfma_f32_16x16x32_bf16 v[20:23], v[132:135], v[212:215], v[20:23]\n"
    "ds_read_b64_tr_b16 v[92:93], v8 offset:64\n"
    "ds_read_b64_tr_b16 v[94:95], v8 offset:320\n"
    "s_waitcnt lgkmcnt(10)\n"
    "v_mfma_f32_16x16x32_bf16 v[24:27], v[136:139], v[212:215], v[24:27]\n"
    "ds_read_b64_tr_b16 v[96:97], v8 offset:96\n"
    "ds_read_b64_tr_b16 v[98:99], v8 offset:352\n"
    "s_waitcnt lgkmcnt(10)\n"
    "v_mfma_f32_16x16x32_bf16 v[28:31], v[140:143], v[212:215], v[28:31]\n"
    "s_waitcnt vmcnt(9)\n"
    "ds_read_b64_tr_b16 v[100:101], v8 offset:4224\n"
    "ds_read_b64_tr_b16 v[102:103], v8 offset:4480\n"
    "s_waitcnt lgkmcnt(10)\n"
    "v_mfma_f32_16x16x32_bf16 v[32:35], v[144:147], v[212:215], v[32:35]\n"
    "ds_read_b64_tr_b16 v[104:105], v8 offset:4256\n"
    "ds_read_b64_tr_b16 v[106:107], v8 offset:4512\n"
    "s_waitcnt lgkmcnt(10)\n"
    "v_mfma_f32_16x16x32_bf16 v[36:39], v[84:87], v[212:215], 0\n"
    "ds_read_b64_tr_b16 v[108:109], v8 offset:4288\n"
    "ds_read_b64_tr_b16 v[110:111], v8 offset:4544\n"
    "s_waitcnt lgkmcnt(10)\n"
    "v_mfma_f32_16x16x32_bf16 v[40:43], v[88:91], v[212:215], 0\n"
    "ds_read_b64_tr_b16 v[112:113], v8 offset:4320\n"
    "ds_read_b64_tr_b16 v[114:115], v8 offset:4576\n"
    "s_waitcnt lgkmcnt(10)\n"
    "v_mfma_f32_16x16x32_bf16 v[44:47], v[92:95], v[212:215], 0\n"
    "s_waitcnt vmcnt(4)\n"
    "ds_read_b64_tr_b16 v[116:117], v8 offset:8448\n"
    "ds_read_b64_tr_b16 v[118:119], v8 offset:8704\n"
    "s_waitcnt lgkmcnt(10)\n"
    "v_mfma_f32_16x16x32_bf16 v[48:51], v[96:99], v[212:215], 0\n"
    "ds_read_b64_tr_b16 v[120:121], v8 offset:8480\n"
    "ds_read_b64_tr_b16 v[122:123], v8 offset:8736\n"
    "s_waitcnt lgkmcnt(10)\n"
    "v_mfma_f32_16x16x32_bf16 v[36:39], v[100:103], v[212:215], v[36:39]\n"
    "ds_read_b64_tr_b16 v[124:125], v8 offset:8512\n"
    "ds_read_b64_tr_b16 v[126:127], v8 offset:8768\n"
    "s_add_u32 s32, s41, 8192\n"
    "buffer_load_dwordx4 v[180:183], v2, s[12:15], s32 offen offset:0 sc1\n"
    "s_waitcnt lgkmcnt(10)\n"
    "v_mfma_f32_16x16x32_bf16 v[40:43], v[104:107], v[212:215], v[40:43]\n"
    "ds_read_b64_tr_b16 v[128:129], v8 offset:8544\n"
    "ds_read_b64_tr_b16 v[130:131], v8 offset:8800\n"
    "buffer_load_dwordx4 v[184:187], v2, s[12:15], s32 offen offset:256 sc1\n"
    "s_waitcnt lgkmcnt(10)\n"
    "v_mfma_f32_16x16x32_bf16 v[44:47], v[108:111], v[212:215], v[44:47]\n"
    "s_waitcnt vmcnt(2)\n"
    "ds_read_b64_tr_b16 v[132:133], v9 offset:0\n"
    "ds_read_b64_tr_b16 v[134:135], v9 offset:256\n"
    "buffer_load_dwordx4 v[188:191], v2, s[12:15], s32 offen offset:512 sc1\n"
    "s_waitcnt lgkmcnt(10)\n"
    "v_mfma_f32_16x16x32_bf16 v[48:51], v[112:115], v[212:215], v[48:51]\n"
    "ds_read_b64_tr_b16 v[136:137], v9 offset:32\n"
    "ds_read_b64_tr_b16 v[138:139], v9 offset:288\n"
    "buffer_load_dwordx4 v[192:195], v2, s[12:15], s32 offen offset:768 sc1\n"
    "s_waitcnt lgkmcnt(10)\n"
    "v_mfma_f32_16x16x32_bf16 v[36:39], v[116:119], v[212:215], v[36:39]\n"
    "ds_read_b64_tr_b16 v[140:141], v9 offset:64\n"
    "ds_read_b64_tr_b16 v[142:143], v9 offset:320\n"
    "s_add_u32 s32, s41, 12288\n"
    "buffer_load_dwordx4 v[196:199], v2, s[12:15], s32 offen offset:0 sc1\n"
    "s_waitcnt lgkmcnt(10)\n"
    "v_mfma_f32_16x16x32_bf16 v[40:43], v[120:123], v[212:215], v[40:43]\n"
    "ds_read_b64_tr_b16 v[144:145], v9 offset:96\n"
    "ds_read_b64_tr_b16 v[146:147], v9 offset:352\n"
    "buffer_load_dwordx4 v[200:203], v2, s[12:15], s32 offen offset:256 sc1\n"
    "s_waitcnt lgkmcnt(10)\n"
    "v_mfma_f32_16x16x32_bf16 v[44:47], v[124:127], v[212:215], v[44:47]\n"
    "buffer_load_dwordx4 v[204:207], v2, s[12:15], s32 offen offset:512 sc1\n"
    "s_waitcnt lgkmcnt(8)\n"
    "v_mfma_f32_16x16x32_bf16 v[48:51], v[128:131], v[212:215], v[48:51]\n"
    "buffer_load_dwordx4 v[208:211], v2, s[12:15], s32 offen offset:768 sc1\n"
    "s_waitcnt lgkmcnt(6)\n"
    "v_mfma_f32_16x16x32_bf16 v[36:39], v[132:135], v[212:215], v[36:39]\n"
    "s_add_u32 s32, s41, 16384\n"
    "buffer_load_dwordx4 a[224:227], v2, s[12:15], s32 offen offset:0 sc1\n"
    "s_waitcnt lgkmcnt(4)\n"
    "v_mfma_f32_16x16x32_bf16 v[40:43], v[136:139], v[212:215], v[40:43]\n"
    "buffer_load_dwordx4 a[228:231], v2, s[12:15], s32 offen offset:256 sc1\n"
    "s_waitcnt lgkmcnt(2)\n"
    "v_mfma_f32_16x16x32_bf16 v[44:47], v[140:143], v[212:215], v[44:47]\n"
    "buffer_load_dwordx4 a[232:235], v2, s[12:15], s32 offen offset:512 sc1\n"
    "s_waitcnt lgkmcnt(0)\n"
    "v_mfma_f32_16x16x32_bf16 v[48:51], v[144:147], v[212:215], v[48:51]\n"
    "buffer_load_dwordx4 a[236:239], v2, s[12:15], s32 offen offset:768 sc1\n"
    "s_add_u32 s32, s41, 20480\n"
    "buffer_load_dwordx4 a[240:243], v2, s[12:15], s32 offen offset:0 sc1\n"
    "buffer_load_dwordx4 a[244:247], v2, s[12:15], s32 offen offset:256 sc1\n"
    "buffer_load_dwordx4 a[248:251], v2, s[12:15], s32 offen offset:512 sc1\n"
    "buffer_load_dwordx4 a[252:255], v2, s[12:15], s32 offen offset:768 sc1\n"
    "s_cmp_eq_u32 s10, 0\n"
    "s_cselect_b32 s49, 64, 0\n"
    "s_cselect_b32 s50, 0, 64\n"
    "v_add_u32 v6, s49, v11\n"
    "v_add_u32 v7, s50, v11\n"
    "v_cmp_ne_u32 vcc, s7, v244\n"
    "s_mov_b32 s9, 0\n"
    "s_mov_b32 exec_lo, 0x00010001\n"
    "s_mov_b32 exec_hi, 0x00010001\n"
    "ds_write_b128 v6, v[20:23] offset:0\n"
    "ds_write_b128 v6, v[24:27] offset:128\n"
    "ds_write_b128 v6, v[28:31] offset:256\n"
    "ds_write_b128 v6, v[32:35] offset:384\n"
    "ds_write_b128 v7, v[36:39] offset:0\n"
    "ds_write_b128 v7, v[40:43] offset:128\n"
    "ds_write_b128 v7, v[44:47] offset:256\n"
    "ds_write_b128 v7, v[48:51] offset:384\n"
    "s_mov_b64 exec, -1\n"
    "s_cbranch_vccnz Lk_flf_7\n"
    "Lk_flr_8:\n"
    "s_add_u32 s8, s6, 28672\n"
    "buffer_store_dwordx4 v[16:19], v4, s[12:15], s8 offen offset:0 sc1\n"
    "buffer_store_dwordx4 v[16:19], v4, s[12:15], s8 offen offset:256 sc1\n"
    "s_add_u32 s8, s6, 43008\n"
    "buffer_store_dwordx4 v[16:19], v4, s[12:15], s8 offen offset:0 sc1\n"
    "buffer_store_dwordx4 v[16:19], v4, s[12:15], s8 offen offset:256 sc1\n"
    "s_waitcnt lgkmcnt(0)\n"
    "s_barrier\n"
    "ds_read_b32 v20, v246 offset:0\n"
    "ds_read_b32 v21, v246 offset:256\n"
    "ds_read_b32 v22, v246 offset:512\n"
    "ds_read_b32 v23, v246 offset:768\n"
    "ds_read_b32 v24, v246 offset:1024\n"
    "ds_read_b32 v25, v246 offset:1280\n"
    "ds_read_b32 v26, v246 offset:1536\n"
    "ds_read_b32 v27, v246 offset:1792\n"
    "s_sub_u32 s49, s56, 0\n"
    "v_add_u32 v246, s49, v246\n"
    "v_add_u32 v11, 2048, v11\n"
    "s_waitcnt lgkmcnt(4)\n"
    "v_pk_add_f32 v[20:21], v[20:21], v[22:23]\n"
    "s_waitcnt lgkmcnt(2)\n"
    "v_pk_add_f32 v[20:21], v[20:21], v[24:25]\n"
    "s_waitcnt lgkmcnt(0)\n"
    "v_pk_add_f32 v[20:21], v[20:21], v[26:27]\n"
    "ds_write_b32 v246, v20 offset:2048\n"
    "ds_write_b32 v246, v21 offset:2304\n"
    "s_branch Lk_p3_9\n"
    "Lk_p3:\n"
    "s_add_u32 s32, s41, 8192\n"
    "buffer_load_dwordx4 v[180:183], v2, s[12:15], s32 offen offset:0 sc1\n"
    "buffer_load_dwordx4 v[184:187], v2, s[12:15], s32 offen offset:256 sc1\n"
    "buffer_load_dwordx4 v[188:191], v2, s[12:15], s32 offen offset:512 sc1\n"
    "buffer_load_dwordx4 v[192:195], v2, s[12:15], s32 offen offset:768 sc1\n"
    "s_add_u32 s32, s41, 12288\n"
    "buffer_load_dwordx4 v[196:199], v2, s[12:15], s32 offen offset:0 sc1\n"
    "buffer_load_dwordx4 v[200:203], v2, s[12:15], s32 offen offset:256 sc1\n"
    "buffer_load_dwordx4 v[204:207], v2, s[12:15], s32 offen offset:512 sc1\n"
    "buffer_load_dwordx4 v[208:211], v2, s[12:15], s32 offen offset:768 sc1\n"
    "s_add_u32 s32, s41, 16384\n"
    "buffer_load_dwordx4 a[224:227], v2, s[12:15], s32 offen offset:0 sc1\n"
    "buffer_load_dwordx4 a[228:231], v2, s[12:15], s32 offen offset:256 sc1\n"
    "buffer_load_dwordx4 a[232:235], v2, s[12:15], s32 offen offset:512 sc1\n"
    "buffer_load_dwordx4 a[236:239], v2, s[12:15], s32 offen offset:768 sc1\n"
    "s_add_u32 s32, s41, 20480\n"
    "buffer_load_dwordx4 a[240:243], v2, s[12:15], s32 offen offset:0 sc1\n"
    "buffer_load_dwordx4 a[244:247], v2, s[12:15], s32 offen offset:256 sc1\n"
    "buffer_load_dwordx4 a[248:251], v2, s[12:15], s32 offen offset:512 sc1\n"
    "buffer_load_dwordx4 a[252:255], v2, s[12:15], s32 offen offset:768 sc1\n"
    "s_waitcnt vmcnt(0)\n"
    "Lk_p3_9:\n"
    "ds_read_b64_tr_b16 v[244:245], v12 offset:25344\n"
    "ds_read_b64_tr_b16 v[246:247], v12 offset:25600\n"
    "ds_read_b64_tr_b16 v[248:249], v5 offset:25344\n"
    "ds_read_b64_tr_b16 v[250:251], v5 offset:25600\n"
    "s_waitcnt vmcnt(16)\n"
    "v_mov_b32 v212, v180\n"
    "v_mov_b32 v213, v184\n"
    "v_mov_b32 v214, v188\n"
    "v_mov_b32 v215, v192\n"
    "v_mov_b32 v216, v181\n"
    "v_mov_b32 v217, v185\n"
    "v_mov_b32 v218, v189\n"
    "v_mov_b32 v219, v193\n"
    "v_mov_b32 v220, v182\n"
    "v_mov_b32 v221, v186\n"
    "v_mov_b32 v222, v190\n"
    "v_mov_b32 v223, v194\n"
    "v_mov_b32 v224, v183\n"
    "v_mov_b32 v225, v187\n"
    "v_mov_b32 v226, v191\n"
    "v_mov_b32 v227, v195\n"
    "v_max3_u32 v9, v212, v213, v214\n"
    "v_max3_u32 v9, v215, v216, v9\n"
    "v_max3_u32 v9, v217, v218, v9\n"
    "v_max3_u32 v9, v219, v220, v9\n"
    "v_max3_u32 v9, v221, v222, v9\n"
    "v_max3_u32 v9, v223, v224, v9\n"
    "v_max3_u32 v9, v225, v226, v9\n"
    "v_max_u32 v9, v227, v9\n"
    "v_cmp_eq_u32 vcc, -1, v9\n"
    "s_cbranch_vccnz Lk_hf_10\n"
    "Lk_hr_11:\n"
    "s_waitcnt lgkmcnt(2)\n"
    "v_mfma_f32_16x16x32_bf16 v[20:23], v[244:247], v[212:215], 0\n"
    "ds_read_b64_tr_b16 v[252:253], v12 offset:25376\n"
    "v_mfma_f32_16x16x32_bf16 v[24:27], v[244:247], v[216:219], 0\n"
    "ds_read_b64_tr_b16 v[254:255], v12 offset:25632\n"
    "v_mfma_f32_16x16x32_bf16 v[28:31], v[244:247], v[220:223], 0\n"
    "s_lshr_b32 s10, s4, 5\n"
    "s_lshl_b32 s10, s10, 7\n"
    "v_lshl_add_u32 v16, v1, 6, s10\n"
    "s_bfe_u32 s16, s4, 0x20003\n"
    "s_lshl_b32 s16, s16, 7\n"
    "v_lshl_add_u32 v18, v1, 5, s16\n"
    "v_mfma_f32_16x16x32_bf16 v[32:35], v[244:247], v[224:227], 0\n"
    "s_waitcnt lgkmcnt(2)\n"
    "v_mfma_f32_16x16x32_bf16 v[36:39], v[248:251], v[212:215], 0\n"
    "ds_read_b64_tr_b16 v[244:245], v5 offset:25376\n"
    "v_mfma_f32_16x16x32_bf16 v[40:43], v[248:251], v[216:219], 0\n"
    "ds_read_b64_tr_b16 v[246:247], v5 offset:25632\n"
    "s_add_u32 s32, s41, 24576\n"
    "buffer_load_dwordx4 v[148:151], v2, s[12:15], s32 offen offset:0 sc1\n"
    "v_mfma_f32_16x16x32_bf16 v[44:47], v[248:251], v[220:223], 0\n"
    "v_mfma_f32_16x16x32_bf16 v[48:51], v[248:251], v[224:227], 0\n"
    "s_waitcnt lgkmcnt(2)\n"
    "v_mfma_f32_16x16x32_bf16 v[52:55], v[252:255], v[212:215], 0\n"
    "ds_read_b64_tr_b16 v[248:249], v12 offset:25408\n"
    "buffer_load_dwordx4 v[152:155], v2, s[12:15], s32 offen offset:256 sc1\n"
    "v_mfma_f32_16x16x32_bf16 v[56:59], v[252:255], v[216:219], 0\n"
    "ds_read_b64_tr_b16 v[250:251], v12 offset:25664\n"
    "s_waitcnt vmcnt(14)\n"
    "v_mfma_f32_16x16x32_bf16 v[60:63], v[252:255], v[220:223], 0\n"
    "v_mov_b32 v228, v196\n"
    "v_mov_b32 v229, v200\n"
    "v_mfma_f32_16x16x32_bf16 v[64:67], v[252:255], v[224:227], 0\n"
    "buffer_load_dwordx4 v[156:159], v2, s[12:15], s32 offen offset:512 sc1\n"
    "v_mov_b32 v230, v204\n"
    "s_waitcnt lgkmcnt(2)\n"
    "v_mfma_f32_16x16x32_bf16 v[68:71], v[244:247], v[212:215], 0\n"
    "ds_read_b64_tr_b16 v[252:253], v5 offset:25408\n"
    "v_mov_b32 v231, v208\n"
    "v_mfma_f32_16x16x32_bf16 v[72:75], v[244:247], v[216:219], 0\n"
    "ds_read_b64_tr_b16 v[254:255], v5 offset:25664\n"
    "v_mov_b32 v232, v197\n"
    "v_mfma_f32_16x16x32_bf16 v[76:79], v[244:247], v[220:223], 0\n"
    "buffer_load_dwordx4 v[160:163], v2, s[12:15], s32 offen offset:768 sc1\n"
    "v_mov_b32 v233, v201\n"
    "v_mfma_f32_16x16x32_bf16 v[80:83], v[244:247], v[224:227], 0\n"
    "v_mov_b32 v234, v205\n"
    "v_mov_b32 v235, v209\n"
    "s_waitcnt lgkmcnt(2)\n"
    "v_mfma_f32_16x16x32_bf16 v[84:87], v[248:251], v[212:215], 0\n"
    "ds_read_b64_tr_b16 v[244:245], v12 offset:25440\n"
    "v_mov_b32 v236, v198\n"
    "v_mfma_f32_16x16x32_bf16 v[88:91], v[248:251], v[216:219], 0\n"
    "ds_read_b64_tr_b16 v[246:247], v12 offset:25696\n"
    "v_mov_b32 v237, v202\n"
    "v_mfma_f32_16x16x32_bf16 v[92:95], v[248:251], v[220:223], 0\n"
    "s_add_u32 s32, s41, 28672\n"
    "buffer_load_dwordx4 v[164:167], v2, s[12:15], s32 offen offset:0 sc1\n"
    "v_mov_b32 v238, v206\n"
    "v_mfma_f32_16x16x32_bf16 v[96:99], v[248:251], v[224:227], 0\n"
    "v_mov_b32 v239, v210\n"
    "v_mov_b32 v240, v199\n"
    "s_waitcnt lgkmcnt(2)\n"
    "v_mfma_f32_16x16x32_bf16 v[100:103], v[252:255], v[212:215], 0\n"
    "ds_read_b64_tr_b16 v[248:249], v5 offset:25440\n"
    "v_mov_b32 v241, v203\n"
    "v_mfma_f32_16x16x32_bf16 v[104:107], v[252:255], v[216:219], 0\n"
    "ds_read_b64_tr_b16 v[250:251], v5 offset:25696\n"
    "buffer_load_dwordx4 v[168:171], v2, s[12:15], s32 offen offset:256 sc1\n"
    "v_mfma_f32_16x16x32_bf16 v[108:111], v[252:255], v[220:223], 0\n"
    "v_mov_b32 v242, v207\n"
    "v_mov_b32 v243, v211\n"
    "v_mfma_f32_16x16x32_bf16 v[112:115], v[252:255], v[224:227], 0\n"
    "v_max3_u32 v9, v228, v229, v230\n"
    "v_max3_u32 v9, v231, v232, v9\n"
    "s_waitcnt lgkmcnt(2)\n"
    "v_mfma_f32_16x16x32_bf16 v[116:119], v[244:247], v[212:215], 0\n"
    "ds_read_b64_tr_b16 v[252:253], v12 offset:0\n"
    "buffer_load_dwordx4 v[172:175], v2, s[12:15], s32 offen offset:512 sc1\n"
    "v_mfma_f32_16x16x32_bf16 v[120:123], v[244:247], v[216:219], 0\n"
    "ds_read_b64_tr_b16 v[254:255], v12 offset:256\n"
    "v_max3_u32 v9, v233, v234, v9\n"
    "v_mfma_f32_16x16x32_bf16 v[124:127], v[244:247], v[220:223], 0\n"
    "v_max3_u32 v9, v235, v236, v9\n"
    "v_max3_u32 v9, v237, v238, v9\n"
    "v_mfma_f32_16x16x32_bf16 v[128:131], v[244:247], v[224:227], 0\n"
    "buffer_load_dwordx4 v[176:179], v2, s[12:15], s32 offen offset:768 sc1\n"
    "v_max3_u32 v9, v239, v240, v9\n"
    "s_waitcnt lgkmcnt(2)\n"
    "v_mfma_f32_16x16x32_bf16 v[132:135], v[248:251], v[212:215], 0\n"
    "ds_read_b64_tr_b16 v[244:245], v5 offset:0\n"
    "v_max3_u32 v9, v241, v242, v9\n"
    "v_mfma_f32_16x16x32_bf16 v[136:139], v[248:251], v[216:219], 0\n"
    "ds_read_b64_tr_b16 v[246:247], v5 offset:256\n"
    "v_max_u32 v9, v243, v9\n"
    "v_mfma_f32_16x16x32_bf16 v[140:143], v[248:251], v[220:223], 0\n"
    "v_cmp_eq_u32 vcc, -1, v9\n"
    "v_mfma_f32_16x16x32_bf16 v[144:147], v[248:251], v[224:227], 0\n"
    "s_cbranch_vccnz Lk_hf_12\n"
    "Lk_hr_13:\n"
    "s_waitcnt lgkmcnt(2)\n"
    "v_mfma_f32_16x16x32_bf16 v[20:23], v[252:255], v[228:231], v[20:23]\n"
    "ds_read_b64_tr_b16 v[248:249], v12 offset:32\n"
    "v_mfma_f32_16x16x32_bf16 v[24:27], v[252:255], v[232:235], v[24:27]\n"
    "ds_read_b64_tr_b16 v[250:251], v12 offset:288\n"
    "v_mfma_f32_16x16x32_bf16 v[28:31], v[252:255], v[236:239], v[28:31]\n"
    "s_add_u32 s32, s41, 32768\n"
    "buffer_load_dwordx4 v[180:183], v2, s[12:15], s32 offen offset:0 sc1\n"
    "v_mfma_f32_16x16x32_bf16 v[32:35], v[252:255], v[240:243], v[32:35]\n"
    "s_waitcnt lgkmcnt(2)\n"
    "v_mfma_f32_16x16x32_bf16 v[36:39], v[244:247], v[228:231], v[36:39]\n"
    "ds_read_b64_tr_b16 v[252:253], v5 offset:32\n"
    "v_mfma_f32_16x16x32_bf16 v[40:43], v[244:247], v[232:235], v[40:43]\n"
    "ds_read_b64_tr_b16 v[254:255], v5 offset:288\n"
    "buffer_load_dwordx4 v[184:187], v2, s[12:15], s32 offen offset:256 sc1\n"
    "v_mfma_f32_16x16x32_bf16 v[44:47], v[244:247], v[236:239], v[44:47]\n"
    "v_mfma_f32_16x16x32_bf16 v[48:51], v[244:247], v[240:243], v[48:51]\n"
    "s_waitcnt lgkmcnt(2)\n"
    "v_mfma_f32_16x16x32_bf16 v[52:55], v[248:251], v[228:231], v[52:55]\n"
    "ds_read_b64_tr_b16 v[244:245], v12 offset:64\n"
    "s_waitcnt vmcnt(18)\n"
    "v_mfma_f32_16x16x32_bf16 v[56:59], v[248:251], v[232:235], v[56:59]\n"
    "ds_read_b64_tr_b16 v[246:247], v12 offset:320\n"
    "buffer_load_dwordx4 v[188:191], v2, s[12:15], s32 offen offset:512 sc1\n"
    "v_mfma_f32_16x16x32_bf16 v[60:63], v[248:251], v[236:239], v[60:63]\n"
    "v_accvgpr_read_b32 v212, a224\n"
    "v_accvgpr_read_b32 v213, a228\n"
    "v_mfma_f32_16x16x32_bf16 v[64:67], v[248:251], v[240:243], v[64:67]\n"
    "v_accvgpr_read_b32 v214, a232\n"
    "v_accvgpr_read_b32 v215, a236\n"
    "s_waitcnt lgkmcnt(2)\n"
    "v_mfma_f32_16x16x32_bf16 v[68:71], v[252:255], v[228:231], v[68:71]\n"
    "ds_read_b64_tr_b16 v[248:249], v5 offset:64\n"
    "buffer_load_dwordx4 v[192:195], v2, s[12:15], s32 offen offset:768 sc1\n"
    "v_mfma_f32_16x16x32_bf16 v[72:75], v[252:255], v[232:235], v[72:75]\n"
    "ds_read_b64_tr_b16 v[250:251], v5 offset:320\n"
    "v_accvgpr_read_b32 v216, a225\n"
    "v_mfma_f32_16x16x32_bf16 v[76:79], v[252:255], v[236:239], v[76:79]\n"
    "v_accvgpr_read_b32 v217, a229\n"
    "v_accvgpr_read_b32 v218, a233\n"
    "v_mfma_f32_16x16x32_bf16 v[80:83], v[252:255], v[240:243], v[80:83]\n"
    "v_accvgpr_read_b32 v219, a237\n"
    "v_accvgpr_read_b32 v220, a226\n"
    "s_waitcnt lgkmcnt(2)\n"
    "v_mfma_f32_16x16x32_bf16 v[84:87], v[244:247], v[228:231], v[84:87]\n"
    "ds_read_b64_tr_b16 v[252:253], v12 offset:96\n"
    "s_add_u32 s32, s41, 36864\n"
    "buffer_load_dwordx4 v[196:199], v2, s[12:15], s32 offen offset:0 sc1\n"
    "v_mfma_f32_16x16x32_bf16 v[88:91], v[244:247], v[232:235], v[88:91]\n"
    "ds_read_b64_tr_b16 v[254:255], v12 offset:352\n"
    "v_accvgpr_read_b32 v221, a230\n"
    "v_mfma_f32_16x16x32_bf16 v[92:95], v[244:247], v[236:239], v[92:95]\n"
    "v_accvgpr_read_b32 v222, a234\n"
    "v_accvgpr_read_b32 v223, a238\n"
    "v_mfma_f32_16x16x32_bf16 v[96:99], v[244:247], v[240:243], v[96:99]\n"
    "v_accvgpr_read_b32 v224, a227\n"
    "v_accvgpr_read_b32 v225, a231\n"
    "s_waitcnt lgkmcnt(2)\n"
    "v_mfma_f32_16x16x32_bf16 v[100:103], v[248:251], v[228:231], v[100:103]\n"
    "ds_read_b64_tr_b16 v[244:245], v5 offset:96\n"
    "buffer_load_dwordx4 v[200:203], v2, s[12:15], s32 offen offset:256 sc1\n"
    "v_mfma_f32_16x16x32_bf16 v[104:107], v[248:251], v[232:235], v[104:107]\n"
    "ds_read_b64_tr_b16 v[246:247], v5 offset:352\n"
    "v_accvgpr_read_b32 v226, a235\n"
    "v_mfma_f32_16x16x32_bf16 v[108:111], v[248:251], v[236:239], v[108:111]\n"
    "v_accvgpr_read_b32 v227, a239\n"
    "v_max3_u32 v9, v212, v213, v214\n"
    "v_mfma_f32_16x16x32_bf16 v[112:115], v[248:251], v[240:243], v[112:115]\n"
    "buffer_load_dwordx4 v[204:207], v2, s[12:15], s32 offen offset:512 sc1\n"
    "v_max3_u32 v9, v215, v216, v9\n"
    "s_waitcnt lgkmcnt(2)\n"
    "v_mfma_f32_16x16x32_bf16 v[116:119], v[252:255], v[228:231], v[116:119]\n"
    "ds_read_b64_tr_b16 v[248:249], v12 offset:29568\n"
    "v_max3_u32 v9, v217, v218, v9\n"
    "v_mfma_f32_16x16x32_bf16 v[120:123], v[252:255], v[232:235], v[120:123]\n"
    "ds_read_b64_tr_b16 v[250:251], v12 offset:29824\n"
    "v_max3_u32 v9, v219, v220, v9\n"
    "v_mfma_f32_16x16x32_bf16 v[124:127], v[252:255], v[236:239], v[124:127]\n"
    "v_max3_u32 v9, v221, v222, v9\n"
    "v_max3_u32 v9, v223, v224, v9\n"
    "v_mfma_f32_16x16x32_bf16 v[128:131], v[252:255], v[240:243], v[128:131]\n"
    "buffer_load_dwordx4 v[208:211], v2, s[12:15], s32 offen offset:768 sc1\n"
    "v_max3_u32 v9, v225, v226, v9\n"
    "s_waitcnt lgkmcnt(2)\n"
    "v_mfma_f32_16x16x32_bf16 v[132:135], v[244:247], v[228:231], v[132:135]\n"
    "ds_read_b64_tr_b16 v[252:253], v5 offset:29568\n"
    "v_max_u32 v9, v227, v9\n"
    "v_mfma_f32_16x16x32_bf16 v[136:139], v[244:247], v[232:235], v[136:139]\n"
    "ds_read_b64_tr_b16 v[254:255], v5 offset:29824\n"
    "v_cmp_eq_u32 vcc, -1, v9\n"
    "v_mfma_f32_16x16x32_bf16 v[140:143], v[244:247], v[236:239], v[140:143]\n"
    "v_mfma_f32_16x16x32_bf16 v[144:147], v[244:247], v[240:243], v[144:147]\n"
    "s_cbranch_vccnz Lk_hf_14\n"
    "Lk_hr_15:\n"
    "s_waitcnt lgkmcnt(2)\n"
    "v_mfma_f32_16x16x32_bf16 v[20:23], v[248:251], v[212:215], v[20:23]\n"
    "ds_read_b64_tr_b16 v[244:245], v12 offset:29600\n"
    "v_mfma_f32_16x16x32_bf16 v[24:27], v[248:251], v[216:219], v[24:27]\n"
    "ds_read_b64_tr_b16 v[246:247], v12 offset:29856\n"
    "v_mfma_f32_16x16x32_bf16 v[28:31], v[248:251], v[220:223], v[28:31]\n"
    "s_add_u32 s32, s41, 0\n"
    "buffer_load_dwordx4 a[224:227], v2, s[12:15], s32 offen offset:0 sc1\n"
    "v_mfma_f32_16x16x32_bf16 v[32:35], v[248:251], v[224:227], v[32:35]\n"
    "s_waitcnt lgkmcnt(2)\n"
    "v_mfma_f32_16x16x32_bf16 v[36:39], v[252:255], v[212:215], v[36:39]\n"
    "ds_read_b64_tr_b16 v[248:249], v5 offset:29600\n"
    "v_mfma_f32_16x16x32_bf16 v[40:43], v[252:255], v[216:219], v[40:43]\n"
    "ds_read_b64_tr_b16 v[250:251], v5 offset:29856\n"
    "v_mfma_f32_16x16x32_bf16 v[44:47], v[252:255], v[220:223], v[44:47]\n"
    "buffer_load_dwordx4 a[228:231], v2, s[12:15], s32 offen offset:256 sc1\n"
    "v_mfma_f32_16x16x32_bf16 v[48:51], v[252:255], v[224:227], v[48:51]\n"
    "s_waitcnt lgkmcnt(2)\n"
    "v_mfma_f32_16x16x32_bf16 v[52:55], v[244:247], v[212:215], v[52:55]\n"
    "ds_read_b64_tr_b16 v[252:253], v12 offset:29632\n"
    "s_waitcnt vmcnt(22)\n"
    "v_mfma_f32_16x16x32_bf16 v[56:59], v[244:247], v[216:219], v[56:59]\n"
    "ds_read_b64_tr_b16 v[254:255], v12 offset:29888\n"
    "v_accvgpr_read_b32 v228, a240\n"
    "v_mfma_f32_16x16x32_bf16 v[60:63], v[244:247], v[220:223], v[60:63]\n"
    "buffer_load_dwordx4 a[232:235], v2, s[12:15], s32 offen offset:512 sc1\n"
    "v_accvgpr_read_b32 v229, a244\n"
    "v_mfma_f32_16x16x32_bf16 v[64:67], v[244:247], v[224:227], v[64:67]\n"
    "v_accvgpr_read_b32 v230, a248\n"
    "v_accvgpr_read_b32 v231, a252\n"
    "s_waitcnt lgkmcnt(2)\n"
    "v_mfma_f32_16x16x32_bf16 v[68:71], v[248:251], v[212:215], v[68:71]\n"
    "ds_read_b64_tr_b16 v[244:245], v5 offset:29632\n"
    "v_accvgpr_read_b32 v232, a241\n"
    "v_mfma_f32_16x16x32_bf16 v[72:75], v[248:251], v[216:219], v[72:75]\n"
    "ds_read_b64_tr_b16 v[246:247], v5 offset:29888\n"
    "v_accvgpr_read_b32 v233, a245\n"
    "v_mfma_f32_16x16x32_bf16 v[76:79], v[248:251], v[220:223], v[76:79]\n"
    "buffer_load_dwordx4 a[236:239], v2, s[12:15], s32 offen offset:768 sc1\n"
    "v_accvgpr_read_b32 v234, a249\n"
    "v_mfma_f32_16x16x32_bf16 v[80:83], v[248:251], v[224:227], v[80:83]\n"
    "v_accvgpr_read_b32 v235, a253\n"
    "v_accvgpr_read_b32 v236, a242\n"
    "s_waitcnt lgkmcnt(2)\n"
    "v_mfma_f32_16x16x32_bf16 v[84:87], v[252:255], v[212:215], v[84:87]\n"
    "ds_read_b64_tr_b16 v[248:249], v12 offset:29664\n"
    "v_accvgpr_read_b32 v237, a246\n"
    "v_mfma_f32_16x16x32_bf16 v[88:91], v[252:255], v[216:219], v[88:91]\n"
    "ds_read_b64_tr_b16 v[250:251], v12 offset:29920\n"
    "v_accvgpr_read_b32 v238, a250\n"
    "v_mfma_f32_16x16x32_bf16 v[92:95], v[252:255], v[220:223], v[92:95]\n"
    "s_add_u32 s11, s40, 0\n"
    "buffer_load_dwordx4 a[0:3], v13, s[12:15], s11 offen offset:0 sc1\n"
    "v_accvgpr_read_b32 v239, a254\n"
    "v_mfma_f32_16x16x32_bf16 v[96:99], v[252:255], v[224:227], v[96:99]\n"
    "v_accvgpr_read_b32 v240, a243\n"
    "v_accvgpr_read_b32 v241, a247\n"
    "s_waitcnt lgkmcnt(2)\n"
    "v_mfma_f32_16x16x32_bf16 v[100:103], v[244:247], v[212:215], v[100:103]\n"
    "ds_read_b64_tr_b16 v[252:253], v5 offset:29664\n"
    "v_accvgpr_read_b32 v242, a251\n"
    "v_mfma_f32_16x16x32_bf16 v[104:107], v[244:247], v[216:219], v[104:107]\n"
    "ds_read_b64_tr_b16 v[254:255], v5 offset:29920\n"
    "v_accvgpr_read_b32 v243, a255\n"
    "v_mfma_f32_16x16x32_bf16 v[108:111], v[244:247], v[220:223], v[108:111]\n"
    "buffer_load_dwordx4 a[4:7], v13, s[12:15], s11 offen offset:256 sc1\n"
    "v_max3_u32 v9, v228, v229, v230\n"
    "v_mfma_f32_16x16x32_bf16 v[112:115], v[244:247], v[224:227], v[112:115]\n"
    "v_max3_u32 v9, v231, v232, v9\n"
    "v_max3_u32 v9, v233, v234, v9\n"
    "s_waitcnt lgkmcnt(2)\n"
    "v_mfma_f32_16x16x32_bf16 v[116:119], v[248:251], v[212:215], v[116:119]\n"
    "ds_read_b64_tr_b16 v[244:245], v12 offset:33792\n"
    "v_max3_u32 v9, v235, v236, v9\n"
    "v_mfma_f32_16x16x32_bf16 v[120:123], v[248:251], v[216:219], v[120:123]\n"
    "ds_read_b64_tr_b16 v[246:247], v12 offset:34048\n"
    "v_max3_u32 v9, v237, v238, v9\n"
    "v_mfma_f32_16x16x32_bf16 v[124:127], v[248:251], v[220:223], v[124:127]\n"
    "buffer_load_dwordx4 a[8:11], v13, s[12:15], s11 offen offset:512 sc1\n"
    "v_max3_u32 v9, v239, v240, v9\n"
    "v_mfma_f32_16x16x32_bf16 v[128:131], v[248:251], v[224:227], v[128:131]\n"
    "v_max3_u32 v9, v241, v242, v9\n"
    "v_max_u32 v9, v243, v9\n"
    "s_waitcnt lgkmcnt(2)\n"
    "v_mfma_f32_16x16x32_bf16 v[132:135], v[252:255], v[212:215], v[132:135]\n"
    "ds_read_b64_tr_b16 v[248:249], v5 offset:33792\n"
    "v_cmp_eq_u32 vcc, -1, v9\n"
    "v_mfma_f32_16x16x32_bf16 v[136:139], v[252:255], v[216:219], v[136:139]\n"
    "ds_read_b64_tr_b16 v[250:251], v5 offset:34048\n"
    "v_mfma_f32_16x16x32_bf16 v[140:143], v[252:255], v[220:223], v[140:143]\n"
    "v_mfma_f32_16x16x32_bf16 v[144:147], v[252:255], v[224:227], v[144:147]\n"
    "s_cbranch_vccnz Lk_hf_16\n"
    "Lk_hr_17:\n"
    "s_waitcnt lgkmcnt(2)\n"
    "v_mfma_f32_16x16x32_bf16 v[20:23], v[244:247], v[228:231], v[20:23]\n"
    "ds_read_b64_tr_b16 v[252:253], v12 offset:33824\n"
    "v_mfma_f32_16x16x32_bf16 v[24:27], v[244:247], v[232:235], v[24:27]\n"
    "ds_read_b64_tr_b16 v[254:255], v12 offset:34080\n"
    "v_mfma_f32_16x16x32_bf16 v[28:31], v[244:247], v[236:239], v[28:31]\n"
    "s_add_u32 s32, s41, 4096\n"
    "buffer_load_dwordx4 a[240:243], v2, s[12:15], s32 offen offset:0 sc1\n"
    "v_mfma_f32_16x16x32_bf16 v[32:35], v[244:247], v[240:243], v[32:35]\n"
    "s_waitcnt lgkmcnt(2)\n"
    "v_mfma_f32_16x16x32_bf16 v[36:39], v[248:251], v[228:231], v[36:39]\n"
    "ds_read_b64_tr_b16 v[244:245], v5 offset:33824\n"
    "v_mfma_f32_16x16x32_bf16 v[40:43], v[248:251], v[232:235], v[40:43]\n"
    "ds_read_b64_tr_b16 v[246:247], v5 offset:34080\n"
    "v_mfma_f32_16x16x32_bf16 v[44:47], v[248:251], v[236:239], v[44:47]\n"
    "buffer_load_dwordx4 a[244:247], v2, s[12:15], s32 offen offset:256 sc1\n"
    "v_mfma_f32_16x16x32_bf16 v[48:51], v[248:251], v[240:243], v[48:51]\n"
    "s_waitcnt lgkmcnt(2)\n"
    "v_mfma_f32_16x16x32_bf16 v[52:55], v[252:255], v[228:231], v[52:55]\n"
    "ds_read_b64_tr_b16 v[248:249], v12 offset:33856\n"
    "s_waitcnt vmcnt(21)\n"
    "v_mfma_f32_16x16x32_bf16 v[56:59], v[252:255], v[232:235], v[56:59]\n"
    "ds_read_b64_tr_b16 v[250:251], v12 offset:34112\n"
    "v_mov_b32 v212, v148\n"
    "v_mfma_f32_16x16x32_bf16 v[60:63], v[252:255], v[236:239], v[60:63]\n"
    "buffer_load_dwordx4 a[248:251], v2, s[12:15], s32 offen offset:512 sc1\n"
    "v_mov_b32 v213, v152\n"
    "v_mfma_f32_16x16x32_bf16 v[64:67], v[252:255], v[240:243], v[64:67]\n"
    "v_mov_b32 v214, v156\n"
    "v_mov_b32 v215, v160\n"
    "s_waitcnt lgkmcnt(2)\n"
    "v_mfma_f32_16x16x32_bf16 v[68:71], v[244:247], v[228:231], v[68:71]\n"
    "ds_read_b64_tr_b16 v[252:253], v5 offset:33856\n"
    "v_mov_b32 v216, v149\n"
    "v_mfma_f32_16x16x32_bf16 v[72:75], v[244:247], v[232:235], v[72:75]\n"
    "ds_read_b64_tr_b16 v[254:255], v5 offset:34112\n"
    "v_mov_b32 v217, v153\n"
    "v_mfma_f32_16x16x32_bf16 v[76:79], v[244:247], v[236:239], v[76:79]\n"
    "buffer_load_dwordx4 a[252:255], v2, s[12:15], s32 offen offset:768 sc1\n"
    "v_mov_b32 v218, v157\n"
    "v_mfma_f32_16x16x32_bf16 v[80:83], v[244:247], v[240:243], v[80:83]\n"
    "v_mov_b32 v219, v161\n"
    "v_mov_b32 v220, v150\n"
    "s_waitcnt lgkmcnt(2)\n"
    "v_mfma_f32_16x16x32_bf16 v[84:87], v[248:251], v[228:231], v[84:87]\n"
    "ds_read_b64_tr_b16 v[244:245], v12 offset:33888\n"
    "v_mov_b32 v221, v154\n"
    "v_mfma_f32_16x16x32_bf16 v[88:91], v[248:251], v[232:235], v[88:91]\n"
    "ds_read_b64_tr_b16 v[246:247], v12 offset:34144\n"
    "v_mov_b32 v222, v158\n"
    "v_mfma_f32_16x16x32_bf16 v[92:95], v[248:251], v[236:239], v[92:95]\n"
    "buffer_load_dwordx4 a[12:15], v13, s[12:15], s11 offen offset:768 sc1\n"
    "v_mov_b32 v223, v162\n"
    "v_mfma_f32_16x16x32_bf16 v[96:99], v[248:251], v[240:243], v[96:99]\n"
    "v_mov_b32 v224, v151\n"
    "v_mov_b32 v225, v155\n"
    "s_waitcnt lgkmcnt(2)\n"
    "v_mfma_f32_16x16x32_bf16 v[100:103], v[252:255], v[228:231], v[100:103]\n"
    "ds_read_b64_tr_b16 v[248:249], v5 offset:33888\n"
    "v_mov_b32 v226, v159\n"
    "v_mfma_f32_16x16x32_bf16 v[104:107], v[252:255], v[232:235], v[104:107]\n"
    "ds_read_b64_tr_b16 v[250:251], v5 offset:34144\n"
    "v_mov_b32 v227, v163\n"
    "v_mfma_f32_16x16x32_bf16 v[108:111], v[252:255], v[236:239], v[108:111]\n"
    "s_add_u32 s11, s40, 16384\n"
    "buffer_load_dwordx4 a[16:19], v13, s[12:15], s11 offen offset:0 sc1\n"
    "v_max3_u32 v9, v212, v213, v214\n"
    "v_mfma_f32_16x16x32_bf16 v[112:115], v[252:255], v[240:243], v[112:115]\n"
    "v_max3_u32 v9, v215, v216, v9\n"
    "v_max3_u32 v9, v217, v218, v9\n"
    "s_waitcnt lgkmcnt(2)\n"
    "v_mfma_f32_16x16x32_bf16 v[116:119], v[244:247], v[228:231], v[116:119]\n"
    "ds_read_b64_tr_b16 v[252:253], v12 offset:38016\n"
    "v_max3_u32 v9, v219, v220, v9\n"
    "v_mfma_f32_16x16x32_bf16 v[120:123], v[244:247], v[232:235], v[120:123]\n"
    "ds_read_b64_tr_b16 v[254:255], v12 offset:38272\n"
    "v_max3_u32 v9, v221, v222, v9\n"
    "v_mfma_f32_16x16x32_bf16 v[124:127], v[244:247], v[236:239], v[124:127]\n"
    "buffer_load_dwordx4 a[20:23], v13, s[12:15], s11 offen offset:256 sc1\n"
    "v_max3_u32 v9, v223, v224, v9\n"
    "v_mfma_f32_16x16x32_bf16 v[128:131], v[244:247], v[240:243], v[128:131]\n"
    "v_max3_u32 v9, v225, v226, v9\n"
    "v_max_u32 v9, v227, v9\n"
    "s_waitcnt lgkmcnt(2)\n"
    "v_mfma_f32_16x16x32_bf16 v[132:135], v[248:251], v[228:231], v[132:135]\n"
    "ds_read_b64_tr_b16 v[244:245], v5 offset:38016\n"
    "v_cmp_eq_u32 vcc, -1, v9\n"
    "v_mfma_f32_16x16x32_bf16 v[136:139], v[248:251], v[232:235], v[136:139]\n"
    "ds_read_b64_tr_b16 v[246:247], v5 offset:38272\n"
    "v_mfma_f32_16x16x32_bf16 v[140:143], v[248:251], v[236:239], v[140:143]\n"
    "v_mfma_f32_16x16x32_bf16 v[144:147], v[248:251], v[240:243], v[144:147]\n"
    "s_cbranch_vccnz Lk_hf_18\n"
    "Lk_hr_19:\n"
    "s_waitcnt lgkmcnt(2)\n"
    "v_mfma_f32_16x16x32_bf16 v[20:23], v[252:255], v[212:215], v[20:23]\n"
    "ds_read_b64_tr_b16 v[248:249], v12 offset:38048\n"
    "v_mfma_f32_16x16x32_bf16 v[24:27], v[252:255], v[216:219], v[24:27]\n"
    "ds_read_b64_tr_b16 v[250:251], v12 offset:38304\n"
    "v_mfma_f32_16x16x32_bf16 v[28:31], v[252:255], v[220:223], v[28:31]\n"
    "s_add_u32 s32, s41, 57344\n"
    "buffer_load_dwordx4 v[148:151], v2, s[12:15], s32 offen offset:0 sc1\n"
    "v_mfma_f32_16x16x32_bf16 v[32:35], v[252:255], v[224:227], v[32:35]\n"
    "s_waitcnt lgkmcnt(2)\n"
    "v_mfma_f32_16x16x32_bf16 v[36:39], v[244:247], v[212:215], v[36:39]\n"
    "ds_read_b64_tr_b16 v[252:253], v5 offset:38048\n"
    "v_mfma_f32_16x16x32_bf16 v[40:43], v[244:247], v[216:219], v[40:43]\n"
    "ds_read_b64_tr_b16 v[254:255], v5 offset:38304\n"
    "v_mfma_f32_16x16x32_bf16 v[44:47], v[244:247], v[220:223], v[44:47]\n"
    "buffer_load_dwordx4 v[152:155], v2, s[12:15], s32 offen offset:256 sc1\n"
    "v_mfma_f32_16x16x32_bf16 v[48:51], v[244:247], v[224:227], v[48:51]\n"
    "s_waitcnt lgkmcnt(2)\n"
    "v_mfma_f32_16x16x32_bf16 v[52:55], v[248:251], v[212:215], v[52:55]\n"
    "ds_read_b64_tr_b16 v[244:245], v12 offset:38080\n"
    "s_waitcnt vmcnt(24)\n"
    "v_mfma_f32_16x16x32_bf16 v[56:59], v[248:251], v[216:219], v[56:59]\n"
    "ds_read_b64_tr_b16 v[246:247], v12 offset:38336\n"
    "v_mov_b32 v228, v164\n"
    "v_mfma_f32_16x16x32_bf16 v[60:63], v[248:251], v[220:223], v[60:63]\n"
    "buffer_load_dwordx4 v[156:159], v2, s[12:15], s32 offen offset:512 sc1\n"
    "v_mov_b32 v229, v168\n"
    "v_mfma_f32_16x16x32_bf16 v[64:67], v[248:251], v[224:227], v[64:67]\n"
    "v_mov_b32 v230, v172\n"
    "v_mov_b32 v231, v176\n"
    "s_waitcnt lgkmcnt(2)\n"
    "v_mfma_f32_16x16x32_bf16 v[68:71], v[252:255], v[212:215], v[68:71]\n"
    "ds_read_b64_tr_b16 v[248:249], v5 offset:38080\n"
    "v_mov_b32 v232, v165\n"
    "v_mfma_f32_16x16x32_bf16 v[72:75], v[252:255], v[216:219], v[72:75]\n"
    "ds_read_b64_tr_b16 v[250:251], v5 offset:38336\n"
    "v_mov_b32 v233, v169\n"
    "v_mfma_f32_16x16x32_bf16 v[76:79], v[252:255], v[220:223], v[76:79]\n"
    "buffer_load_dwordx4 v[160:163], v2, s[12:15], s32 offen offset:768 sc1\n"
    "v_mov_b32 v234, v173\n"
    "v_mfma_f32_16x16x32_bf16 v[80:83], v[252:255], v[224:227], v[80:83]\n"
    "v_mov_b32 v235, v177\n"
    "v_mov_b32 v236, v166\n"
    "s_waitcnt lgkmcnt(2)\n"
    "v_mfma_f32_16x16x32_bf16 v[84:87], v[244:247], v[212:215], v[84:87]\n"
    "ds_read_b64_tr_b16 v[252:253], v12 offset:38112\n"
    "v_mov_b32 v237, v170\n"
    "v_mfma_f32_16x16x32_bf16 v[88:91], v[244:247], v[216:219], v[88:91]\n"
    "ds_read_b64_tr_b16 v[254:255], v12 offset:38368\n"
    "v_mov_b32 v238, v174\n"
    "v_mfma_f32_16x16x32_bf16 v[92:95], v[244:247], v[220:223], v[92:95]\n"
    "buffer_load_dwordx4 a[24:27], v13, s[12:15], s11 offen offset:512 sc1\n"
    "v_mov_b32 v239, v178\n"
    "v_mfma_f32_16x16x32_bf16 v[96:99], v[244:247], v[224:227], v[96:99]\n"
    "v_mov_b32 v240, v167\n"
    "v_mov_b32 v241, v171\n"
    "s_waitcnt lgkmcnt(2)\n"
    "v_mfma_f32_16x16x32_bf16 v[100:103], v[248:251], v[212:215], v[100:103]\n"
    "ds_read_b64_tr_b16 v[244:245], v5 offset:38112\n"
    "v_mov_b32 v242, v175\n"
    "v_mfma_f32_16x16x32_bf16 v[104:107], v[248:251], v[216:219], v[104:107]\n"
    "ds_read_b64_tr_b16 v[246:247], v5 offset:38368\n"
    "v_mov_b32 v243, v179\n"
    "v_mfma_f32_16x16x32_bf16 v[108:111], v[248:251], v[220:223], v[108:111]\n"
    "buffer_load_dwordx4 a[28:31], v13, s[12:15], s11 offen offset:768 sc1\n"
    "v_max3_u32 v9, v228, v229, v230\n"
    "v_mfma_f32_16x16x32_bf16 v[112:115], v[248:251], v[224:227], v[112:115]\n"
    "v_max3_u32 v9, v231, v232, v9\n"
    "v_max3_u32 v9, v233, v234, v9\n"
    "s_waitcnt lgkmcnt(2)\n"
    "v_mfma_f32_16x16x32_bf16 v[116:119], v[252:255], v[212:215], v[116:119]\n"
    "ds_read_b64_tr_b16 v[248:249], v12 offset:4224\n"
    "v_max3_u32 v9, v235, v236, v9\n"
    "v_mfma_f32_16x16x32_bf16 v[120:123], v[252:255], v[216:219], v[120:123]\n"
    "ds_read_b64_tr_b16 v[250:251], v12 offset:4480\n"
    "v_max3_u32 v9, v237, v238, v9\n"
    "v_mfma_f32_16x16x32_bf16 v[124:127], v[252:255], v[220:223], v[124:127]\n"
    "s_add_u32 s11, s40, 32768\n"
    "buffer_load_dwordx4 a[32:35], v13, s[12:15], s11 offen offset:0 sc1\n"
    "v_max3_u32 v9, v239, v240, v9\n"
    "v_mfma_f32_16x16x32_bf16 v[128:131], v[252:255], v[224:227], v[128:131]\n"
    "v_max3_u32 v9, v241, v242, v9\n"
    "v_max_u32 v9, v243, v9\n"
    "s_waitcnt lgkmcnt(2)\n"
    "v_mfma_f32_16x16x32_bf16 v[132:135], v[244:247], v[212:215], v[132:135]\n"
    "ds_read_b64_tr_b16 v[252:253], v5 offset:4224\n"
    "v_cmp_eq_u32 vcc, -1, v9\n"
    "v_mfma_f32_16x16x32_bf16 v[136:139], v[244:247], v[216:219], v[136:139]\n"
    "ds_read_b64_tr_b16 v[254:255], v5 offset:4480\n"
    "v_mfma_f32_16x16x32_bf16 v[140:143], v[244:247], v[220:223], v[140:143]\n"
    "v_mfma_f32_16x16x32_bf16 v[144:147], v[244:247], v[224:227], v[144:147]\n"
    "s_cbranch_vccnz Lk_hf_20\n"
    "Lk_hr_21:\n"
    "s_waitcnt lgkmcnt(2)\n"
    "v_mfma_f32_16x16x32_bf16 v[20:23], v[248:251], v[228:231], v[20:23]\n"
    "ds_read_b64_tr_b16 v[244:245], v12 offset:4256\n"
    "v_mfma_f32_16x16x32_bf16 v[24:27], v[248:251], v[232:235], v[24:27]\n"
    "ds_read_b64_tr_b16 v[246:247], v12 offset:4512\n"
    "v_mfma_f32_16x16x32_bf16 v[28:31], v[248:251], v[236:239], v[28:31]\n"
    "s_add_u32 s32, s41, 61440\n"
    "buffer_load_dwordx4 v[164:167], v2, s[12:15], s32 offen offset:0 sc1\n"
    "v_mfma_f32_16x16x32_bf16 v[32:35], v[248:251], v[240:243], v[32:35]\n"
    "s_waitcnt lgkmcnt(2)\n"
    "v_mfma_f32_16x16x32_bf16 v[36:39], v[252:255], v[228:231], v[36:39]\n"
    "ds_read_b64_tr_b16 v[248:249], v5 offset:4256\n"
    "v_mfma_f32_16x16x32_bf16 v[40:43], v[252:255], v[232:235], v[40:43]\n"
    "ds_read_b64_tr_b16 v[250:251], v5 offset:4512\n"
    "v_mfma_f32_16x16x32_bf16 v[44:47], v[252:255], v[236:239], v[44:47]\n"
    "buffer_load_dwordx4 v[168:171], v2, s[12:15], s32 offen offset:256 sc1\n"
    "v_mfma_f32_16x16x32_bf16 v[48:51], v[252:255], v[240:243], v[48:51]\n"
    "s_waitcnt lgkmcnt(2)\n"
    "v_mfma_f32_16x16x32_bf16 v[52:55], v[244:247], v[228:231], v[52:55]\n"
    "ds_read_b64_tr_b16 v[252:253], v12 offset:4288\n"
    "s_waitcnt vmcnt(27)\n"
    "v_mfma_f32_16x16x32_bf16 v[56:59], v[244:247], v[232:235], v[56:59]\n"
    "ds_read_b64_tr_b16 v[254:255], v12 offset:4544\n"
    "v_mov_b32 v212, v180\n"
    "v_mfma_f32_16x16x32_bf16 v[60:63], v[244:247], v[236:239], v[60:63]\n"
    "buffer_load_dwordx4 v[172:175], v2, s[12:15], s32 offen offset:512 sc1\n"
    "v_mov_b32 v213, v184\n"
    "v_mfma_f32_16x16x32_bf16 v[64:67], v[244:247], v[240:243], v[64:67]\n"
    "v_mov_b32 v214, v188\n"
    "v_mov_b32 v215, v192\n"
    "s_waitcnt lgkmcnt(2)\n"
    "v_mfma_f32_16x16x32_bf16 v[68:71], v[248:251], v[228:231], v[68:71]\n"
    "ds_read_b64_tr_b16 v[244:245], v5 offset:4288\n"
    "v_mov_b32 v216, v181\n"
    "v_mfma_f32_16x16x32_bf16 v[72:75], v[248:251], v[232:235], v[72:75]\n"
    "ds_read_b64_tr_b16 v[246:247], v5 offset:4544\n"
    "v_mov_b32 v217, v185\n"
    "v_mfma_f32_16x16x32_bf16 v[76:79], v[248:251], v[236:239], v[76:79]\n"
    "buffer_load_dwordx4 v[176:179], v2, s[12:15], s32 offen offset:768 sc1\n"
    "v_mov_b32 v218, v189\n"
    "v_mfma_f32_16x16x32_bf16 v[80:83], v[248:251], v[240:243], v[80:83]\n"
    "v_mov_b32 v219, v193\n"
    "v_mov_b32 v220, v182\n"
    "s_waitcnt lgkmcnt(2)\n"
    "v_mfma_f32_16x16x32_bf16 v[84:87], v[252:255], v[228:231], v[84:87]\n"
    "ds_read_b64_tr_b16 v[248:249], v12 offset:4320\n"
    "v_mov_b32 v221, v186\n"
    "v_mfma_f32_16x16x32_bf16 v[88:91], v[252:255], v[232:235], v[88:91]\n"
    "ds_read_b64_tr_b16 v[250:251], v12 offset:4576\n"
    "v_mov_b32 v222, v190\n"
    "v_mfma_f32_16x16x32_bf16 v[92:95], v[252:255], v[236:239], v[92:95]\n"
    "buffer_load_dwordx4 a[36:39], v13, s[12:15], s11 offen offset:256 sc1\n"
    "v_mov_b32 v223, v194\n"
    "v_mfma_f32_16x16x32_bf16 v[96:99], v[252:255], v[240:243], v[96:99]\n"
    "v_mov_b32 v224, v183\n"
    "v_mov_b32 v225, v187\n"
    "s_waitcnt lgkmcnt(2)\n"
    "v_mfma_f32_16x16x32_bf16 v[100:103], v[244:247], v[228:231], v[100:103]\n"
    "ds_read_b64_tr_b16 v[252:253], v5 offset:4320\n"
    "v_mov_b32 v226, v191\n"
    "v_mfma_f32_16x16x32_bf16 v[104:107], v[244:247], v[232:235], v[104:107]\n"
    "ds_read_b64_tr_b16 v[254:255], v5 offset:4576\n"
    "v_mov_b32 v227, v195\n"
    "v_mfma_f32_16x16x32_bf16 v[108:111], v[244:247], v[236:239], v[108:111]\n"
    "buffer_load_dwordx4 a[40:43], v13, s[12:15], s11 offen offset:512 sc1\n"
    "v_mfma_f32_16x16x32_bf16 v[112:115], v[244:247], v[240:243], v[112:115]\n"
    "s_waitcnt lgkmcnt(2)\n"
    "v_mfma_f32_16x16x32_bf16 v[116:119], v[248:251], v[228:231], v[116:119]\n"
    "ds_read_b64_tr_b16 v[244:245], v12 offset:42240\n"
    "v_mfma_f32_16x16x32_bf16 v[120:123], v[248:251], v[232:235], v[120:123]\n"
    "ds_read_b64_tr_b16 v[246:247], v12 offset:42496\n"
    "v_mfma_f32_16x16x32_bf16 v[124:127], v[248:251], v[236:239], v[124:127]\n"
    "buffer_load_dwordx4 a[44:47], v13, s[12:15], s11 offen offset:768 sc1\n"
    "v_mfma_f32_16x16x32_bf16 v[128:131], v[248:251], v[240:243], v[128:131]\n"
    "s_waitcnt lgkmcnt(2)\n"
    "v_mfma_f32_16x16x32_bf16 v[132:135], v[252:255], v[228:231], v[132:135]\n"
    "ds_read_b64_tr_b16 v[248:249], v5 offset:42240\n"
    "v_mfma_f32_16x16x32_bf16 v[136:139], v[252:255], v[232:235], v[136:139]\n"
    "ds_read_b64_tr_b16 v[250:251], v5 offset:42496\n"
    "v_mfma_f32_16x16x32_bf16 v[140:143], v[252:255], v[236:239], v[140:143]\n"
    "v_mfma_f32_16x16x32_bf16 v[144:147], v[252:255], v[240:243], v[144:147]\n"
    "s_waitcnt lgkmcnt(2)\n"
    "v_mfma_f32_16x16x32_bf16 v[20:23], v[244:247], v[212:215], v[20:23]\n"
    "ds_read_b64_tr_b16 v[252:253], v12 offset:42272\n"
    "v_mfma_f32_16x16x32_bf16 v[24:27], v[244:247], v[216:219], v[24:27]\n"
    "ds_read_b64_tr_b16 v[254:255], v12 offset:42528\n"
    "v_mfma_f32_16x16x32_bf16 v[28:31], v[244:247], v[220:223], v[28:31]\n"
    "s_add_u32 s32, s41, 49152\n"
    "buffer_load_dwordx4 v[180:183], v2, s[12:15], s32 offen offset:0 sc1\n"
    "v_mfma_f32_16x16x32_bf16 v[32:35], v[244:247], v[224:227], v[32:35]\n"
    "s_waitcnt lgkmcnt(2)\n"
    "v_mfma_f32_16x16x32_bf16 v[36:39], v[248:251], v[212:215], v[36:39]\n"
    "ds_read_b64_tr_b16 v[244:245], v5 offset:42272\n"
    "v_mfma_f32_16x16x32_bf16 v[40:43], v[248:251], v[216:219], v[40:43]\n"
    "ds_read_b64_tr_b16 v[246:247], v5 offset:42528\n"
    "v_mfma_f32_16x16x32_bf16 v[44:47], v[248:251], v[220:223], v[44:47]\n"
    "buffer_load_dwordx4 v[184:187], v2, s[12:15], s32 offen offset:256 sc1\n"
    "v_mfma_f32_16x16x32_bf16 v[48:51], v[248:251], v[224:227], v[48:51]\n"
    "s_waitcnt lgkmcnt(2)\n"
    "v_mfma_f32_16x16x32_bf16 v[52:55], v[252:255], v[212:215], v[52:55]\n"
    "ds_read_b64_tr_b16 v[248:249], v12 offset:42304\n"
    "s_waitcnt vmcnt(30)\n"
    "v_mfma_f32_16x16x32_bf16 v[56:59], v[252:255], v[216:219], v[56:59]\n"
    "ds_read_b64_tr_b16 v[250:251], v12 offset:42560\n"
    "v_mov_b32 v228, v196\n"
    "v_mfma_f32_16x16x32_bf16 v[60:63], v[252:255], v[220:223], v[60:63]\n"
    "buffer_load_dwordx4 v[188:191], v2, s[12:15], s32 offen offset:512 sc1\n"
    "v_mov_b32 v229, v200\n"
    "v_mfma_f32_16x16x32_bf16 v[64:67], v[252:255], v[224:227], v[64:67]\n"
    "v_mov_b32 v230, v204\n"
    "v_mov_b32 v231, v208\n"
    "s_waitcnt lgkmcnt(2)\n"
    "v_mfma_f32_16x16x32_bf16 v[68:71], v[244:247], v[212:215], v[68:71]\n"
    "ds_read_b64_tr_b16 v[252:253], v5 offset:42304\n"
    "v_mov_b32 v232, v197\n"
    "v_mfma_f32_16x16x32_bf16 v[72:75], v[244:247], v[216:219], v[72:75]\n"
    "ds_read_b64_tr_b16 v[254:255], v5 offset:42560\n"
    "v_mov_b32 v233, v201\n"
    "v_mfma_f32_16x16x32_bf16 v[76:79], v[244:247], v[220:223], v[76:79]\n"
    "buffer_load_dwordx4 v[192:195], v2, s[12:15], s32 offen offset:768 sc1\n"
    "v_mov_b32 v234, v205\n"
    "v_mfma_f32_16x16x32_bf16 v[80:83], v[244:247], v[224:227], v[80:83]\n"
    "v_mov_b32 v235, v209\n"
    "v_mov_b32 v236, v198\n"
    "s_waitcnt lgkmcnt(2)\n"
    "v_mfma_f32_16x16x32_bf16 v[84:87], v[248:251], v[212:215], v[84:87]\n"
    "ds_read_b64_tr_b16 v[244:245], v12 offset:42336\n"
    "v_mov_b32 v237, v202\n"
    "v_mfma_f32_16x16x32_bf16 v[88:91], v[248:251], v[216:219], v[88:91]\n"
    "ds_read_b64_tr_b16 v[246:247], v12 offset:42592\n"
    "v_mov_b32 v238, v206\n"
    "v_mfma_f32_16x16x32_bf16 v[92:95], v[248:251], v[220:223], v[92:95]\n"
    "s_add_u32 s11, s40, 2048\n"
    "buffer_load_dwordx4 a[48:51], v14, s[12:15], s11 offen offset:0 sc1\n"
    "v_mov_b32 v239, v210\n"
    "v_mfma_f32_16x16x32_bf16 v[96:99], v[248:251], v[224:227], v[96:99]\n"
    "v_mov_b32 v240, v199\n"
    "v_mov_b32 v241, v203\n"
    "s_waitcnt lgkmcnt(2)\n"
    "v_mfma_f32_16x16x32_bf16 v[100:103], v[252:255], v[212:215], v[100:103]\n"
    "ds_read_b64_tr_b16 v[248:249], v5 offset:42336\n"
    "v_mov_b32 v242, v207\n"
    "v_mfma_f32_16x16x32_bf16 v[104:107], v[252:255], v[216:219], v[104:107]\n"
    "ds_read_b64_tr_b16 v[250:251], v5 offset:42592\n"
    "v_mov_b32 v243, v211\n"
    "v_mfma_f32_16x16x32_bf16 v[108:111], v[252:255], v[220:223], v[108:111]\n"
    "buffer_load_dwordx4 a[52:55], v14, s[12:15], s11 offen offset:256 sc1\n"
    "v_mfma_f32_16x16x32_bf16 v[112:115], v[252:255], v[224:227], v[112:115]\n"
    "s_waitcnt lgkmcnt(2)\n"
    "v_mfma_f32_16x16x32_bf16 v[116:119], v[244:247], v[212:215], v[116:119]\n"
    "ds_read_b64_tr_b16 v[252:253], v12 offset:46464\n"
    "v_mfma_f32_16x16x32_bf16 v[120:123], v[244:247], v[216:219], v[120:123]\n"
    "ds_read_b64_tr_b16 v[254:255], v12 offset:46720\n"
    "v_mfma_f32_16x16x32_bf16 v[124:127], v[244:247], v[220:223], v[124:127]\n"
    "buffer_load_dwordx4 a[56:59], v14, s[12:15], s11 offen offset:512 sc1\n"
    "v_mfma_f32_16x16x32_bf16 v[128:131], v[244:247], v[224:227], v[128:131]\n"
    "s_waitcnt lgkmcnt(2)\n"
    "v_mfma_f32_16x16x32_bf16 v[132:135], v[248:251], v[212:215], v[132:135]\n"
    "ds_read_b64_tr_b16 v[244:245], v5 offset:46464\n"
    "v_mfma_f32_16x16x32_bf16 v[136:139], v[248:251], v[216:219], v[136:139]\n"
    "ds_read_b64_tr_b16 v[246:247], v5 offset:46720\n"
    "v_mfma_f32_16x16x32_bf16 v[140:143], v[248:251], v[220:223], v[140:143]\n"
    "v_mfma_f32_16x16x32_bf16 v[144:147], v[248:251], v[224:227], v[144:147]\n"
    "s_waitcnt lgkmcnt(2)\n"
    "v_mfma_f32_16x16x32_bf16 v[20:23], v[252:255], v[228:231], v[20:23]\n"
    "ds_read_b64_tr_b16 v[248:249], v12 offset:46496\n"
    "v_mfma_f32_16x16x32_bf16 v[24:27], v[252:255], v[232:235], v[24:27]\n"
    "ds_read_b64_tr_b16 v[250:251], v12 offset:46752\n"
    "v_mfma_f32_16x16x32_bf16 v[28:31], v[252:255], v[236:239], v[28:31]\n"
    "s_add_u32 s32, s41, 53248\n"
    "buffer_load_dwordx4 v[196:199], v2, s[12:15], s32 offen offset:0 sc1\n"
    "v_mfma_f32_16x16x32_bf16 v[32:35], v[252:255], v[240:243], v[32:35]\n"
    "s_waitcnt lgkmcnt(2)\n"
    "v_mfma_f32_16x16x32_bf16 v[36:39], v[244:247], v[228:231], v[36:39]\n"
    "ds_read_b64_tr_b16 v[252:253], v5 offset:46496\n"
    "v_mfma_f32_16x16x32_bf16 v[40:43], v[244:247], v[232:235], v[40:43]\n"
    "ds_read_b64_tr_b16 v[254:255], v5 offset:46752\n"
    "v_mfma_f32_16x16x32_bf16 v[44:47], v[244:247], v[236:239], v[44:47]\n"
    "buffer_load_dwordx4 v[200:203], v2, s[12:15], s32 offen offset:256 sc1\n"
    "v_mfma_f32_16x16x32_bf16 v[48:51], v[244:247], v[240:243], v[48:51]\n"
    "s_waitcnt lgkmcnt(2)\n"
    "v_mfma_f32_16x16x32_bf16 v[52:55], v[248:251], v[228:231], v[52:55]\n"
    "ds_read_b64_tr_b16 v[244:245], v12 offset:46528\n"
    "s_waitcnt vmcnt(33)\n"
    "v_mfma_f32_16x16x32_bf16 v[56:59], v[248:251], v[232:235], v[56:59]\n"
    "ds_read_b64_tr_b16 v[246:247], v12 offset:46784\n"
    "v_accvgpr_read_b32 v212, a224\n"
    "v_mfma_f32_16x16x32_bf16 v[60:63], v[248:251], v[236:239], v[60:63]\n"
    "buffer_load_dwordx4 v[204:207], v2, s[12:15], s32 offen offset:512 sc1\n"
    "v_accvgpr_read_b32 v213, a228\n"
    "v_mfma_f32_16x16x32_bf16 v[64:67], v[248:251], v[240:243], v[64:67]\n"
    "v_accvgpr_read_b32 v214, a232\n"
    "v_accvgpr_read_b32 v215, a236\n"
    "s_waitcnt lgkmcnt(2)\n"
    "v_mfma_f32_16x16x32_bf16 v[68:71], v[252:255], v[228:231], v[68:71]\n"
    "ds_read_b64_tr_b16 v[248:249], v5 offset:46528\n"
    "v_accvgpr_read_b32 v216, a225\n"
    "v_mfma_f32_16x16x32_bf16 v[72:75], v[252:255], v[232:235], v[72:75]\n"
    "ds_read_b64_tr_b16 v[250:251], v5 offset:46784\n"
    "v_accvgpr_read_b32 v217, a229\n"
    "v_mfma_f32_16x16x32_bf16 v[76:79], v[252:255], v[236:239], v[76:79]\n"
    "buffer_load_dwordx4 v[208:211], v2, s[12:15], s32 offen offset:768 sc1\n"
    "v_accvgpr_read_b32 v218, a233\n"
    "v_mfma_f32_16x16x32_bf16 v[80:83], v[252:255], v[240:243], v[80:83]\n"
    "v_accvgpr_read_b32 v219, a237\n"
    "v_accvgpr_read_b32 v220, a226\n"
    "s_waitcnt lgkmcnt(2)\n"
    "v_mfma_f32_16x16x32_bf16 v[84:87], v[244:247], v[228:231], v[84:87]\n"
    "ds_read_b64_tr_b16 v[252:253], v12 offset:46560\n"
    "v_accvgpr_read_b32 v221, a230\n"
    "v_mfma_f32_16x16x32_bf16 v[88:91], v[244:247], v[232:235], v[88:91]\n"
    "ds_read_b64_tr_b16 v[254:255], v12 offset:46816\n"
    "v_accvgpr_read_b32 v222, a234\n"
    "v_mfma_f32_16x16x32_bf16 v[92:95], v[244:247], v[236:239], v[92:95]\n"
    "buffer_load_dwordx4 a[60:63], v14, s[12:15], s11 offen offset:768 sc1\n"
    "v_accvgpr_read_b32 v223, a238\n"
    "v_mfma_f32_16x16x32_bf16 v[96:99], v[244:247], v[240:243], v[96:99]\n"
    "v_accvgpr_read_b32 v224, a227\n"
    "v_accvgpr_read_b32 v225, a231\n"
    "s_waitcnt lgkmcnt(2)\n"
    "v_mfma_f32_16x16x32_bf16 v[100:103], v[248:251], v[228:231], v[100:103]\n"
    "ds_read_b64_tr_b16 v[244:245], v5 offset:46560\n"
    "v_accvgpr_read_b32 v226, a235\n"
    "v_mfma_f32_16x16x32_bf16 v[104:107], v[248:251], v[232:235], v[104:107]\n"
    "ds_read_b64_tr_b16 v[246:247], v5 offset:46816\n"
    "v_accvgpr_read_b32 v227, a239\n"
    "v_mfma_f32_16x16x32_bf16 v[108:111], v[248:251], v[236:239], v[108:111]\n"
    "s_add_u32 s11, s40, 10240\n"
    "buffer_load_dwordx4 a[64:67], v13, s[12:15], s11 offen offset:0 sc1\n"
    "v_mfma_f32_16x16x32_bf16 v[112:115], v[248:251], v[240:243], v[112:115]\n"
    "s_waitcnt lgkmcnt(2)\n"
    "v_mfma_f32_16x16x32_bf16 v[116:119], v[252:255], v[228:231], v[116:119]\n"
    "ds_read_b64_tr_b16 v[248:249], v12 offset:16896\n"
    "v_mfma_f32_16x16x32_bf16 v[120:123], v[252:255], v[232:235], v[120:123]\n"
    "ds_read_b64_tr_b16 v[250:251], v12 offset:17152\n"
    "v_mfma_f32_16x16x32_bf16 v[124:127], v[252:255], v[236:239], v[124:127]\n"
    "buffer_load_dwordx4 a[68:71], v13, s[12:15], s11 offen offset:256 sc1\n"
    "v_mfma_f32_16x16x32_bf16 v[128:131], v[252:255], v[240:243], v[128:131]\n"
    "s_waitcnt lgkmcnt(2)\n"
    "v_mfma_f32_16x16x32_bf16 v[132:135], v[244:247], v[228:231], v[132:135]\n"
    "ds_read_b64_tr_b16 v[252:253], v5 offset:16896\n"
    "v_mfma_f32_16x16x32_bf16 v[136:139], v[244:247], v[232:235], v[136:139]\n"
    "ds_read_b64_tr_b16 v[254:255], v5 offset:17152\n"
    "v_mfma_f32_16x16x32_bf16 v[140:143], v[244:247], v[236:239], v[140:143]\n"
    "v_mfma_f32_16x16x32_bf16 v[144:147], v[244:247], v[240:243], v[144:147]\n"
    "s_waitcnt lgkmcnt(2)\n"
    "v_mfma_f32_16x16x32_bf16 v[20:23], v[248:251], v[212:215], v[20:23]\n"
    "ds_read_b64_tr_b16 v[244:245], v12 offset:16928\n"
    "v_mfma_f32_16x16x32_bf16 v[24:27], v[248:251], v[216:219], v[24:27]\n"
    "ds_read_b64_tr_b16 v[246:247], v12 offset:17184\n"
    "v_mfma_f32_16x16x32_bf16 v[28:31], v[248:251], v[220:223], v[28:31]\n"
    "s_add_u32 s32, s41, 40960\n"
    "buffer_load_dwordx4 a[224:227], v2, s[12:15], s32 offen offset:0 sc1\n"
    "v_mfma_f32_16x16x32_bf16 v[32:35], v[248:251], v[224:227], v[32:35]\n"
    "s_waitcnt lgkmcnt(2)\n"
    "v_mfma_f32_16x16x32_bf16 v[36:39], v[252:255], v[212:215], v[36:39]\n"
    "ds_read_b64_tr_b16 v[248:249], v5 offset:16928\n"
    "v_mfma_f32_16x16x32_bf16 v[40:43], v[252:255], v[216:219], v[40:43]\n"
    "ds_read_b64_tr_b16 v[250:251], v5 offset:17184\n"
    "v_mfma_f32_16x16x32_bf16 v[44:47], v[252:255], v[220:223], v[44:47]\n"
    "buffer_load_dwordx4 a[228:231], v2, s[12:15], s32 offen offset:256 sc1\n"
    "v_mfma_f32_16x16x32_bf16 v[48:51], v[252:255], v[224:227], v[48:51]\n"
    "s_waitcnt lgkmcnt(2)\n"
    "v_mfma_f32_16x16x32_bf16 v[52:55], v[244:247], v[212:215], v[52:55]\n"
    "ds_read_b64_tr_b16 v[252:253], v12 offset:16960\n"
    "s_waitcnt vmcnt(33)\n"
    "v_mfma_f32_16x16x32_bf16 v[56:59], v[244:247], v[216:219], v[56:59]\n"
    "ds_read_b64_tr_b16 v[254:255], v12 offset:17216\n"
    "v_accvgpr_read_b32 v228, a240\n"
    "v_mfma_f32_16x16x32_bf16 v[60:63], v[244:247], v[220:223], v[60:63]\n"
    "buffer_load_dwordx4 a[232:235], v2, s[12:15], s32 offen offset:512 sc1\n"
    "v_accvgpr_read_b32 v229, a244\n"
    "v_mfma_f32_16x16x32_bf16 v[64:67], v[244:247], v[224:227], v[64:67]\n"
    "v_accvgpr_read_b32 v230, a248\n"
    "v_accvgpr_read_b32 v231, a252\n"
    "s_waitcnt lgkmcnt(2)\n"
    "v_mfma_f32_16x16x32_bf16 v[68:71], v[248:251], v[212:215], v[68:71]\n"
    "ds_read_b64_tr_b16 v[244:245], v5 offset:16960\n"
    "v_accvgpr_read_b32 v232, a241\n"
    "v_mfma_f32_16x16x32_bf16 v[72:75], v[248:251], v[216:219], v[72:75]\n"
    "ds_read_b64_tr_b16 v[246:247], v5 offset:17216\n"
    "v_accvgpr_read_b32 v233, a245\n"
    "v_mfma_f32_16x16x32_bf16 v[76:79], v[248:251], v[220:223], v[76:79]\n"
    "buffer_load_dwordx4 a[236:239], v2, s[12:15], s32 offen offset:768 sc1\n"
    "v_accvgpr_read_b32 v234, a249\n"
    "v_mfma_f32_16x16x32_bf16 v[80:83], v[248:251], v[224:227], v[80:83]\n"
    "v_accvgpr_read_b32 v235, a253\n"
    "v_accvgpr_read_b32 v236, a242\n"
    "s_waitcnt lgkmcnt(2)\n"
    "v_mfma_f32_16x16x32_bf16 v[84:87], v[252:255], v[212:215], v[84:87]\n"
    "ds_read_b64_tr_b16 v[248:249], v12 offset:16992\n"
    "v_accvgpr_read_b32 v237, a246\n"
    "v_mfma_f32_16x16x32_bf16 v[88:91], v[252:255], v[216:219], v[88:91]\n"
    "ds_read_b64_tr_b16 v[250:251], v12 offset:17248\n"
    "v_accvgpr_read_b32 v238, a250\n"
    "v_mfma_f32_16x16x32_bf16 v[92:95], v[252:255], v[220:223], v[92:95]\n"
    "buffer_load_dwordx4 a[72:75], v13, s[12:15], s11 offen offset:512 sc1\n"
    "v_accvgpr_read_b32 v239, a254\n"
    "v_mfma_f32_16x16x32_bf16 v[96:99], v[252:255], v[224:227], v[96:99]\n"
    "v_accvgpr_read_b32 v240, a243\n"
    "v_accvgpr_read_b32 v241, a247\n"
    "s_waitcnt lgkmcnt(2)\n"
    "v_mfma_f32_16x16x32_bf16 v[100:103], v[244:247], v[212:215], v[100:103]\n"
    "ds_read_b64_tr_b16 v[252:253], v5 offset:16992\n"
    "v_accvgpr_read_b32 v242, a251\n"
    "v_mfma_f32_16x16x32_bf16 v[104:107], v[244:247], v[216:219], v[104:107]\n"
    "ds_read_b64_tr_b16 v[254:255], v5 offset:17248\n"
    "v_accvgpr_read_b32 v243, a255\n"
    "v_mfma_f32_16x16x32_bf16 v[108:111], v[244:247], v[220:223], v[108:111]\n"
    "buffer_load_dwordx4 a[76:79], v13, s[12:15], s11 offen offset:768 sc1\n"
    "v_mfma_f32_16x16x32_bf16 v[112:115], v[244:247], v[224:227], v[112:115]\n"
    "s_waitcnt lgkmcnt(2)\n"
    "v_mfma_f32_16x16x32_bf16 v[116:119], v[248:251], v[212:215], v[116:119]\n"
    "ds_read_b64_tr_b16 v[244:245], v12 offset:21120\n"
    "v_mfma_f32_16x16x32_bf16 v[120:123], v[248:251], v[216:219], v[120:123]\n"
    "ds_read_b64_tr_b16 v[246:247], v12 offset:21376\n"
    "v_mfma_f32_16x16x32_bf16 v[124:127], v[248:251], v[220:223], v[124:127]\n"
    "s_add_u32 s11, s40, 26624\n"
    "buffer_load_dwordx4 a[80:83], v13, s[12:15], s11 offen offset:0 sc1\n"
    "v_mfma_f32_16x16x32_bf16 v[128:131], v[248:251], v[224:227], v[128:131]\n"
    "s_waitcnt lgkmcnt(2)\n"
    "v_mfma_f32_16x16x32_bf16 v[132:135], v[252:255], v[212:215], v[132:135]\n"
    "ds_read_b64_tr_b16 v[248:249], v5 offset:21120\n"
    "v_mfma_f32_16x16x32_bf16 v[136:139], v[252:255], v[216:219], v[136:139]\n"
    "ds_read_b64_tr_b16 v[250:251], v5 offset:21376\n"
    "v_mfma_f32_16x16x32_bf16 v[140:143], v[252:255], v[220:223], v[140:143]\n"
    "v_mfma_f32_16x16x32_bf16 v[144:147], v[252:255], v[224:227], v[144:147]\n"
    "s_waitcnt lgkmcnt(2)\n"
    "v_mfma_f32_16x16x32_bf16 v[20:23], v[244:247], v[228:231], v[20:23]\n"
    "ds_read_b64_tr_b16 v[252:253], v12 offset:21152\n"
    "v_mfma_f32_16x16x32_bf16 v[24:27], v[244:247], v[232:235], v[24:27]\n"
    "ds_read_b64_tr_b16 v[254:255], v12 offset:21408\n"
    "v_mfma_f32_16x16x32_bf16 v[28:31], v[244:247], v[236:239], v[28:31]\n"
    "buffer_load_dword v6, off, s[12:15], s62 sc1\n"
    "v_mfma_f32_16x16x32_bf16 v[32:35], v[244:247], v[240:243], v[32:35]\n"
    "s_waitcnt lgkmcnt(2)\n"
    "v_mfma_f32_16x16x32_bf16 v[36:39], v[248:251], v[228:231], v[36:39]\n"
    "ds_read_b64_tr_b16 v[244:245], v5 offset:21152\n"
    "v_mfma_f32_16x16x32_bf16 v[40:43], v[248:251], v[232:235], v[40:43]\n"
    "ds_read_b64_tr_b16 v[246:247], v5 offset:21408\n"
    "s_add_u32 s32, s41, 45056\n"
    "buffer_load_dwordx4 a[240:243], v2, s[12:15], s32 offen offset:0 sc1\n"
    "v_mfma_f32_16x16x32_bf16 v[44:47], v[248:251], v[236:239], v[44:47]\n"
    "v_mfma_f32_16x16x32_bf16 v[48:51], v[248:251], v[240:243], v[48:51]\n"
    "s_waitcnt lgkmcnt(2)\n"
    "v_mfma_f32_16x16x32_bf16 v[52:55], v[252:255], v[228:231], v[52:55]\n"
    "ds_read_b64_tr_b16 v[248:249], v12 offset:21184\n"
    "s_waitcnt vmcnt(33)\n"
    "v_mfma_f32_16x16x32_bf16 v[56:59], v[252:255], v[232:235], v[56:59]\n"
    "ds_read_b64_tr_b16 v[250:251], v12 offset:21440\n"
    "buffer_load_dwordx4 a[244:247], v2, s[12:15], s32 offen offset:256 sc1\n"
    "v_mfma_f32_16x16x32_bf16 v[60:63], v[252:255], v[236:239], v[60:63]\n"
    "v_mov_b32 v212, v148\n"
    "v_mov_b32 v213, v152\n"
    "v_mfma_f32_16x16x32_bf16 v[64:67], v[252:255], v[240:243], v[64:67]\n"
    "v_mov_b32 v214, v156\n"
    "v_mov_b32 v215, v160\n"
    "s_waitcnt lgkmcnt(2)\n"
    "v_mfma_f32_16x16x32_bf16 v[68:71], v[244:247], v[228:231], v[68:71]\n"
    "ds_read_b64_tr_b16 v[252:253], v5 offset:21184\n"
    "buffer_load_dwordx4 a[248:251], v2, s[12:15], s32 offen offset:512 sc1\n"
    "v_mfma_f32_16x16x32_bf16 v[72:75], v[244:247], v[232:235], v[72:75]\n"
    "ds_read_b64_tr_b16 v[254:255], v5 offset:21440\n"
    "v_mov_b32 v216, v149\n"
    "v_mfma_f32_16x16x32_bf16 v[76:79], v[244:247], v[236:239], v[76:79]\n"
    "v_mov_b32 v217, v153\n"
    "v_mov_b32 v218, v157\n"
    "v_mfma_f32_16x16x32_bf16 v[80:83], v[244:247], v[240:243], v[80:83]\n"
    "v_mov_b32 v219, v161\n"
    "v_mov_b32 v220, v150\n"
    "s_waitcnt lgkmcnt(2)\n"
    "v_mfma_f32_16x16x32_bf16 v[84:87], v[248:251], v[228:231], v[84:87]\n"
    "ds_read_b64_tr_b16 v[244:245], v12 offset:21216\n"
    "buffer_load_dwordx4 a[252:255], v2, s[12:15], s32 offen offset:768 sc1\n"
    "v_mfma_f32_16x16x32_bf16 v[88:91], v[248:251], v[232:235], v[88:91]\n"
    "ds_read_b64_tr_b16 v[246:247], v12 offset:21472\n"
    "v_mov_b32 v221, v154\n"
    "v_mfma_f32_16x16x32_bf16 v[92:95], v[248:251], v[236:239], v[92:95]\n"
    "v_mov_b32 v222, v158\n"
    "v_mov_b32 v223, v162\n"
    "v_mfma_f32_16x16x32_bf16 v[96:99], v[248:251], v[240:243], v[96:99]\n"
    "v_mov_b32 v224, v151\n"
    "v_mov_b32 v225, v155\n"
    "s_waitcnt lgkmcnt(2)\n"
    "v_mfma_f32_16x16x32_bf16 v[100:103], v[252:255], v[228:231], v[100:103]\n"
    "ds_read_b64_tr_b16 v[248:249], v5 offset:21216\n"
    "buffer_load_dwordx4 a[84:87], v13, s[12:15], s11 offen offset:256 sc1\n"
    "v_mfma_f32_16x16x32_bf16 v[104:107], v[252:255], v[232:235], v[104:107]\n"
    "ds_read_b64_tr_b16 v[250:251], v5 offset:21472\n"
    "v_mov_b32 v226, v159\n"
    "v_mfma_f32_16x16x32_bf16 v[108:111], v[252:255], v[236:239], v[108:111]\n"
    "v_mov_b32 v227, v163\n"
    "v_mfma_f32_16x16x32_bf16 v[112:115], v[252:255], v[240:243], v[112:115]\n"
    "buffer_load_dwordx4 a[88:91], v13, s[12:15], s11 offen offset:512 sc1\n"
    "s_waitcnt lgkmcnt(2)\n"
    "v_mfma_f32_16x16x32_bf16 v[116:119], v[244:247], v[228:231], v[116:119]\n"
    "ds_read_b64_tr_b16 v[252:253], v12 offset:63360\n"
    "v_mfma_f32_16x16x32_bf16 v[120:123], v[244:247], v[232:235], v[120:123]\n"
    "ds_read_b64_tr_b16 v[254:255], v12 offset:63616\n"
    "v_mfma_f32_16x16x32_bf16 v[124:127], v[244:247], v[236:239], v[124:127]\n"
    "v_mfma_f32_16x16x32_bf16 v[128:131], v[244:247], v[240:243], v[128:131]\n"
    "buffer_load_dwordx4 a[92:95], v13, s[12:15], s11 offen offset:768 sc1\n"
    "s_waitcnt lgkmcnt(2)\n"
    "v_mfma_f32_16x16x32_bf16 v[132:135], v[248:251], v[228:231], v[132:135]\n"
    "ds_read_b64_tr_b16 v[244:245], v5 offset:63360\n"
    "v_mfma_f32_16x16x32_bf16 v[136:139], v[248:251], v[232:235], v[136:139]\n"
    "ds_read_b64_tr_b16 v[246:247], v5 offset:63616\n"
    "v_mfma_f32_16x16x32_bf16 v[140:143], v[248:251], v[236:239], v[140:143]\n"
    "v_mfma_f32_16x16x32_bf16 v[144:147], v[248:251], v[240:243], v[144:147]\n"
    "s_waitcnt lgkmcnt(2)\n"
    "v_mfma_f32_16x16x32_bf16 v[20:23], v[252:255], v[212:215], v[20:23]\n"
    "ds_read_b64_tr_b16 v[248:249], v12 offset:63392\n"
    "v_mfma_f32_16x16x32_bf16 v[24:27], v[252:255], v[216:219], v[24:27]\n"
    "ds_read_b64_tr_b16 v[250:251], v12 offset:63648\n"
    "v_mfma_f32_16x16x32_bf16 v[28:31], v[252:255], v[220:223], v[28:31]\n"
    "s_add_u32 s11, s40, 43008\n"
    "buffer_load_dwordx4 a[96:99], v13, s[12:15], s11 offen offset:0 sc1\n"
    "v_mfma_f32_16x16x32_bf16 v[32:35], v[252:255], v[224:227], v[32:35]\n"
    "s_waitcnt lgkmcnt(2)\n"
    "v_mfma_f32_16x16x32_bf16 v[36:39], v[244:247], v[212:215], v[36:39]\n"
    "ds_read_b64_tr_b16 v[252:253], v5 offset:63392\n"
    "v_mfma_f32_16x16x32_bf16 v[40:43], v[244:247], v[216:219], v[40:43]\n"
    "ds_read_b64_tr_b16 v[254:255], v5 offset:63648\n"
    "v_mfma_f32_16x16x32_bf16 v[44:47], v[244:247], v[220:223], v[44:47]\n"
    "buffer_load_dwordx4 a[100:103], v13, s[12:15], s11 offen offset:256 sc1\n"
    "v_mfma_f32_16x16x32_bf16 v[48:51], v[244:247], v[224:227], v[48:51]\n"
    "s_waitcnt lgkmcnt(2)\n"
    "v_mfma_f32_16x16x32_bf16 v[52:55], v[248:251], v[212:215], v[52:55]\n"
    "ds_read_b64_tr_b16 v[244:245], v12 offset:63424\n"
    "s_waitcnt vmcnt(34)\n"
    "v_mfma_f32_16x16x32_bf16 v[56:59], v[248:251], v[216:219], v[56:59]\n"
    "ds_read_b64_tr_b16 v[246:247], v12 offset:63680\n"
    "v_mov_b32 v228, v164\n"
    "v_mfma_f32_16x16x32_bf16 v[60:63], v[248:251], v[220:223], v[60:63]\n"
    "v_mov_b32 v229, v168\n"
    "v_mov_b32 v230, v172\n"
    "v_mfma_f32_16x16x32_bf16 v[64:67], v[248:251], v[224:227], v[64:67]\n"
    "buffer_load_dwordx4 a[104:107], v13, s[12:15], s11 offen offset:512 sc1\n"
    "v_mov_b32 v231, v176\n"
    "s_waitcnt lgkmcnt(2)\n"
    "v_mfma_f32_16x16x32_bf16 v[68:71], v[252:255], v[212:215], v[68:71]\n"
    "ds_read_b64_tr_b16 v[248:249], v5 offset:63424\n"
    "v_mov_b32 v232, v165\n"
    "v_mfma_f32_16x16x32_bf16 v[72:75], v[252:255], v[216:219], v[72:75]\n"
    "ds_read_b64_tr_b16 v[250:251], v5 offset:63680\n"
    "v_mov_b32 v233, v169\n"
    "v_mfma_f32_16x16x32_bf16 v[76:79], v[252:255], v[220:223], v[76:79]\n"
    "v_mov_b32 v234, v173\n"
    "v_mov_b32 v235, v177\n"
    "v_mfma_f32_16x16x32_bf16 v[80:83], v[252:255], v[224:227], v[80:83]\n"
    "v_mov_b32 v236, v166\n"
    "v_mov_b32 v237, v170\n"
    "s_waitcnt lgkmcnt(2)\n"
    "v_mfma_f32_16x16x32_bf16 v[84:87], v[244:247], v[212:215], v[84:87]\n"
    "ds_read_b64_tr_b16 v[252:253], v12 offset:63456\n"
    "buffer_load_dwordx4 a[108:111], v13, s[12:15], s11 offen offset:768 sc1\n"
    "v_mfma_f32_16x16x32_bf16 v[88:91], v[244:247], v[216:219], v[88:91]\n"
    "ds_read_b64_tr_b16 v[254:255], v12 offset:63712\n"
    "v_mov_b32 v238, v174\n"
    "v_mfma_f32_16x16x32_bf16 v[92:95], v[244:247], v[220:223], v[92:95]\n"
    "v_mov_b32 v239, v178\n"
    "v_mov_b32 v240, v167\n"
    "v_mfma_f32_16x16x32_bf16 v[96:99], v[244:247], v[224:227], v[96:99]\n"
    "v_mov_b32 v241, v171\n"
    "v_mov_b32 v242, v175\n"
    "s_waitcnt lgkmcnt(2)\n"
    "v_mfma_f32_16x16x32_bf16 v[100:103], v[248:251], v[212:215], v[100:103]\n"
    "ds_read_b64_tr_b16 v[244:245], v5 offset:63456\n"
    "v_mov_b32 v243, v179\n"
    "v_mfma_f32_16x16x32_bf16 v[104:107], v[248:251], v[216:219], v[104:107]\n"
    "ds_read_b64_tr_b16 v[246:247], v5 offset:63712\n"
    "s_add_u32 s11, s40, 4096\n"
    "buffer_load_dwordx4 a[112:115], v13, s[12:15], s11 offen offset:0 sc1\n"
    "v_mfma_f32_16x16x32_bf16 v[108:111], v[248:251], v[220:223], v[108:111]\n"
    "v_mfma_f32_16x16x32_bf16 v[112:115], v[248:251], v[224:227], v[112:115]\n"
    "s_waitcnt lgkmcnt(2)\n"
    "v_mfma_f32_16x16x32_bf16 v[116:119], v[252:255], v[212:215], v[116:119]\n"
    "ds_read_b64_tr_b16 v[248:249], v12 offset:12672\n"
    "v_mfma_f32_16x16x32_bf16 v[120:123], v[252:255], v[216:219], v[120:123]\n"
    "ds_read_b64_tr_b16 v[250:251], v12 offset:12928\n"
    "v_mfma_f32_16x16x32_bf16 v[124:127], v[252:255], v[220:223], v[124:127]\n"
    "buffer_load_dwordx4 a[116:119], v13, s[12:15], s11 offen offset:256 sc1\n"
    "v_mfma_f32_16x16x32_bf16 v[128:131], v[252:255], v[224:227], v[128:131]\n"
    "s_waitcnt lgkmcnt(2)\n"
    "v_mfma_f32_16x16x32_bf16 v[132:135], v[244:247], v[212:215], v[132:135]\n"
    "ds_read_b64_tr_b16 v[252:253], v5 offset:12672\n"
    "v_mfma_f32_16x16x32_bf16 v[136:139], v[244:247], v[216:219], v[136:139]\n"
    "ds_read_b64_tr_b16 v[254:255], v5 offset:12928\n"
    "v_mfma_f32_16x16x32_bf16 v[140:143], v[244:247], v[220:223], v[140:143]\n"
    "v_mfma_f32_16x16x32_bf16 v[144:147], v[244:247], v[224:227], v[144:147]\n"
    "s_waitcnt lgkmcnt(2)\n"
    "v_mfma_f32_16x16x32_bf16 v[20:23], v[248:251], v[228:231], v[20:23]\n"
    "ds_read_b64_tr_b16 v[244:245], v12 offset:12704\n"
    "s_waitcnt vmcnt(31)\n"
    "v_mfma_f32_16x16x32_bf16 v[24:27], v[248:251], v[232:235], v[24:27]\n"
    "ds_read_b64_tr_b16 v[246:247], v12 offset:12960\n"
    "v_mov_b32 v212, v180\n"
    "v_mfma_f32_16x16x32_bf16 v[28:31], v[248:251], v[236:239], v[28:31]\n"
    "buffer_load_dwordx4 a[120:123], v13, s[12:15], s11 offen offset:512 sc1\n"
    "v_mov_b32 v213, v184\n"
    "v_mfma_f32_16x16x32_bf16 v[32:35], v[248:251], v[240:243], v[32:35]\n"
    "v_mov_b32 v214, v188\n"
    "v_mov_b32 v215, v192\n"
    "s_waitcnt lgkmcnt(2)\n"
    "v_mfma_f32_16x16x32_bf16 v[36:39], v[252:255], v[228:231], v[36:39]\n"
    "ds_read_b64_tr_b16 v[248:249], v5 offset:12704\n"
    "v_mov_b32 v216, v181\n"
    "v_mfma_f32_16x16x32_bf16 v[40:43], v[252:255], v[232:235], v[40:43]\n"
    "ds_read_b64_tr_b16 v[250:251], v5 offset:12960\n"
    "v_mov_b32 v217, v185\n"
    "v_mfma_f32_16x16x32_bf16 v[44:47], v[252:255], v[236:239], v[44:47]\n"
    "v_mov_b32 v218, v189\n"
    "v_mov_b32 v219, v193\n"
    "v_mfma_f32_16x16x32_bf16 v[48:51], v[252:255], v[240:243], v[48:51]\n"
    "v_mov_b32 v220, v182\n"
    "v_mov_b32 v221, v186\n"
    "s_waitcnt lgkmcnt(2)\n"
    "v_mfma_f32_16x16x32_bf16 v[52:55], v[244:247], v[228:231], v[52:55]\n"
    "ds_read_b64_tr_b16 v[252:253], v12 offset:12736\n"
    "v_mov_b32 v222, v190\n"
    "v_mfma_f32_16x16x32_bf16 v[56:59], v[244:247], v[232:235], v[56:59]\n"
    "ds_read_b64_tr_b16 v[254:255], v12 offset:12992\n"
    "buffer_load_dwordx4 a[124:127], v13, s[12:15], s11 offen offset:768 sc1\n"
    "v_mfma_f32_16x16x32_bf16 v[60:63], v[244:247], v[236:239], v[60:63]\n"
    "v_mov_b32 v223, v194\n"
    "v_mov_b32 v224, v183\n"
    "v_mfma_f32_16x16x32_bf16 v[64:67], v[244:247], v[240:243], v[64:67]\n"
    "v_mov_b32 v225, v187\n"
    "v_mov_b32 v226, v191\n"
    "s_waitcnt lgkmcnt(2)\n"
    "v_mfma_f32_16x16x32_bf16 v[68:71], v[248:251], v[228:231], v[68:71]\n"
    "ds_read_b64_tr_b16 v[244:245], v5 offset:12736\n"
    "v_mov_b32 v227, v195\n"
    "v_mfma_f32_16x16x32_bf16 v[72:75], v[248:251], v[232:235], v[72:75]\n"
    "ds_read_b64_tr_b16 v[246:247], v5 offset:12992\n"
    "v_mfma_f32_16x16x32_bf16 v[76:79], v[248:251], v[236:239], v[76:79]\n"
    "v_mfma_f32_16x16x32_bf16 v[80:83], v[248:251], v[240:243], v[80:83]\n"
    "s_waitcnt lgkmcnt(2)\n"
    "v_mfma_f32_16x16x32_bf16 v[84:87], v[252:255], v[228:231], v[84:87]\n"
    "ds_read_b64_tr_b16 v[248:249], v12 offset:12768\n"
    "s_add_u32 s11, s40, 20480\n"
    "buffer_load_dwordx4 a[128:131], v13, s[12:15], s11 offen offset:0 sc1\n"
    "v_mfma_f32_16x16x32_bf16 v[88:91], v[252:255], v[232:235], v[88:91]\n"
    "ds_read_b64_tr_b16 v[250:251], v12 offset:13024\n"
    "v_mfma_f32_16x16x32_bf16 v[92:95], v[252:255], v[236:239], v[92:95]\n"
    "v_mfma_f32_16x16x32_bf16 v[96:99], v[252:255], v[240:243], v[96:99]\n"
    "s_waitcnt lgkmcnt(2)\n"
    "v_mfma_f32_16x16x32_bf16 v[100:103], v[244:247], v[228:231], v[100:103]\n"
    "ds_read_b64_tr_b16 v[252:253], v5 offset:12768\n"
    "v_mfma_f32_16x16x32_bf16 v[104:107], v[244:247], v[232:235], v[104:107]\n"
    "ds_read_b64_tr_b16 v[254:255], v5 offset:13024\n"
    "v_mfma_f32_16x16x32_bf16 v[108:111], v[244:247], v[236:239], v[108:111]\n"
    "v_mfma_f32_16x16x32_bf16 v[112:115], v[244:247], v[240:243], v[112:115]\n"
    "buffer_load_dwordx4 a[132:135], v13, s[12:15], s11 offen offset:256 sc1\n"
    "s_waitcnt lgkmcnt(2)\n"
    "v_mfma_f32_16x16x32_bf16 v[116:119], v[248:251], v[228:231], v[116:119]\n"
    "ds_read_b64_tr_b16 v[244:245], v12 offset:54912\n"
    "v_mfma_f32_16x16x32_bf16 v[120:123], v[248:251], v[232:235], v[120:123]\n"
    "ds_read_b64_tr_b16 v[246:247], v12 offset:55168\n"
    "v_mfma_f32_16x16x32_bf16 v[124:127], v[248:251], v[236:239], v[124:127]\n"
    "v_mfma_f32_16x16x32_bf16 v[128:131], v[248:251], v[240:243], v[128:131]\n"
    "s_waitcnt lgkmcnt(2)\n"
    "v_mfma_f32_16x16x32_bf16 v[132:135], v[252:255], v[228:231], v[132:135]\n"
    "ds_read_b64_tr_b16 v[248:249], v5 offset:54912\n"
    "v_mfma_f32_16x16x32_bf16 v[136:139], v[252:255], v[232:235], v[136:139]\n"
    "ds_read_b64_tr_b16 v[250:251], v5 offset:55168\n"
    "v_mfma_f32_16x16x32_bf16 v[140:143], v[252:255], v[236:239], v[140:143]\n"
    "v_mfma_f32_16x16x32_bf16 v[144:147], v[252:255], v[240:243], v[144:147]\n"
    "s_waitcnt lgkmcnt(2)\n"
    "v_mfma_f32_16x16x32_bf16 v[20:23], v[244:247], v[212:215], v[20:23]\n"
    "ds_read_b64_tr_b16 v[252:253], v12 offset:54944\n"
    "s_waitcnt vmcnt(17)\n"
    "v_mfma_f32_16x16x32_bf16 v[24:27], v[244:247], v[216:219], v[24:27]\n"
    "ds_read_b64_tr_b16 v[254:255], v12 offset:55200\n"
    "v_cmp_ne_u32 vcc, s7, v6\n"
    "v_mfma_f32_16x16x32_bf16 v[28:31], v[244:247], v[220:223], v[28:31]\n"
    "buffer_load_dword v7, off, s[12:15], s57 sc1\n"
    "v_mfma_f32_16x16x32_bf16 v[32:35], v[244:247], v[224:227], v[32:35]\n"
    "s_waitcnt lgkmcnt(2)\n"
    "v_mfma_f32_16x16x32_bf16 v[36:39], v[248:251], v[212:215], v[36:39]\n"
    "ds_read_b64_tr_b16 v[244:245], v5 offset:54944\n"
    "s_cbranch_vccnz Lk_f1f_22\n"
    "Lk_f1r_23:\n"
    "v_mfma_f32_16x16x32_bf16 v[40:43], v[248:251], v[216:219], v[40:43]\n"
    "ds_read_b64_tr_b16 v[246:247], v5 offset:55200\n"
    "buffer_load_dwordx4 v[148:151], v10, s[12:15], s45 offen offset:0 sc1\n"
    "v_mfma_f32_16x16x32_bf16 v[44:47], v[248:251], v[220:223], v[44:47]\n"
    "buffer_load_dwordx4 a[136:139], v13, s[12:15], s11 offen offset:512 sc1\n"
    "buffer_load_dwordx4 v[152:155], v10, s[12:15], s45 offen offset:16 sc1\n"
    "v_mfma_f32_16x16x32_bf16 v[48:51], v[248:251], v[224:227], v[48:51]\n"
    "buffer_load_dwordx4 v[156:159], v10, s[12:15], s45 offen offset:32 sc1\n"
    "buffer_load_dwordx4 v[160:163], v10, s[12:15], s45 offen offset:48 sc1\n"
    "s_waitcnt lgkmcnt(2)\n"
    "v_mfma_f32_16x16x32_bf16 v[52:55], v[252:255], v[212:215], v[52:55]\n"
    "ds_read_b64_tr_b16 v[248:249], v12 offset:54976\n"
    "v_mfma_f32_16x16x32_bf16 v[56:59], v[252:255], v[216:219], v[56:59]\n"
    "ds_read_b64_tr_b16 v[250:251], v12 offset:55232\n"
    "v_mov_b32 v228, v196\n"
    "v_mfma_f32_16x16x32_bf16 v[60:63], v[252:255], v[220:223], v[60:63]\n"
    "buffer_load_dwordx4 a[140:143], v13, s[12:15], s11 offen offset:768 sc1\n"
    "v_mov_b32 v229, v200\n"
    "v_mfma_f32_16x16x32_bf16 v[64:67], v[252:255], v[224:227], v[64:67]\n"
    "v_mov_b32 v230, v204\n"
    "v_mov_b32 v231, v208\n"
    "s_waitcnt lgkmcnt(2)\n"
    "v_mfma_f32_16x16x32_bf16 v[68:71], v[244:247], v[212:215], v[68:71]\n"
    "ds_read_b64_tr_b16 v[252:253], v5 offset:54976\n"
    "v_mov_b32 v232, v197\n"
    "v_mfma_f32_16x16x32_bf16 v[72:75], v[244:247], v[216:219], v[72:75]\n"
    "ds_read_b64_tr_b16 v[254:255], v5 offset:55232\n"
    "v_mov_b32 v233, v201\n"
    "v_mfma_f32_16x16x32_bf16 v[76:79], v[244:247], v[220:223], v[76:79]\n"
    "s_add_u32 s11, s40, 36864\n"
    "buffer_load_dwordx4 a[144:147], v13, s[12:15], s11 offen offset:0 sc1\n"
    "v_mov_b32 v234, v205\n"
    "v_mfma_f32_16x16x32_bf16 v[80:83], v[244:247], v[224:227], v[80:83]\n"
    "v_mov_b32 v235, v209\n"
    "v_mov_b32 v236, v198\n"
    "s_waitcnt lgkmcnt(2)\n"
    "v_mfma_f32_16x16x32_bf16 v[84:87], v[248:251], v[212:215], v[84:87]\n"
    "ds_read_b64_tr_b16 v[244:245], v12 offset:55008\n"
    "v_mov_b32 v237, v202\n"
    "v_mfma_f32_16x16x32_bf16 v[88:91], v[248:251], v[216:219], v[88:91]\n"
    "ds_read_b64_tr_b16 v[246:247], v12 offset:55264\n"
    "v_mov_b32 v238, v206\n"
    "v_mfma_f32_16x16x32_bf16 v[92:95], v[248:251], v[220:223], v[92:95]\n"
    "buffer_load_dwordx4 a[148:151], v13, s[12:15], s11 offen offset:256 sc1\n"
    "v_mov_b32 v239, v210\n"
    "v_mfma_f32_16x16x32_bf16 v[96:99], v[248:251], v[224:227], v[96:99]\n"
    "v_mov_b32 v240, v199\n"
    "v_mov_b32 v241, v203\n"
    "s_waitcnt lgkmcnt(2)\n"
    "v_mfma_f32_16x16x32_bf16 v[100:103], v[252:255], v[212:215], v[100:103]\n"
    "ds_read_b64_tr_b16 v[248:249], v5 offset:55008\n"
    "v_mov_b32 v242, v207\n"
    "v_mfma_f32_16x16x32_bf16 v[104:107], v[252:255], v[216:219], v[104:107]\n"
    "ds_read_b64_tr_b16 v[250:251], v5 offset:55264\n"
    "v_mov_b32 v243, v211\n"
    "v_mfma_f32_16x16x32_bf16 v[108:111], v[252:255], v[220:223], v[108:111]\n"
    "buffer_load_dwordx4 a[152:155], v13, s[12:15], s11 offen offset:512 sc1\n"
    "v_mfma_f32_16x16x32_bf16 v[112:115], v[252:255], v[224:227], v[112:115]\n"
    "s_waitcnt lgkmcnt(2)\n"
    "v_mfma_f32_16x16x32_bf16 v[116:119], v[244:247], v[212:215], v[116:119]\n"
    "ds_read_b64_tr_b16 v[252:253], v12 offset:59136\n"
    "v_mfma_f32_16x16x32_bf16 v[120:123], v[244:247], v[216:219], v[120:123]\n"
    "ds_read_b64_tr_b16 v[254:255], v12 offset:59392\n"
    "v_mfma_f32_16x16x32_bf16 v[124:127], v[244:247], v[220:223], v[124:127]\n"
    "buffer_load_dwordx4 a[156:159], v13, s[12:15], s11 offen offset:768 sc1\n"
    "v_mfma_f32_16x16x32_bf16 v[128:131], v[244:247], v[224:227], v[128:131]\n"
    "s_waitcnt lgkmcnt(2)\n"
    "v_mfma_f32_16x16x32_bf16 v[132:135], v[248:251], v[212:215], v[132:135]\n"
    "ds_read_b64_tr_b16 v[244:245], v5 offset:59136\n"
    "v_mfma_f32_16x16x32_bf16 v[136:139], v[248:251], v[216:219], v[136:139]\n"
    "ds_read_b64_tr_b16 v[246:247], v5 offset:59392\n"
    "v_mfma_f32_16x16x32_bf16 v[140:143], v[248:251], v[220:223], v[140:143]\n"
    "v_mfma_f32_16x16x32_bf16 v[144:147], v[248:251], v[224:227], v[144:147]\n"
    "s_waitcnt lgkmcnt(2)\n"
    "v_mfma_f32_16x16x32_bf16 v[20:23], v[252:255], v[228:231], v[20:23]\n"
    "ds_read_b64_tr_b16 v[248:249], v12 offset:59168\n"
    "v_mfma_f32_16x16x32_bf16 v[24:27], v[252:255], v[232:235], v[24:27]\n"
    "ds_read_b64_tr_b16 v[250:251], v12 offset:59424\n"
    "v_accvgpr_read_b32 v212, a224\n"
    "v_mfma_f32_16x16x32_bf16 v[28:31], v[252:255], v[236:239], v[28:31]\n"
    "s_add_u32 s11, s40, 6144\n"
    "buffer_load_dwordx4 a[160:163], v14, s[12:15], s11 offen offset:0 sc1\n"
    "v_accvgpr_read_b32 v213, a228\n"
    "v_mfma_f32_16x16x32_bf16 v[32:35], v[252:255], v[240:243], v[32:35]\n"
    "v_accvgpr_read_b32 v214, a232\n"
    "v_accvgpr_read_b32 v215, a236\n"
    "s_waitcnt lgkmcnt(2)\n"
    "v_mfma_f32_16x16x32_bf16 v[36:39], v[244:247], v[228:231], v[36:39]\n"
    "ds_read_b64_tr_b16 v[252:253], v5 offset:59168\n"
    "v_accvgpr_read_b32 v216, a225\n"
    "v_mfma_f32_16x16x32_bf16 v[40:43], v[244:247], v[232:235], v[40:43]\n"
    "ds_read_b64_tr_b16 v[254:255], v5 offset:59424\n"
    "v_accvgpr_read_b32 v217, a229\n"
    "v_mfma_f32_16x16x32_bf16 v[44:47], v[244:247], v[236:239], v[44:47]\n"
    "buffer_load_dwordx4 a[164:167], v14, s[12:15], s11 offen offset:256 sc1\n"
    "v_accvgpr_read_b32 v218, a233\n"
    "v_mfma_f32_16x16x32_bf16 v[48:51], v[244:247], v[240:243], v[48:51]\n"
    "v_accvgpr_read_b32 v219, a237\n"
    "v_accvgpr_read_b32 v220, a226\n"
    "s_waitcnt lgkmcnt(2)\n"
    "v_mfma_f32_16x16x32_bf16 v[52:55], v[248:251], v[228:231], v[52:55]\n"
    "ds_read_b64_tr_b16 v[244:245], v12 offset:59200\n"
    "v_accvgpr_read_b32 v221, a230\n"
    "v_mfma_f32_16x16x32_bf16 v[56:59], v[248:251], v[232:235], v[56:59]\n"
    "ds_read_b64_tr_b16 v[246:247], v12 offset:59456\n"
    "v_accvgpr_read_b32 v222, a234\n"
    "v_mfma_f32_16x16x32_bf16 v[60:63], v[248:251], v[236:239], v[60:63]\n"
    "v_accvgpr_read_b32 v223, a238\n"
    "v_accvgpr_read_b32 v224, a227\n"
    "v_mfma_f32_16x16x32_bf16 v[64:67], v[248:251], v[240:243], v[64:67]\n"
    "buffer_load_dwordx4 a[168:171], v14, s[12:15], s11 offen offset:512 sc1\n"
    "v_accvgpr_read_b32 v225, a231\n"
    "s_waitcnt lgkmcnt(2)\n"
    "v_mfma_f32_16x16x32_bf16 v[68:71], v[252:255], v[228:231], v[68:71]\n"
    "ds_read_b64_tr_b16 v[248:249], v5 offset:59200\n"
    "v_accvgpr_read_b32 v226, a235\n"
    "v_mfma_f32_16x16x32_bf16 v[72:75], v[252:255], v[232:235], v[72:75]\n"
    "ds_read_b64_tr_b16 v[250:251], v5 offset:59456\n"
    "v_accvgpr_read_b32 v227, a239\n"
    "v_mfma_f32_16x16x32_bf16 v[76:79], v[252:255], v[236:239], v[76:79]\n"
    "v_mfma_f32_16x16x32_bf16 v[80:83], v[252:255], v[240:243], v[80:83]\n"
    "s_waitcnt lgkmcnt(2)\n"
    "v_mfma_f32_16x16x32_bf16 v[84:87], v[244:247], v[228:231], v[84:87]\n"
    "ds_read_b64_tr_b16 v[252:253], v12 offset:59232\n"
    "buffer_load_dwordx4 a[172:175], v14, s[12:15], s11 offen offset:768 sc1\n"
    "v_mfma_f32_16x16x32_bf16 v[88:91], v[244:247], v[232:235], v[88:91]\n"
    "ds_read_b64_tr_b16 v[254:255], v12 offset:59488\n"
    "v_mfma_f32_16x16x32_bf16 v[92:95], v[244:247], v[236:239], v[92:95]\n"
    "v_mfma_f32_16x16x32_bf16 v[96:99], v[244:247], v[240:243], v[96:99]\n"
    "s_waitcnt lgkmcnt(2)\n"
    "v_mfma_f32_16x16x32_bf16 v[100:103], v[248:251], v[228:231], v[100:103]\n"
    "ds_read_b64_tr_b16 v[244:245], v5 offset:59232\n"
    "v_mfma_f32_16x16x32_bf16 v[104:107], v[248:251], v[232:235], v[104:107]\n"
    "ds_read_b64_tr_b16 v[246:247], v5 offset:59488\n"
    "s_add_u32 s11, s40, 14336\n"
    "buffer_load_dwordx4 a[176:179], v13, s[12:15], s11 offen offset:0 sc1\n"
    "v_mfma_f32_16x16x32_bf16 v[108:111], v[248:251], v[236:239], v[108:111]\n"
    "v_mfma_f32_16x16x32_bf16 v[112:115], v[248:251], v[240:243], v[112:115]\n"
    "s_waitcnt lgkmcnt(2)\n"
    "v_mfma_f32_16x16x32_bf16 v[116:119], v[252:255], v[228:231], v[116:119]\n"
    "ds_read_b64_tr_b16 v[248:249], v12 offset:50688\n"
    "v_mfma_f32_16x16x32_bf16 v[120:123], v[252:255], v[232:235], v[120:123]\n"
    "ds_read_b64_tr_b16 v[250:251], v12 offset:50944\n"
    "v_mfma_f32_16x16x32_bf16 v[124:127], v[252:255], v[236:239], v[124:127]\n"
    "buffer_load_dwordx4 a[180:183], v13, s[12:15], s11 offen offset:256 sc1\n"
    "v_mfma_f32_16x16x32_bf16 v[128:131], v[252:255], v[240:243], v[128:131]\n"
    "s_waitcnt lgkmcnt(2)\n"
    "v_mfma_f32_16x16x32_bf16 v[132:135], v[244:247], v[228:231], v[132:135]\n"
    "ds_read_b64_tr_b16 v[252:253], v5 offset:50688\n"
    "v_mfma_f32_16x16x32_bf16 v[136:139], v[244:247], v[232:235], v[136:139]\n"
    "ds_read_b64_tr_b16 v[254:255], v5 offset:50944\n"
    "v_mfma_f32_16x16x32_bf16 v[140:143], v[244:247], v[236:239], v[140:143]\n"
    "v_mfma_f32_16x16x32_bf16 v[144:147], v[244:247], v[240:243], v[144:147]\n"
    "s_waitcnt lgkmcnt(2)\n"
    "v_mfma_f32_16x16x32_bf16 v[20:23], v[248:251], v[212:215], v[20:23]\n"
    "ds_read_b64_tr_b16 v[244:245], v12 offset:50720\n"
    "s_waitcnt vmcnt(30)\n"
    "v_mfma_f32_16x16x32_bf16 v[24:27], v[248:251], v[216:219], v[24:27]\n"
    "ds_read_b64_tr_b16 v[246:247], v12 offset:50976\n"
    "v_accvgpr_read_b32 v228, a240\n"
    "v_mfma_f32_16x16x32_bf16 v[28:31], v[248:251], v[220:223], v[28:31]\n"
    "buffer_load_dwordx4 a[184:187], v13, s[12:15], s11 offen offset:512 sc1\n"
    "v_accvgpr_read_b32 v229, a244\n"
    "v_mfma_f32_16x16x32_bf16 v[32:35], v[248:251], v[224:227], v[32:35]\n"
    "v_accvgpr_read_b32 v230, a248\n"
    "v_accvgpr_read_b32 v231, a252\n"
    "s_waitcnt lgkmcnt(2)\n"
    "v_mfma_f32_16x16x32_bf16 v[36:39], v[252:255], v[212:215], v[36:39]\n"
    "ds_read_b64_tr_b16 v[248:249], v5 offset:50720\n"
    "v_accvgpr_read_b32 v232, a241\n"
    "v_mfma_f32_16x16x32_bf16 v[40:43], v[252:255], v[216:219], v[40:43]\n"
    "ds_read_b64_tr_b16 v[250:251], v5 offset:50976\n"
    "v_accvgpr_read_b32 v233, a245\n"
    "v_mfma_f32_16x16x32_bf16 v[44:47], v[252:255], v[220:223], v[44:47]\n"
    "buffer_load_dwordx4 a[188:191], v13, s[12:15], s11 offen offset:768 sc1\n"
    "v_accvgpr_read_b32 v234, a249\n"
    "v_mfma_f32_16x16x32_bf16 v[48:51], v[252:255], v[224:227], v[48:51]\n"
    "v_accvgpr_read_b32 v235, a253\n"
    "v_accvgpr_read_b32 v236, a242\n"
    "s_waitcnt lgkmcnt(2)\n"
    "v_mfma_f32_16x16x32_bf16 v[52:55], v[244:247], v[212:215], v[52:55]\n"
    "ds_read_b64_tr_b16 v[252:253], v12 offset:50752\n"
    "v_accvgpr_read_b32 v237, a246\n"
    "v_mfma_f32_16x16x32_bf16 v[56:59], v[244:247], v[216:219], v[56:59]\n"
    "ds_read_b64_tr_b16 v[254:255], v12 offset:51008\n"
    "v_accvgpr_read_b32 v238, a250\n"
    "v_mfma_f32_16x16x32_bf16 v[60:63], v[244:247], v[220:223], v[60:63]\n"
    "v_accvgpr_read_b32 v239, a254\n"
    "v_accvgpr_read_b32 v240, a243\n"
    "v_mfma_f32_16x16x32_bf16 v[64:67], v[244:247], v[224:227], v[64:67]\n"
    "s_add_u32 s11, s40, 30720\n"
    "buffer_load_dwordx4 a[192:195], v13, s[12:15], s11 offen offset:0 sc1\n"
    "v_accvgpr_read_b32 v241, a247\n"
    "s_waitcnt lgkmcnt(2)\n"
    "v_mfma_f32_16x16x32_bf16 v[68:71], v[248:251], v[212:215], v[68:71]\n"
    "ds_read_b64_tr_b16 v[244:245], v5 offset:50752\n"
    "v_accvgpr_read_b32 v242, a251\n"
    "v_mfma_f32_16x16x32_bf16 v[72:75], v[248:251], v[216:219], v[72:75]\n"
    "ds_read_b64_tr_b16 v[246:247], v5 offset:51008\n"
    "v_accvgpr_read_b32 v243, a255\n"
    "v_mfma_f32_16x16x32_bf16 v[76:79], v[248:251], v[220:223], v[76:79]\n"
    "v_mfma_f32_16x16x32_bf16 v[80:83], v[248:251], v[224:227], v[80:83]\n"
    "s_waitcnt lgkmcnt(2)\n"
    "v_mfma_f32_16x16x32_bf16 v[84:87], v[252:255], v[212:215], v[84:87]\n"
    "ds_read_b64_tr_b16 v[248:249], v12 offset:50784\n"
    "buffer_load_dwordx4 a[196:199], v13, s[12:15], s11 offen offset:256 sc1\n"
    "v_mfma_f32_16x16x32_bf16 v[88:91], v[252:255], v[216:219], v[88:91]\n"
    "ds_read_b64_tr_b16 v[250:251], v12 offset:51040\n"
    "v_mfma_f32_16x16x32_bf16 v[92:95], v[252:255], v[220:223], v[92:95]\n"
    "v_mfma_f32_16x16x32_bf16 v[96:99], v[252:255], v[224:227], v[96:99]\n"
    "s_waitcnt lgkmcnt(2)\n"
    "v_mfma_f32_16x16x32_bf16 v[100:103], v[244:247], v[212:215], v[100:103]\n"
    "ds_read_b64_tr_b16 v[252:253], v5 offset:50784\n"
    "v_mfma_f32_16x16x32_bf16 v[104:107], v[244:247], v[216:219], v[104:107]\n"
    "ds_read_b64_tr_b16 v[254:255], v5 offset:51040\n"
    "buffer_load_dwordx4 a[200:203], v13, s[12:15], s11 offen offset:512 sc1\n"
    "v_mfma_f32_16x16x32_bf16 v[108:111], v[244:247], v[220:223], v[108:111]\n"
    "v_mfma_f32_16x16x32_bf16 v[112:115], v[244:247], v[224:227], v[112:115]\n"
    "s_waitcnt lgkmcnt(2)\n"
    "v_mfma_f32_16x16x32_bf16 v[116:119], v[248:251], v[212:215], v[116:119]\n"
    "ds_read_b64_tr_b16 v[244:245], v12 offset:8448\n"
    "v_mfma_f32_16x16x32_bf16 v[120:123], v[248:251], v[216:219], v[120:123]\n"
    "ds_read_b64_tr_b16 v[246:247], v12 offset:8704\n"
    "v_mfma_f32_16x16x32_bf16 v[124:127], v[248:251], v[220:223], v[124:127]\n"
    "buffer_load_dwordx4 a[204:207], v13, s[12:15], s11 offen offset:768 sc1\n"
    "v_mfma_f32_16x16x32_bf16 v[128:131], v[248:251], v[224:227], v[128:131]\n"
    "s_waitcnt lgkmcnt(2)\n"
    "v_mfma_f32_16x16x32_bf16 v[132:135], v[252:255], v[212:215], v[132:135]\n"
    "ds_read_b64_tr_b16 v[248:249], v5 offset:8448\n"
    "v_mfma_f32_16x16x32_bf16 v[136:139], v[252:255], v[216:219], v[136:139]\n"
    "ds_read_b64_tr_b16 v[250:251], v5 offset:8704\n"
    "v_mfma_f32_16x16x32_bf16 v[140:143], v[252:255], v[220:223], v[140:143]\n"
    "v_mfma_f32_16x16x32_bf16 v[144:147], v[252:255], v[224:227], v[144:147]\n"
    "s_waitcnt lgkmcnt(2)\n"
    "v_mfma_f32_16x16x32_bf16 v[20:23], v[244:247], v[228:231], v[20:23]\n"
    "ds_read_b64_tr_b16 v[252:253], v12 offset:8480\n"
    "s_waitcnt vmcnt(22)\n"
    "v_mfma_f32_16x16x32_bf16 v[24:27], v[244:247], v[232:235], v[24:27]\n"
    "ds_read_b64_tr_b16 v[254:255], v12 offset:8736\n"
    "v_cmp_ne_u32 vcc, s7, v7\n"
    "v_mfma_f32_16x16x32_bf16 v[28:31], v[244:247], v[236:239], v[28:31]\n"
    "s_add_u32 s11, s40, 47104\n"
    "buffer_load_dwordx4 a[208:211], v13, s[12:15], s11 offen offset:0 sc1\n"
    "v_mfma_f32_16x16x32_bf16 v[32:35], v[244:247], v[240:243], v[32:35]\n"
    "s_waitcnt lgkmcnt(2)\n"
    "v_mfma_f32_16x16x32_bf16 v[36:39], v[248:251], v[228:231], v[36:39]\n"
    "ds_read_b64_tr_b16 v[244:245], v5 offset:8480\n"
    "s_cbranch_vccnz Lk_f0f_24\n"
    "Lk_f0r_25:\n"
    "v_mfma_f32_16x16x32_bf16 v[40:43], v[248:251], v[232:235], v[40:43]\n"
    "ds_read_b64_tr_b16 v[246:247], v5 offset:8736\n"
    "v_mfma_f32_16x16x32_bf16 v[44:47], v[248:251], v[236:239], v[44:47]\n"
    "v_mfma_f32_16x16x32_bf16 v[48:51], v[248:251], v[240:243], v[48:51]\n"
    "s_waitcnt lgkmcnt(2)\n"
    "v_mfma_f32_16x16x32_bf16 v[52:55], v[252:255], v[228:231], v[52:55]\n"
    "ds_read_b64_tr_b16 v[248:249], v12 offset:8512\n"
    "v_mfma_f32_16x16x32_bf16 v[56:59], v[252:255], v[232:235], v[56:59]\n"
    "ds_read_b64_tr_b16 v[250:251], v12 offset:8768\n"
    "buffer_load_dwordx4 a[212:215], v13, s[12:15], s11 offen offset:256 sc1\n"
    "v_mfma_f32_16x16x32_bf16 v[60:63], v[252:255], v[236:239], v[60:63]\n"
    "v_mfma_f32_16x16x32_bf16 v[64:67], v[252:255], v[240:243], v[64:67]\n"
    "v_cmp_u_f32 vcc, v20, v24\n"
    "v_cmp_u_f32 s[26:27], v28, v32\n"
    "s_waitcnt lgkmcnt(2)\n"
    "v_mfma_f32_16x16x32_bf16 v[68:71], v[244:247], v[228:231], v[68:71]\n"
    "ds_read_b64_tr_b16 v[252:253], v5 offset:8512\n"
    "ds_read_b128 v[212:215], v11 offset:0\n"
    "v_mfma_f32_16x16x32_bf16 v[72:75], v[244:247], v[232:235], v[72:75]\n"
    "ds_read_b64_tr_b16 v[254:255], v5 offset:8768\n"
    "ds_read_b128 v[216:219], v11 offset:64\n"
    "v_mfma_f32_16x16x32_bf16 v[76:79], v[244:247], v[236:239], v[76:79]\n"
    "s_or_b64 vcc, vcc, s[26:27]\n"
    "v_mfma_f32_16x16x32_bf16 v[80:83], v[244:247], v[240:243], v[80:83]\n"
    "s_waitcnt lgkmcnt(4)\n"
    "v_mfma_f32_16x16x32_bf16 v[84:87], v[248:251], v[228:231], v[84:87]\n"
    "ds_read_b64_tr_b16 v[244:245], v12 offset:8544\n"
    "buffer_load_dwordx4 a[216:219], v13, s[12:15], s11 offen offset:512 sc1\n"
    "v_mfma_f32_16x16x32_bf16 v[88:91], v[248:251], v[232:235], v[88:91]\n"
    "ds_read_b64_tr_b16 v[246:247], v12 offset:8800\n"
    "v_mfma_f32_16x16x32_bf16 v[92:95], v[248:251], v[236:239], v[92:95]\n"
    "v_mfma_f32_16x16x32_bf16 v[96:99], v[248:251], v[240:243], v[96:99]\n"
    "s_waitcnt lgkmcnt(3)\n"
    "v_mfma_f32_16x16x32_bf16 v[100:103], v[252:255], v[228:231], v[100:103]\n"
    "ds_read_b64_tr_b16 v[248:249], v5 offset:8544\n"
    "v_mfma_f32_16x16x32_bf16 v[104:107], v[252:255], v[232:235], v[104:107]\n"
    "ds_read_b64_tr_b16 v[250:251], v5 offset:8800\n"
    "v_mfma_f32_16x16x32_bf16 v[108:111], v[252:255], v[236:239], v[108:111]\n"
    "v_mfma_f32_16x16x32_bf16 v[112:115], v[252:255], v[240:243], v[112:115]\n"
    "buffer_load_dwordx4 a[220:223], v13, s[12:15], s11 offen offset:768 sc1\n"
    "s_waitcnt lgkmcnt(2)\n"
    "v_mfma_f32_16x16x32_bf16 v[116:119], v[244:247], v[228:231], v[116:119]\n"
    "v_mfma_f32_16x16x32_bf16 v[120:123], v[244:247], v[232:235], v[120:123]\n"
    "v_mfma_f32_16x16x32_bf16 v[124:127], v[244:247], v[236:239], v[124:127]\n"
    "v_mfma_f32_16x16x32_bf16 v[128:131], v[244:247], v[240:243], v[128:131]\n"
    "s_waitcnt lgkmcnt(0)\n"
    "v_mfma_f32_16x16x32_bf16 v[132:135], v[248:251], v[228:231], v[132:135]\n"
    "v_mfma_f32_16x16x32_bf16 v[136:139], v[248:251], v[232:235], v[136:139]\n"
    "v_mfma_f32_16x16x32_bf16 v[140:143], v[248:251], v[236:239], v[140:143]\n"
    "v_mfma_f32_16x16x32_bf16 v[144:147], v[248:251], v[240:243], v[144:147]\n"
    "s_cbranch_vccnz Lk_p3redo\n"
    "Lk_p3ok:\n"
    "v_lshlrev_b32 v12, 4, v0\n"
    "s_waitcnt vmcnt(21)\n"
    "v_pk_fma_f32 v[20:21], v[212:213], v[148:149], v[20:21] op_sel:[0,0,0] op_sel_hi:[1,0,1]\n"
    "v_pk_fma_f32 v[24:25], v[212:213], v[152:153], v[24:25] op_sel:[0,0,0] op_sel_hi:[1,0,1]\n"
    "v_pk_fma_f32 v[28:29], v[212:213], v[156:157], v[28:29] op_sel:[0,0,0] op_sel_hi:[1,0,1]\n"
    "v_pk_fma_f32 v[32:33], v[212:213], v[160:161], v[32:33] op_sel:[0,0,0] op_sel_hi:[1,0,1]\n"
    "v_pk_fma_f32 v[36:37], v[216:217], v[148:149], v[36:37] op_sel:[0,0,0] op_sel_hi:[1,0,1]\n"
    "v_pk_fma_f32 v[40:41], v[216:217], v[152:153], v[40:41] op_sel:[0,0,0] op_sel_hi:[1,0,1]\n"
    "v_pk_fma_f32 v[44:45], v[216:217], v[156:157], v[44:45] op_sel:[0,0,0] op_sel_hi:[1,0,1]\n"
    "v_pk_fma_f32 v[48:49], v[216:217], v[160:161], v[48:49] op_sel:[0,0,0] op_sel_hi:[1,0,1]\n"
    "v_pk_mul_f32 v[228:229], v[20:21], v[148:149] op_sel:[0,1] op_sel_hi:[1,1]\n"
    "v_pk_mul_f32 v[230:231], v[24:25], v[152:153] op_sel:[0,1] op_sel_hi:[1,1]\n"
    "v_pk_mul_f32 v[232:233], v[28:29], v[156:157] op_sel:[0,1] op_sel_hi:[1,1]\n"
    "v_pk_mul_f32 v[234:235], v[32:33], v[160:161] op_sel:[0,1] op_sel_hi:[1,1]\n"
    "v_exp_f32 v228, v228\n"
    "v_exp_f32 v229, v229\n"
    "v_pk_mul_f32 v[20:21], v[20:21], v[36:37]\n"
    "v_exp_f32 v230, v230\n"
    "v_exp_f32 v231, v231\n"
    "v_pk_mul_f32 v[24:25], v[24:25], v[40:41]\n"
    "v_exp_f32 v232, v232\n"
    "v_exp_f32 v233, v233\n"
    "v_pk_mul_f32 v[28:29], v[28:29], v[44:45]\n"
    "v_exp_f32 v234, v234\n"
    "v_exp_f32 v235, v235\n"
    "v_pk_mul_f32 v[32:33], v[32:33], v[48:49]\n"
    "v_pk_fma_f32 v[228:229], v[228:229], v[150:151], v[150:151] op_sel:[0,0,0] op_sel_hi:[1,0,0]\n"
    "v_pk_fma_f32 v[230:231], v[230:231], v[154:155], v[154:155] op_sel:[0,0,0] op_sel_hi:[1,0,0]\n"
    "v_pk_fma_f32 v[232:233], v[232:233], v[158:159], v[158:159] op_sel:[0,0,0] op_sel_hi:[1,0,0]\n"
    "v_pk_fma_f32 v[234:235], v[234:235], v[162:163], v[162:163] op_sel:[0,0,0] op_sel_hi:[1,0,0]\n"
    "v_rcp_f32 v228, v228\n"
    "v_rcp_f32 v229, v229\n"
    "v_rcp_f32 v230, v230\n"
    "v_rcp_f32 v231, v231\n"
    "v_rcp_f32 v232, v232\n"
    "v_rcp_f32 v233, v233\n"
    "v_rcp_f32 v234, v234\n"
    "v_rcp_f32 v235, v235\n"
    "v_pk_mul_f32 v[20:21], v[20:21], v[228:229]\n"
    "v_pk_mul_f32 v[24:25], v[24:25], v[230:231]\n"
    "v_pk_mul_f32 v[28:29], v[28:29], v[232:233]\n"
    "v_pk_mul_f32 v[32:33], v[32:33], v[234:235]\n"
    "v_cvt_pk_bf16_f32 v236, v20, v21\n"
    "v_cvt_pk_bf16_f32 v237, v24, v25\n"
    "v_cvt_pk_bf16_f32 v238, v28, v29\n"
    "v_cvt_pk_bf16_f32 v239, v32, v33\n"
    "buffer_store_dwordx4 v[236:239], v4, s[12:15], s6 offen offset:0 sc1\n"
    "buffer_load_dwordx4 v[244:247], v12, s[12:15], s36 offen sc1\n"
    "buffer_load_dwordx4 v[248:251], v12, s[12:15], s37 offen sc1\n"
    "ds_read_b128 v[220:223], v11 offset:128\n"
    "ds_read_b128 v[224:227], v11 offset:192\n"
    "v_pk_fma_f32 v[22:23], v[214:215], v[148:149], v[22:23] op_sel:[0,0,0] op_sel_hi:[1,0,1]\n"
    "v_pk_fma_f32 v[26:27], v[214:215], v[152:153], v[26:27] op_sel:[0,0,0] op_sel_hi:[1,0,1]\n"
    "v_pk_fma_f32 v[30:31], v[214:215], v[156:157], v[30:31] op_sel:[0,0,0] op_sel_hi:[1,0,1]\n"
    "v_pk_fma_f32 v[34:35], v[214:215], v[160:161], v[34:35] op_sel:[0,0,0] op_sel_hi:[1,0,1]\n"
    "v_pk_fma_f32 v[38:39], v[218:219], v[148:149], v[38:39] op_sel:[0,0,0] op_sel_hi:[1,0,1]\n"
    "v_pk_fma_f32 v[42:43], v[218:219], v[152:153], v[42:43] op_sel:[0,0,0] op_sel_hi:[1,0,1]\n"
    "v_pk_fma_f32 v[46:47], v[218:219], v[156:157], v[46:47] op_sel:[0,0,0] op_sel_hi:[1,0,1]\n"
    "v_pk_fma_f32 v[50:51], v[218:219], v[160:161], v[50:51] op_sel:[0,0,0] op_sel_hi:[1,0,1]\n"
    "v_pk_mul_f32 v[228:229], v[22:23], v[148:149] op_sel:[0,1] op_sel_hi:[1,1]\n"
    "v_pk_mul_f32 v[230:231], v[26:27], v[152:153] op_sel:[0,1] op_sel_hi:[1,1]\n"
    "v_pk_mul_f32 v[232:233], v[30:31], v[156:157] op_sel:[0,1] op_sel_hi:[1,1]\n"
    "v_pk_mul_f32 v[234:235], v[34:35], v[160:161] op_sel:[0,1] op_sel_hi:[1,1]\n"
    "v_exp_f32 v228, v228\n"
    "v_exp_f32 v229, v229\n"
    "v_pk_mul_f32 v[22:23], v[22:23], v[38:39]\n"
    "v_exp_f32 v230, v230\n"
    "v_exp_f32 v231, v231\n"
    "v_pk_mul_f32 v[26:27], v[26:27], v[42:43]\n"
    "v_exp_f32 v232, v232\n"
    "v_exp_f32 v233, v233\n"
    "v_pk_mul_f32 v[30:31], v[30:31], v[46:47]\n"
    "v_exp_f32 v234, v234\n"
    "v_exp_f32 v235, v235\n"
    "v_pk_mul_f32 v[34:35], v[34:35], v[50:51]\n"
    "v_pk_fma_f32 v[228:229], v[228:229], v[150:151], v[150:151] op_sel:[0,0,0] op_sel_hi:[1,0,0]\n"
    "v_pk_fma_f32 v[230:231], v[230:231], v[154:155], v[154:155] op_sel:[0,0,0] op_sel_hi:[1,0,0]\n"
    "v_pk_fma_f32 v[232:233], v[232:233], v[158:159], v[158:159] op_sel:[0,0,0] op_sel_hi:[1,0,0]\n"
    "v_pk_fma_f32 v[234:235], v[234:235], v[162:163], v[162:163] op_sel:[0,0,0] op_sel_hi:[1,0,0]\n"
    "v_rcp_f32 v228, v228\n"
    "v_rcp_f32 v229, v229\n"
    "v_rcp_f32 v230, v230\n"
    "v_rcp_f32 v231, v231\n"
    "v_rcp_f32 v232, v232\n"
    "v_rcp_f32 v233, v233\n"
    "v_rcp_f32 v234, v234\n"
    "v_rcp_f32 v235, v235\n"
    "v_pk_mul_f32 v[22:23], v[22:23], v[228:229]\n"
    "v_pk_mul_f32 v[26:27], v[26:27], v[230:231]\n"
    "v_pk_mul_f32 v[30:31], v[30:31], v[232:233]\n"
    "v_pk_mul_f32 v[34:35], v[34:35], v[234:235]\n"
    "v_cvt_pk_bf16_f32 v240, v22, v23\n"
    "v_cvt_pk_bf16_f32 v241, v26, v27\n"
    "v_cvt_pk_bf16_f32 v242, v30, v31\n"
    "v_cvt_pk_bf16_f32 v243, v34, v35\n"
    "buffer_store_dwordx4 v[240:243], v4, s[12:15], s6 offen offset:256 sc1\n"
    "s_waitcnt lgkmcnt(0)\n"
    "v_pk_fma_f32 v[52:53], v[220:221], v[148:149], v[52:53] op_sel:[0,0,0] op_sel_hi:[1,0,1]\n"
    "v_pk_fma_f32 v[56:57], v[220:221], v[152:153], v[56:57] op_sel:[0,0,0] op_sel_hi:[1,0,1]\n"
    "v_pk_fma_f32 v[60:61], v[220:221], v[156:157], v[60:61] op_sel:[0,0,0] op_sel_hi:[1,0,1]\n"
    "v_pk_fma_f32 v[64:65], v[220:221], v[160:161], v[64:65] op_sel:[0,0,0] op_sel_hi:[1,0,1]\n"
    "v_pk_fma_f32 v[68:69], v[224:225], v[148:149], v[68:69] op_sel:[0,0,0] op_sel_hi:[1,0,1]\n"
    "v_pk_fma_f32 v[72:73], v[224:225], v[152:153], v[72:73] op_sel:[0,0,0] op_sel_hi:[1,0,1]\n"
    "v_pk_fma_f32 v[76:77], v[224:225], v[156:157], v[76:77] op_sel:[0,0,0] op_sel_hi:[1,0,1]\n"
    "v_pk_fma_f32 v[80:81], v[224:225], v[160:161], v[80:81] op_sel:[0,0,0] op_sel_hi:[1,0,1]\n"
    "v_pk_mul_f32 v[228:229], v[52:53], v[148:149] op_sel:[0,1] op_sel_hi:[1,1]\n"
    "v_pk_mul_f32 v[230:231], v[56:57], v[152:153] op_sel:[0,1] op_sel_hi:[1,1]\n"
    "v_pk_mul_f32 v[232:233], v[60:61], v[156:157] op_sel:[0,1] op_sel_hi:[1,1]\n"
    "v_pk_mul_f32 v[234:235], v[64:65], v[160:161] op_sel:[0,1] op_sel_hi:[1,1]\n"
    "v_exp_f32 v228, v228\n"
    "v_exp_f32 v229, v229\n"
    "v_pk_mul_f32 v[52:53], v[52:53], v[68:69]\n"
    "v_exp_f32 v230, v230\n"
    "v_exp_f32 v231, v231\n"
    "v_pk_mul_f32 v[56:57], v[56:57], v[72:73]\n"
    "v_exp_f32 v232, v232\n"
    "v_exp_f32 v233, v233\n"
    "v_pk_mul_f32 v[60:61], v[60:61], v[76:77]\n"
    "v_exp_f32 v234, v234\n"
    "v_exp_f32 v235, v235\n"
    "v_pk_mul_f32 v[64:65], v[64:65], v[80:81]\n"
    "v_pk_fma_f32 v[228:229], v[228:229], v[150:151], v[150:151] op_sel:[0,0,0] op_sel_hi:[1,0,0]\n"
    "v_pk_fma_f32 v[230:231], v[230:231], v[154:155], v[154:155] op_sel:[0,0,0] op_sel_hi:[1,0,0]\n"
    "v_pk_fma_f32 v[232:233], v[232:233], v[158:159], v[158:159] op_sel:[0,0,0] op_sel_hi:[1,0,0]\n"
    "v_pk_fma_f32 v[234:235], v[234:235], v[162:163], v[162:163] op_sel:[0,0,0] op_sel_hi:[1,0,0]\n"
    "v_rcp_f32 v228, v228\n"
    "v_rcp_f32 v229, v229\n"
    "v_rcp_f32 v230, v230\n"
    "v_rcp_f32 v231, v231\n"
    "v_rcp_f32 v232, v232\n"
    "v_rcp_f32 v233, v233\n"
    "v_rcp_f32 v234, v234\n"
    "v_rcp_f32 v235, v235\n"
    "v_pk_mul_f32 v[52:53], v[52:53], v[228:229]\n"
    "v_pk_mul_f32 v[56:57], v[56:57], v[230:231]\n"
    "v_pk_mul_f32 v[60:61], v[60:61], v[232:233]\n"
    "v_pk_mul_f32 v[64:65], v[64:65], v[234:235]\n"
    "v_cvt_pk_bf16_f32 v236, v52, v53\n"
    "v_cvt_pk_bf16_f32 v237, v56, v57\n"
    "v_cvt_pk_bf16_f32 v238, v60, v61\n"
    "v_cvt_pk_bf16_f32 v239, v64, v65\n"
    "s_add_u32 s8, s6, 14336\n"
    "buffer_store_dwordx4 v[236:239], v4, s[12:15], s8 offen offset:0 sc1\n"
    "ds_read_b128 v[212:215], v11 offset:256\n"
    "ds_read_b128 v[216:219], v11 offset:320\n"
    "v_pk_fma_f32 v[54:55], v[222:223], v[148:149], v[54:55] op_sel:[0,0,0] op_sel_hi:[1,0,1]\n"
    "v_pk_fma_f32 v[58:59], v[222:223], v[152:153], v[58:59] op_sel:[0,0,0] op_sel_hi:[1,0,1]\n"
    "v_pk_fma_f32 v[62:63], v[222:223], v[156:157], v[62:63] op_sel:[0,0,0] op_sel_hi:[1,0,1]\n"
    "v_pk_fma_f32 v[66:67], v[222:223], v[160:161], v[66:67] op_sel:[0,0,0] op_sel_hi:[1,0,1]\n"
    "v_pk_fma_f32 v[70:71], v[226:227], v[148:149], v[70:71] op_sel:[0,0,0] op_sel_hi:[1,0,1]\n"
    "v_pk_fma_f32 v[74:75], v[226:227], v[152:153], v[74:75] op_sel:[0,0,0] op_sel_hi:[1,0,1]\n"
    "v_pk_fma_f32 v[78:79], v[226:227], v[156:157], v[78:79] op_sel:[0,0,0] op_sel_hi:[1,0,1]\n"
    "v_pk_fma_f32 v[82:83], v[226:227], v[160:161], v[82:83] op_sel:[0,0,0] op_sel_hi:[1,0,1]\n"
    "v_pk_mul_f32 v[228:229], v[54:55], v[148:149] op_sel:[0,1] op_sel_hi:[1,1]\n"
    "v_pk_mul_f32 v[230:231], v[58:59], v[152:153] op_sel:[0,1] op_sel_hi:[1,1]\n"
    "v_pk_mul_f32 v[232:233], v[62:63], v[156:157] op_sel:[0,1] op_sel_hi:[1,1]\n"
    "v_pk_mul_f32 v[234:235], v[66:67], v[160:161] op_sel:[0,1] op_sel_hi:[1,1]\n"
    "v_exp_f32 v228, v228\n"
    "v_exp_f32 v229, v229\n"
    "v_pk_mul_f32 v[54:55], v[54:55], v[70:71]\n"
    "v_exp_f32 v230, v230\n"
    "v_exp_f32 v231, v231\n"
    "v_pk_mul_f32 v[58:59], v[58:59], v[74:75]\n"
    "v_exp_f32 v232, v232\n"
    "v_exp_f32 v233, v233\n"
    "v_pk_mul_f32 v[62:63], v[62:63], v[78:79]\n"
    "v_exp_f32 v234, v234\n"
    "v_exp_f32 v235, v235\n"
    "v_pk_mul_f32 v[66:67], v[66:67], v[82:83]\n"
    "v_pk_fma_f32 v[228:229], v[228:229], v[150:151], v[150:151] op_sel:[0,0,0] op_sel_hi:[1,0,0]\n"
    "v_pk_fma_f32 v[230:231], v[230:231], v[154:155], v[154:155] op_sel:[0,0,0] op_sel_hi:[1,0,0]\n"
    "v_pk_fma_f32 v[232:233], v[232:233], v[158:159], v[158:159] op_sel:[0,0,0] op_sel_hi:[1,0,0]\n"
    "v_pk_fma_f32 v[234:235], v[234:235], v[162:163], v[162:163] op_sel:[0,0,0] op_sel_hi:[1,0,0]\n"
    "v_rcp_f32 v228, v228\n"
    "v_rcp_f32 v229, v229\n"
    "v_rcp_f32 v230, v230\n"
    "v_rcp_f32 v231, v231\n"
    "v_rcp_f32 v232, v232\n"
    "v_rcp_f32 v233, v233\n"
    "v_rcp_f32 v234, v234\n"
    "v_rcp_f32 v235, v235\n"
    "v_pk_mul_f32 v[54:55], v[54:55], v[228:229]\n"
    "v_pk_mul_f32 v[58:59], v[58:59], v[230:231]\n"
    "v_pk_mul_f32 v[62:63], v[62:63], v[232:233]\n"
    "v_pk_mul_f32 v[66:67], v[66:67], v[234:235]\n"
    "v_cvt_pk_bf16_f32 v240, v54, v55\n"
    "v_cvt_pk_bf16_f32 v241, v58, v59\n"
    "v_cvt_pk_bf16_f32 v242, v62, v63\n"
    "v_cvt_pk_bf16_f32 v243, v66, v67\n"
    "buffer_store_dwordx4 v[240:243], v4, s[12:15], s8 offen offset:256 sc1\n"
    "s_waitcnt lgkmcnt(0)\n"
    "v_pk_fma_f32 v[84:85], v[212:213], v[148:149], v[84:85] op_sel:[0,0,0] op_sel_hi:[1,0,1]\n"
    "v_pk_fma_f32 v[88:89], v[212:213], v[152:153], v[88:89] op_sel:[0,0,0] op_sel_hi:[1,0,1]\n"
    "v_pk_fma_f32 v[92:93], v[212:213], v[156:157], v[92:93] op_sel:[0,0,0] op_sel_hi:[1,0,1]\n"
    "v_pk_fma_f32 v[96:97], v[212:213], v[160:161], v[96:97] op_sel:[0,0,0] op_sel_hi:[1,0,1]\n"
    "v_pk_fma_f32 v[100:101], v[216:217], v[148:149], v[100:101] op_sel:[0,0,0] op_sel_hi:[1,0,1]\n"
    "v_pk_fma_f32 v[104:105], v[216:217], v[152:153], v[104:105] op_sel:[0,0,0] op_sel_hi:[1,0,1]\n"
    "v_pk_fma_f32 v[108:109], v[216:217], v[156:157], v[108:109] op_sel:[0,0,0] op_sel_hi:[1,0,1]\n"
    "v_pk_fma_f32 v[112:113], v[216:217], v[160:161], v[112:113] op_sel:[0,0,0] op_sel_hi:[1,0,1]\n"
    "v_pk_mul_f32 v[228:229], v[84:85], v[148:149] op_sel:[0,1] op_sel_hi:[1,1]\n"
    "v_pk_mul_f32 v[230:231], v[88:89], v[152:153] op_sel:[0,1] op_sel_hi:[1,1]\n"
    "v_pk_mul_f32 v[232:233], v[92:93], v[156:157] op_sel:[0,1] op_sel_hi:[1,1]\n"
    "v_pk_mul_f32 v[234:235], v[96:97], v[160:161] op_sel:[0,1] op_sel_hi:[1,1]\n"
    "v_exp_f32 v228, v228\n"
    "v_exp_f32 v229, v229\n"
    "v_pk_mul_f32 v[84:85], v[84:85], v[100:101]\n"
    "v_exp_f32 v230, v230\n"
    "v_exp_f32 v231, v231\n"
    "v_pk_mul_f32 v[88:89], v[88:89], v[104:105]\n"
    "v_exp_f32 v232, v232\n"
    "v_exp_f32 v233, v233\n"
    "v_pk_mul_f32 v[92:93], v[92:93], v[108:109]\n"
    "v_exp_f32 v234, v234\n"
    "v_exp_f32 v235, v235\n"
    "v_pk_mul_f32 v[96:97], v[96:97], v[112:113]\n"
    "v_pk_fma_f32 v[228:229], v[228:229], v[150:151], v[150:151] op_sel:[0,0,0] op_sel_hi:[1,0,0]\n"
    "v_pk_fma_f32 v[230:231], v[230:231], v[154:155], v[154:155] op_sel:[0,0,0] op_sel_hi:[1,0,0]\n"
    "v_pk_fma_f32 v[232:233], v[232:233], v[158:159], v[158:159] op_sel:[0,0,0] op_sel_hi:[1,0,0]\n"
    "v_pk_fma_f32 v[234:235], v[234:235], v[162:163], v[162:163] op_sel:[0,0,0] op_sel_hi:[1,0,0]\n"
    "v_rcp_f32 v228, v228\n"
    "v_rcp_f32 v229, v229\n"
    "v_rcp_f32 v230, v230\n"
    "v_rcp_f32 v231, v231\n"
    "v_rcp_f32 v232, v232\n"
    "v_rcp_f32 v233, v233\n"
    "v_rcp_f32 v234, v234\n"
    "v_rcp_f32 v235, v235\n"
    "v_pk_mul_f32 v[84:85], v[84:85], v[228:229]\n"
    "v_pk_mul_f32 v[88:89], v[88:89], v[230:231]\n"
    "v_pk_mul_f32 v[92:93], v[92:93], v[232:233]\n"
    "v_pk_mul_f32 v[96:97], v[96:97], v[234:235]\n"
    "v_cvt_pk_bf16_f32 v236, v84, v85\n"
    "v_cvt_pk_bf16_f32 v237, v88, v89\n"
    "v_cvt_pk_bf16_f32 v238, v92, v93\n"
    "v_cvt_pk_bf16_f32 v239, v96, v97\n"
    "s_add_u32 s8, s6, 28672\n"
    "buffer_store_dwordx4 v[236:239], v4, s[12:15], s8 offen offset:0 sc1\n"
    "ds_read_b128 v[220:223], v11 offset:384\n"
    "ds_read_b128 v[224:227], v11 offset:448\n"
    "v_pk_fma_f32 v[86:87], v[214:215], v[148:149], v[86:87] op_sel:[0,0,0] op_sel_hi:[1,0,1]\n"
    "v_pk_fma_f32 v[90:91], v[214:215], v[152:153], v[90:91] op_sel:[0,0,0] op_sel_hi:[1,0,1]\n"
    "v_pk_fma_f32 v[94:95], v[214:215], v[156:157], v[94:95] op_sel:[0,0,0] op_sel_hi:[1,0,1]\n"
    "v_pk_fma_f32 v[98:99], v[214:215], v[160:161], v[98:99] op_sel:[0,0,0] op_sel_hi:[1,0,1]\n"
    "v_pk_fma_f32 v[102:103], v[218:219], v[148:149], v[102:103] op_sel:[0,0,0] op_sel_hi:[1,0,1]\n"
    "v_pk_fma_f32 v[106:107], v[218:219], v[152:153], v[106:107] op_sel:[0,0,0] op_sel_hi:[1,0,1]\n"
    "v_pk_fma_f32 v[110:111], v[218:219], v[156:157], v[110:111] op_sel:[0,0,0] op_sel_hi:[1,0,1]\n"
    "v_pk_fma_f32 v[114:115], v[218:219], v[160:161], v[114:115] op_sel:[0,0,0] op_sel_hi:[1,0,1]\n"
    "v_pk_mul_f32 v[228:229], v[86:87], v[148:149] op_sel:[0,1] op_sel_hi:[1,1]\n"
    "v_pk_mul_f32 v[230:231], v[90:91], v[152:153] op_sel:[0,1] op_sel_hi:[1,1]\n"
    "v_pk_mul_f32 v[232:233], v[94:95], v[156:157] op_sel:[0,1] op_sel_hi:[1,1]\n"
    "v_pk_mul_f32 v[234:235], v[98:99], v[160:161] op_sel:[0,1] op_sel_hi:[1,1]\n"
    "v_exp_f32 v228, v228\n"
    "v_exp_f32 v229, v229\n"
    "v_pk_mul_f32 v[86:87], v[86:87], v[102:103]\n"
    "v_exp_f32 v230, v230\n"
    "v_exp_f32 v231, v231\n"
    "v_pk_mul_f32 v[90:91], v[90:91], v[106:107]\n"
    "v_exp_f32 v232, v232\n"
    "v_exp_f32 v233, v233\n"
    "v_pk_mul_f32 v[94:95], v[94:95], v[110:111]\n"
    "v_exp_f32 v234, v234\n"
    "v_exp_f32 v235, v235\n"
    "v_pk_mul_f32 v[98:99], v[98:99], v[114:115]\n"
    "v_pk_fma_f32 v[228:229], v[228:229], v[150:151], v[150:151] op_sel:[0,0,0] op_sel_hi:[1,0,0]\n"
    "v_pk_fma_f32 v[230:231], v[230:231], v[154:155], v[154:155] op_sel:[0,0,0] op_sel_hi:[1,0,0]\n"
    "v_pk_fma_f32 v[232:233], v[232:233], v[158:159], v[158:159] op_sel:[0,0,0] op_sel_hi:[1,0,0]\n"
    "v_pk_fma_f32 v[234:235], v[234:235], v[162:163], v[162:163] op_sel:[0,0,0] op_sel_hi:[1,0,0]\n"
    "v_rcp_f32 v228, v228\n"
    "v_rcp_f32 v229, v229\n"
    "v_rcp_f32 v230, v230\n"
    "v_rcp_f32 v231, v231\n"
    "v_rcp_f32 v232, v232\n"
    "v_rcp_f32 v233, v233\n"
    "v_rcp_f32 v234, v234\n"
    "v_rcp_f32 v235, v235\n"
    "v_pk_mul_f32 v[86:87], v[86:87], v[228:229]\n"
    "v_pk_mul_f32 v[90:91], v[90:91], v[230:231]\n"
    "v_pk_mul_f32 v[94:95], v[94:95], v[232:233]\n"
    "v_pk_mul_f32 v[98:99], v[98:99], v[234:235]\n"
    "v_cvt_pk_bf16_f32 v240, v86, v87\n"
    "v_cvt_pk_bf16_f32 v241, v90, v91\n"
    "v_cvt_pk_bf16_f32 v242, v94, v95\n"
    "v_cvt_pk_bf16_f32 v243, v98, v99\n"
    "buffer_store_dwordx4 v[240:243], v4, s[12:15], s8 offen offset:256 sc1\n"
    "s_waitcnt lgkmcnt(0)\n"
    "v_pk_fma_f32 v[116:117], v[220:221], v[148:149], v[116:117] op_sel:[0,0,0] op_sel_hi:[1,0,1]\n"
    "v_pk_fma_f32 v[120:121], v[220:221], v[152:153], v[120:121] op_sel:[0,0,0] op_sel_hi:[1,0,1]\n"
    "v_pk_fma_f32 v[124:125], v[220:221], v[156:157], v[124:125] op_sel:[0,0,0] op_sel_hi:[1,0,1]\n"
    "v_pk_fma_f32 v[128:129], v[220:221], v[160:161], v[128:129] op_sel:[0,0,0] op_sel_hi:[1,0,1]\n"
    "v_pk_fma_f32 v[132:133], v[224:225], v[148:149], v[132:133] op_sel:[0,0,0] op_sel_hi:[1,0,1]\n"
    "v_pk_fma_f32 v[136:137], v[224:225], v[152:153], v[136:137] op_sel:[0,0,0] op_sel_hi:[1,0,1]\n"
    "v_pk_fma_f32 v[140:141], v[224:225], v[156:157], v[140:141] op_sel:[0,0,0] op_sel_hi:[1,0,1]\n"
    "v_pk_fma_f32 v[144:145], v[224:225], v[160:161], v[144:145] op_sel:[0,0,0] op_sel_hi:[1,0,1]\n"
    "v_pk_mul_f32 v[228:229], v[116:117], v[148:149] op_sel:[0,1] op_sel_hi:[1,1]\n"
    "v_pk_mul_f32 v[230:231], v[120:121], v[152:153] op_sel:[0,1] op_sel_hi:[1,1]\n"
    "v_pk_mul_f32 v[232:233], v[124:125], v[156:157] op_sel:[0,1] op_sel_hi:[1,1]\n"
    "v_pk_mul_f32 v[234:235], v[128:129], v[160:161] op_sel:[0,1] op_sel_hi:[1,1]\n"
    "v_exp_f32 v228, v228\n"
    "v_exp_f32 v229, v229\n"
    "v_pk_mul_f32 v[116:117], v[116:117], v[132:133]\n"
    "v_exp_f32 v230, v230\n"
    "v_exp_f32 v231, v231\n"
    "v_pk_mul_f32 v[120:121], v[120:121], v[136:137]\n"
    "v_exp_f32 v232, v232\n"
    "v_exp_f32 v233, v233\n"
    "v_pk_mul_f32 v[124:125], v[124:125], v[140:141]\n"
    "v_exp_f32 v234, v234\n"
    "v_exp_f32 v235, v235\n"
    "v_pk_mul_f32 v[128:129], v[128:129], v[144:145]\n"
    "v_pk_fma_f32 v[228:229], v[228:229], v[150:151], v[150:151] op_sel:[0,0,0] op_sel_hi:[1,0,0]\n"
    "v_pk_fma_f32 v[230:231], v[230:231], v[154:155], v[154:155] op_sel:[0,0,0] op_sel_hi:[1,0,0]\n"
    "v_pk_fma_f32 v[232:233], v[232:233], v[158:159], v[158:159] op_sel:[0,0,0] op_sel_hi:[1,0,0]\n"
    "v_pk_fma_f32 v[234:235], v[234:235], v[162:163], v[162:163] op_sel:[0,0,0] op_sel_hi:[1,0,0]\n"
    "v_rcp_f32 v228, v228\n"
    "v_rcp_f32 v229, v229\n"
    "v_rcp_f32 v230, v230\n"
    "v_rcp_f32 v231, v231\n"
    "v_rcp_f32 v232, v232\n"
    "v_rcp_f32 v233, v233\n"
    "v_rcp_f32 v234, v234\n"
    "v_rcp_f32 v235, v235\n"
    "v_pk_mul_f32 v[116:117], v[116:117], v[228:229]\n"
    "v_pk_mul_f32 v[120:121], v[120:121], v[230:231]\n"
    "v_pk_mul_f32 v[124:125], v[124:125], v[232:233]\n"
    "v_pk_mul_f32 v[128:129], v[128:129], v[234:235]\n"
    "v_cvt_pk_bf16_f32 v236, v116, v117\n"
    "v_cvt_pk_bf16_f32 v237, v120, v121\n"
    "v_cvt_pk_bf16_f32 v238, v124, v125\n"
    "v_cvt_pk_bf16_f32 v239, v128, v129\n"
    "s_add_u32 s8, s6, 43008\n"
    "buffer_store_dwordx4 v[236:239], v4, s[12:15], s8 offen offset:0 sc1\n"
    "v_pk_fma_f32 v[118:119], v[222:223], v[148:149], v[118:119] op_sel:[0,0,0] op_sel_hi:[1,0,1]\n"
    "v_pk_fma_f32 v[122:123], v[222:223], v[152:153], v[122:123] op_sel:[0,0,0] op_sel_hi:[1,0,1]\n"
    "v_pk_fma_f32 v[126:127], v[222:223], v[156:157], v[126:127] op_sel:[0,0,0] op_sel_hi:[1,0,1]\n"
    "v_pk_fma_f32 v[130:131], v[222:223], v[160:161], v[130:131] op_sel:[0,0,0] op_sel_hi:[1,0,1]\n"
    "v_pk_fma_f32 v[134:135], v[226:227], v[148:149], v[134:135] op_sel:[0,0,0] op_sel_hi:[1,0,1]\n"
    "v_pk_fma_f32 v[138:139], v[226:227], v[152:153], v[138:139] op_sel:[0,0,0] op_sel_hi:[1,0,1]\n"
    "v_pk_fma_f32 v[142:143], v[226:227], v[156:157], v[142:143] op_sel:[0,0,0] op_sel_hi:[1,0,1]\n"
    "v_pk_fma_f32 v[146:147], v[226:227], v[160:161], v[146:147] op_sel:[0,0,0] op_sel_hi:[1,0,1]\n"
    "v_pk_mul_f32 v[228:229], v[118:119], v[148:149] op_sel:[0,1] op_sel_hi:[1,1]\n"
    "v_pk_mul_f32 v[230:231], v[122:123], v[152:153] op_sel:[0,1] op_sel_hi:[1,1]\n"
    "v_pk_mul_f32 v[232:233], v[126:127], v[156:157] op_sel:[0,1] op_sel_hi:[1,1]\n"
    "v_pk_mul_f32 v[234:235], v[130:131], v[160:161] op_sel:[0,1] op_sel_hi:[1,1]\n"
    "v_exp_f32 v228, v228\n"
    "v_exp_f32 v229, v229\n"
    "v_pk_mul_f32 v[118:119], v[118:119], v[134:135]\n"
    "v_exp_f32 v230, v230\n"
    "v_exp_f32 v231, v231\n"
    "v_pk_mul_f32 v[122:123], v[122:123], v[138:139]\n"
    "v_exp_f32 v232, v232\n"
    "v_exp_f32 v233, v233\n"
    "v_pk_mul_f32 v[126:127], v[126:127], v[142:143]\n"
    "v_exp_f32 v234, v234\n"
    "v_exp_f32 v235, v235\n"
    "v_pk_mul_f32 v[130:131], v[130:131], v[146:147]\n"
    "v_pk_fma_f32 v[228:229], v[228:229], v[150:151], v[150:151] op_sel:[0,0,0] op_sel_hi:[1,0,0]\n"
    "v_pk_fma_f32 v[230:231], v[230:231], v[154:155], v[154:155] op_sel:[0,0,0] op_sel_hi:[1,0,0]\n"
    "v_pk_fma_f32 v[232:233], v[232:233], v[158:159], v[158:159] op_sel:[0,0,0] op_sel_hi:[1,0,0]\n"
    "v_pk_fma_f32 v[234:235], v[234:235], v[162:163], v[162:163] op_sel:[0,0,0] op_sel_hi:[1,0,0]\n"
    "v_rcp_f32 v228, v228\n"
    "v_rcp_f32 v229, v229\n"
    "v_rcp_f32 v230, v230\n"
    "v_rcp_f32 v231, v231\n"
    "v_rcp_f32 v232, v232\n"
    "v_rcp_f32 v233, v233\n"
    "v_rcp_f32 v234, v234\n"
    "v_rcp_f32 v235, v235\n"
    "v_pk_mul_f32 v[118:119], v[118:119], v[228:229]\n"
    "v_pk_mul_f32 v[122:123], v[122:123], v[230:231]\n"
    "v_pk_mul_f32 v[126:127], v[126:127], v[232:233]\n"
    "v_pk_mul_f32 v[130:131], v[130:131], v[234:235]\n"
    "v_cvt_pk_bf16_f32 v240, v118, v119\n"
    "v_cvt_pk_bf16_f32 v241, v122, v123\n"
    "v_cvt_pk_bf16_f32 v242, v126, v127\n"
    "v_cvt_pk_bf16_f32 v243, v130, v131\n"
    "buffer_store_dwordx4 v[240:243], v4, s[12:15], s8 offen offset:256 sc1\n"
    "Lk_join:\n"
    "s_add_u32 s49, s42, 0\n"
    "buffer_load_dwordx4 v[164:167], v2, s[12:15], s49 offen offset:0 sc1\n"
    "buffer_load_dwordx4 v[168:171], v2, s[12:15], s49 offen offset:256 sc1\n"
    "buffer_load_dwordx4 v[172:175], v2, s[12:15], s49 offen offset:512 sc1\n"
    "buffer_load_dwordx4 v[176:179], v2, s[12:15], s49 offen offset:768 sc1\n"
    "s_add_u32 s49, s42, 4096\n"
    "buffer_load_dwordx4 v[180:183], v2, s[12:15], s49 offen offset:0 sc1\n"
    "buffer_load_dwordx4 v[184:187], v2, s[12:15], s49 offen offset:256 sc1\n"
    "buffer_load_dwordx4 v[188:191], v2, s[12:15], s49 offen offset:512 sc1\n"
    "buffer_load_dwordx4 v[192:195], v2, s[12:15], s49 offen offset:768 sc1\n"
    "s_add_u32 s49, s42, 8192\n"
    "buffer_load_dwordx4 v[196:199], v2, s[12:15], s49 offen offset:0 sc1\n"
    "buffer_load_dwordx4 v[200:203], v2, s[12:15], s49 offen offset:256 sc1\n"
    "buffer_load_dwordx4 v[204:207], v2, s[12:15], s49 offen offset:512 sc1\n"
    "buffer_load_dwordx4 v[208:211], v2, s[12:15], s49 offen offset:768 sc1\n"
    "s_add_u32 s49, s42, 12288\n"
    "buffer_load_dwordx4 a[224:227], v2, s[12:15], s49 offen offset:0 sc1\n"
    "buffer_load_dwordx4 a[228:231], v2, s[12:15], s49 offen offset:256 sc1\n"
    "buffer_load_dwordx4 a[232:235], v2, s[12:15], s49 offen offset:512 sc1\n"
    "buffer_load_dwordx4 a[236:239], v2, s[12:15], s49 offen offset:768 sc1\n"
    "s_mov_b32 s9, 0\n"
    "Lk_p4:\n"
    "s_waitcnt vmcnt(12)\n"
    "v_mov_b32 v212, v164\n"
    "v_mov_b32 v213, v168\n"
    "v_mov_b32 v214, v172\n"
    "v_mov_b32 v215, v176\n"
    "v_mov_b32 v216, v165\n"
    "v_mov_b32 v217, v169\n"
    "v_mov_b32 v218, v173\n"
    "v_mov_b32 v219, v177\n"
    "v_mov_b32 v220, v166\n"
    "v_mov_b32 v221, v170\n"
    "v_mov_b32 v222, v174\n"
    "v_mov_b32 v223, v178\n"
    "v_mov_b32 v224, v167\n"
    "v_mov_b32 v225, v171\n"
    "v_mov_b32 v226, v175\n"
    "v_mov_b32 v227, v179\n"
    "v_max3_u32 v9, v212, v213, v214\n"
    "v_max3_u32 v9, v215, v216, v9\n"
    "v_max3_u32 v9, v217, v218, v9\n"
    "v_max3_u32 v9, v219, v220, v9\n"
    "v_max3_u32 v9, v221, v222, v9\n"
    "v_max3_u32 v9, v223, v224, v9\n"
    "v_max3_u32 v9, v225, v226, v9\n"
    "v_max_u32 v9, v227, v9\n"
    "v_cmp_eq_u32 vcc, -1, v9\n"
    "s_cbranch_vccnz Lk_Hf_26\n"
    "Lk_Hr_27:\n"
    "v_mfma_f32_16x16x32_bf16 v[20:23], v[212:215], a[0:3], 0\n"
    "s_add_u32 s49, s42, 16384\n"
    "buffer_load_dwordx4 a[240:243], v2, s[12:15], s49 offen offset:0 sc1\n"
    "v_mfma_f32_16x16x32_bf16 v[24:27], v[216:219], a[0:3], 0\n"
    "s_waitcnt vmcnt(9)\n"
    "v_mov_b32 v228, v180\n"
    "v_mfma_f32_16x16x32_bf16 v[28:31], v[220:223], a[0:3], 0\n"
    "v_mov_b32 v229, v184\n"
    "v_mov_b32 v230, v188\n"
    "v_mfma_f32_16x16x32_bf16 v[32:35], v[224:227], a[0:3], 0\n"
    "buffer_load_dwordx4 a[244:247], v2, s[12:15], s49 offen offset:256 sc1\n"
    "v_mov_b32 v231, v192\n"
    "v_mov_b32 v232, v181\n"
    "v_mfma_f32_16x16x32_bf16 v[36:39], v[212:215], a[4:7], 0\n"
    "v_mov_b32 v233, v185\n"
    "v_mov_b32 v234, v189\n"
    "v_mfma_f32_16x16x32_bf16 v[40:43], v[216:219], a[4:7], 0\n"
    "v_mov_b32 v235, v193\n"
    "v_mov_b32 v236, v182\n"
    "v_mfma_f32_16x16x32_bf16 v[44:47], v[220:223], a[4:7], 0\n"
    "v_mov_b32 v237, v186\n"
    "v_mov_b32 v238, v190\n"
    "v_mfma_f32_16x16x32_bf16 v[48:51], v[224:227], a[4:7], 0\n"
    "buffer_load_dwordx4 a[248:251], v2, s[12:15], s49 offen offset:512 sc1\n"
    "v_mov_b32 v239, v194\n"
    "v_mov_b32 v240, v183\n"
    "v_mfma_f32_16x16x32_bf16 v[52:55], v[212:215], a[8:11], 0\n"
    "v_mov_b32 v241, v187\n"
    "v_mov_b32 v242, v191\n"
    "v_mfma_f32_16x16x32_bf16 v[56:59], v[216:219], a[8:11], 0\n"
    "v_mov_b32 v243, v195\n"
    "v_max3_u32 v9, v228, v229, v230\n"
    "v_mfma_f32_16x16x32_bf16 v[60:63], v[220:223], a[8:11], 0\n"
    "v_max3_u32 v9, v231, v232, v9\n"
    "v_max3_u32 v9, v233, v234, v9\n"
    "v_mfma_f32_16x16x32_bf16 v[64:67], v[224:227], a[8:11], 0\n"
    "buffer_load_dwordx4 a[252:255], v2, s[12:15], s49 offen offset:768 sc1\n"
    "v_max3_u32 v9, v235, v236, v9\n"
    "v_max3_u32 v9, v237, v238, v9\n"
    "v_mfma_f32_16x16x32_bf16 v[68:71], v[212:215], a[12:15], 0\n"
    "v_max3_u32 v9, v239, v240, v9\n"
    "v_max3_u32 v9, v241, v242, v9\n"
    "v_mfma_f32_16x16x32_bf16 v[72:75], v[216:219], a[12:15], 0\n"
    "v_max_u32 v9, v243, v9\n"
    "v_cmp_eq_u32 vcc, -1, v9\n"
    "v_mfma_f32_16x16x32_bf16 v[76:79], v[220:223], a[12:15], 0\n"
    "v_mfma_f32_16x16x32_bf16 v[80:83], v[224:227], a[12:15], 0\n"
    "s_cbranch_vccnz Lk_Hf_28\n"
    "Lk_Hr_29:\n"
    "v_mfma_f32_16x16x32_bf16 v[20:23], v[228:231], a[16:19], v[20:23]\n"
    "s_add_u32 s49, s42, 20480\n"
    "buffer_load_dwordx4 v[148:151], v2, s[12:15], s49 offen offset:0 sc1\n"
    "v_mfma_f32_16x16x32_bf16 v[24:27], v[232:235], a[16:19], v[24:27]\n"
    "s_waitcnt vmcnt(9)\n"
    "v_mov_b32 v212, v196\n"
    "v_mfma_f32_16x16x32_bf16 v[28:31], v[236:239], a[16:19], v[28:31]\n"
    "v_mov_b32 v213, v200\n"
    "v_mov_b32 v214, v204\n"
    "v_mfma_f32_16x16x32_bf16 v[32:35], v[240:243], a[16:19], v[32:35]\n"
    "buffer_load_dwordx4 v[152:155], v2, s[12:15], s49 offen offset:256 sc1\n"
    "v_mov_b32 v215, v208\n"
    "v_mov_b32 v216, v197\n"
    "v_mfma_f32_16x16x32_bf16 v[36:39], v[228:231], a[20:23], v[36:39]\n"
    "v_mov_b32 v217, v201\n"
    "v_mov_b32 v218, v205\n"
    "v_mfma_f32_16x16x32_bf16 v[40:43], v[232:235], a[20:23], v[40:43]\n"
    "v_mov_b32 v219, v209\n"
    "v_mov_b32 v220, v198\n"
    "v_mfma_f32_16x16x32_bf16 v[44:47], v[236:239], a[20:23], v[44:47]\n"
    "v_mov_b32 v221, v202\n"
    "v_mov_b32 v222, v206\n"
    "v_mfma_f32_16x16x32_bf16 v[48:51], v[240:243], a[20:23], v[48:51]\n"
    "buffer_load_dwordx4 v[156:159], v2, s[12:15], s49 offen offset:512 sc1\n"
    "v_mov_b32 v223, v210\n"
    "v_mov_b32 v224, v199\n"
    "v_mfma_f32_16x16x32_bf16 v[52:55], v[228:231], a[24:27], v[52:55]\n"
    "v_mov_b32 v225, v203\n"
    "v_mov_b32 v226, v207\n"
    "v_mfma_f32_16x16x32_bf16 v[56:59], v[232:235], a[24:27], v[56:59]\n"
    "v_mov_b32 v227, v211\n"
    "v_max3_u32 v9, v212, v213, v214\n"
    "v_mfma_f32_16x16x32_bf16 v[60:63], v[236:239], a[24:27], v[60:63]\n"
    "v_max3_u32 v9, v215, v216, v9\n"
    "v_max3_u32 v9, v217, v218, v9\n"
    "v_mfma_f32_16x16x32_bf16 v[64:67], v[240:243], a[24:27], v[64:67]\n"
    "buffer_load_dwordx4 v[160:163], v2, s[12:15], s49 offen offset:768 sc1\n"
    "v_max3_u32 v9, v219, v220, v9\n"
    "v_max3_u32 v9, v221, v222, v9\n"
    "v_mfma_f32_16x16x32_bf16 v[68:71], v[228:231], a[28:31], v[68:71]\n"
    "v_max3_u32 v9, v223, v224, v9\n"
    "v_max3_u32 v9, v225, v226, v9\n"
    "v_mfma_f32_16x16x32_bf16 v[72:75], v[232:235], a[28:31], v[72:75]\n"
    "v_max_u32 v9, v227, v9\n"
    "v_cmp_eq_u32 vcc, -1, v9\n"
    "v_mfma_f32_16x16x32_bf16 v[76:79], v[236:239], a[28:31], v[76:79]\n"
    "v_mfma_f32_16x16x32_bf16 v[80:83], v[240:243], a[28:31], v[80:83]\n"
    "s_cbranch_vccnz Lk_Hf_30\n"
    "Lk_Hr_31:\n"
    "v_mfma_f32_16x16x32_bf16 v[20:23], v[212:215], a[32:35], v[20:23]\n"
    "s_add_u32 s49, s42, 24576\n"
    "buffer_load_dwordx4 v[84:87], v2, s[12:15], s49 offen offset:0 sc1\n"
    "v_mfma_f32_16x16x32_bf16 v[24:27], v[216:219], a[32:35], v[24:27]\n"
    "s_waitcnt vmcnt(9)\n"
    "v_accvgpr_read_b32 v228, a224\n"
    "v_mfma_f32_16x16x32_bf16 v[28:31], v[220:223], a[32:35], v[28:31]\n"
    "v_accvgpr_read_b32 v229, a228\n"
    "v_accvgpr_read_b32 v230, a232\n"
    "v_mfma_f32_16x16x32_bf16 v[32:35], v[224:227], a[32:35], v[32:35]\n"
    "buffer_load_dwordx4 v[88:91], v2, s[12:15], s49 offen offset:256 sc1\n"
    "v_accvgpr_read_b32 v231, a236\n"
    "v_accvgpr_read_b32 v232, a225\n"
    "v_mfma_f32_16x16x32_bf16 v[36:39], v[212:215], a[36:39], v[36:39]\n"
    "v_accvgpr_read_b32 v233, a229\n"
    "v_accvgpr_read_b32 v234, a233\n"
    "v_mfma_f32_16x16x32_bf16 v[40:43], v[216:219], a[36:39], v[40:43]\n"
    "v_accvgpr_read_b32 v235, a237\n"
    "v_accvgpr_read_b32 v236, a226\n"
    "v_mfma_f32_16x16x32_bf16 v[44:47], v[220:223], a[36:39], v[44:47]\n"
    "v_accvgpr_read_b32 v237, a230\n"
    "v_accvgpr_read_b32 v238, a234\n"
    "v_mfma_f32_16x16x32_bf16 v[48:51], v[224:227], a[36:39], v[48:51]\n"
    "buffer_load_dwordx4 v[92:95], v2, s[12:15], s49 offen offset:512 sc1\n"
    "v_accvgpr_read_b32 v239, a238\n"
    "v_accvgpr_read_b32 v240, a227\n"
    "v_mfma_f32_16x16x32_bf16 v[52:55], v[212:215], a[40:43], v[52:55]\n"
    "v_accvgpr_read_b32 v241, a231\n"
    "v_accvgpr_read_b32 v242, a235\n"
    "v_mfma_f32_16x16x32_bf16 v[56:59], v[216:219], a[40:43], v[56:59]\n"
    "v_accvgpr_read_b32 v243, a239\n"
    "v_max3_u32 v9, v228, v229, v230\n"
    "v_mfma_f32_16x16x32_bf16 v[60:63], v[220:223], a[40:43], v[60:63]\n"
    "v_max3_u32 v9, v231, v232, v9\n"
    "v_max3_u32 v9, v233, v234, v9\n"
    "v_mfma_f32_16x16x32_bf16 v[64:67], v[224:227], a[40:43], v[64:67]\n"
    "buffer_load_dwordx4 v[96:99], v2, s[12:15], s49 offen offset:768 sc1\n"
    "v_max3_u32 v9, v235, v236, v9\n"
    "v_max3_u32 v9, v237, v238, v9\n"
    "v_mfma_f32_16x16x32_bf16 v[68:71], v[212:215], a[44:47], v[68:71]\n"
    "v_max3_u32 v9, v239, v240, v9\n"
    "v_max3_u32 v9, v241, v242, v9\n"
    "v_mfma_f32_16x16x32_bf16 v[72:75], v[216:219], a[44:47], v[72:75]\n"
    "v_max_u32 v9, v243, v9\n"
    "v_cmp_eq_u32 vcc, -1, v9\n"
    "v_mfma_f32_16x16x32_bf16 v[76:79], v[220:223], a[44:47], v[76:79]\n"
    "v_mfma_f32_16x16x32_bf16 v[80:83], v[224:227], a[44:47], v[80:83]\n"
    "s_cbranch_vccnz Lk_Hf_32\n"
    "Lk_Hr_33:\n"
    "v_mfma_f32_16x16x32_bf16 v[20:23], v[228:231], a[48:51], v[20:23]\n"
    "s_add_u32 s49, s42, 28672\n"
    "buffer_load_dwordx4 v[100:103], v2, s[12:15], s49 offen offset:0 sc1\n"
    "v_mfma_f32_16x16x32_bf16 v[24:27], v[232:235], a[48:51], v[24:27]\n"
    "s_waitcnt vmcnt(9)\n"
    "v_accvgpr_read_b32 v212, a240\n"
    "v_mfma_f32_16x16x32_bf16 v[28:31], v[236:239], a[48:51], v[28:31]\n"
    "v_accvgpr_read_b32 v213, a244\n"
    "v_accvgpr_read_b32 v214, a248\n"
    "v_mfma_f32_16x16x32_bf16 v[32:35], v[240:243], a[48:51], v[32:35]\n"
    "buffer_load_dwordx4 v[104:107], v2, s[12:15], s49 offen offset:256 sc1\n"
    "v_accvgpr_read_b32 v215, a252\n"
    "v_accvgpr_read_b32 v216, a241\n"
    "v_mfma_f32_16x16x32_bf16 v[36:39], v[228:231], a[52:55], v[36:39]\n"
    "v_accvgpr_read_b32 v217, a245\n"
    "v_accvgpr_read_b32 v218, a249\n"
    "v_mfma_f32_16x16x32_bf16 v[40:43], v[232:235], a[52:55], v[40:43]\n"
    "v_accvgpr_read_b32 v219, a253\n"
    "v_accvgpr_read_b32 v220, a242\n"
    "v_mfma_f32_16x16x32_bf16 v[44:47], v[236:239], a[52:55], v[44:47]\n"
    "v_accvgpr_read_b32 v221, a246\n"
    "v_accvgpr_read_b32 v222, a250\n"
    "v_mfma_f32_16x16x32_bf16 v[48:51], v[240:243], a[52:55], v[48:51]\n"
    "buffer_load_dwordx4 v[108:111], v2, s[12:15], s49 offen offset:512 sc1\n"
    "v_accvgpr_read_b32 v223, a254\n"
    "v_accvgpr_read_b32 v224, a243\n"
    "v_mfma_f32_16x16x32_bf16 v[52:55], v[228:231], a[56:59], v[52:55]\n"
    "v_accvgpr_read_b32 v225, a247\n"
    "v_accvgpr_read_b32 v226, a251\n"
    "v_mfma_f32_16x16x32_bf16 v[56:59], v[232:235], a[56:59], v[56:59]\n"
    "v_accvgpr_read_b32 v227, a255\n"
    "v_max3_u32 v9, v212, v213, v214\n"
    "v_mfma_f32_16x16x32_bf16 v[60:63], v[236:239], a[56:59], v[60:63]\n"
    "v_max3_u32 v9, v215, v216, v9\n"
    "v_max3_u32 v9, v217, v218, v9\n"
    "v_mfma_f32_16x16x32_bf16 v[64:67], v[240:243], a[56:59], v[64:67]\n"
    "buffer_load_dwordx4 v[112:115], v2, s[12:15], s49 offen offset:768 sc1\n"
    "v_max3_u32 v9, v219, v220, v9\n"
    "v_max3_u32 v9, v221, v222, v9\n"
    "v_mfma_f32_16x16x32_bf16 v[68:71], v[228:231], a[60:63], v[68:71]\n"
    "v_max3_u32 v9, v223, v224, v9\n"
    "v_max3_u32 v9, v225, v226, v9\n"
    "v_mfma_f32_16x16x32_bf16 v[72:75], v[232:235], a[60:63], v[72:75]\n"
    "v_max_u32 v9, v227, v9\n"
    "v_cmp_eq_u32 vcc, -1, v9\n"
    "v_mfma_f32_16x16x32_bf16 v[76:79], v[236:239], a[60:63], v[76:79]\n"
    "v_mfma_f32_16x16x32_bf16 v[80:83], v[240:243], a[60:63], v[80:83]\n"
    "s_cbranch_vccnz Lk_Hf_34\n"
    "Lk_Hr_35:\n"
    "v_mfma_f32_16x16x32_bf16 v[20:23], v[212:215], a[64:67], v[20:23]\n"
    "s_add_u32 s49, s42, 32768\n"
    "buffer_load_dwordx4 v[116:119], v2, s[12:15], s49 offen offset:0 sc1\n"
    "v_mfma_f32_16x16x32_bf16 v[24:27], v[216:219], a[64:67], v[24:27]\n"
    "s_waitcnt vmcnt(9)\n"
    "v_mov_b32 v228, v148\n"
    "v_mfma_f32_16x16x32_bf16 v[28:31], v[220:223], a[64:67], v[28:31]\n"
    "v_mov_b32 v229, v152\n"
    "v_mov_b32 v230, v156\n"
    "v_mfma_f32_16x16x32_bf16 v[32:35], v[224:227], a[64:67], v[32:35]\n"
    "buffer_load_dwordx4 v[120:123], v2, s[12:15], s49 offen offset:256 sc1\n"
    "v_mov_b32 v231, v160\n"
    "v_mov_b32 v232, v149\n"
    "v_mfma_f32_16x16x32_bf16 v[36:39], v[212:215], a[68:71], v[36:39]\n"
    "v_mov_b32 v233, v153\n"
    "v_mov_b32 v234, v157\n"
    "v_mfma_f32_16x16x32_bf16 v[40:43], v[216:219], a[68:71], v[40:43]\n"
    "v_mov_b32 v235, v161\n"
    "v_mov_b32 v236, v150\n"
    "v_mfma_f32_16x16x32_bf16 v[44:47], v[220:223], a[68:71], v[44:47]\n"
    "v_mov_b32 v237, v154\n"
    "v_mov_b32 v238, v158\n"
    "v_mfma_f32_16x16x32_bf16 v[48:51], v[224:227], a[68:71], v[48:51]\n"
    "buffer_load_dwordx4 v[124:127], v2, s[12:15], s49 offen offset:512 sc1\n"
    "v_mov_b32 v239, v162\n"
    "v_mov_b32 v240, v151\n"
    "v_mfma_f32_16x16x32_bf16 v[52:55], v[212:215], a[72:75], v[52:55]\n"
    "v_mov_b32 v241, v155\n"
    "v_mov_b32 v242, v159\n"
    "v_mfma_f32_16x16x32_bf16 v[56:59], v[216:219], a[72:75], v[56:59]\n"
    "v_mov_b32 v243, v163\n"
    "v_max3_u32 v9, v228, v229, v230\n"
    "v_mfma_f32_16x16x32_bf16 v[60:63], v[220:223], a[72:75], v[60:63]\n"
    "v_max3_u32 v9, v231, v232, v9\n"
    "v_max3_u32 v9, v233, v234, v9\n"
    "v_mfma_f32_16x16x32_bf16 v[64:67], v[224:227], a[72:75], v[64:67]\n"
    "buffer_load_dwordx4 v[128:131], v2, s[12:15], s49 offen offset:768 sc1\n"
    "v_max3_u32 v9, v235, v236, v9\n"
    "v_max3_u32 v9, v237, v238, v9\n"
    "v_mfma_f32_16x16x32_bf16 v[68:71], v[212:215], a[76:79], v[68:71]\n"
    "v_max3_u32 v9, v239, v240, v9\n"
    "v_max3_u32 v9, v241, v242, v9\n"
    "v_mfma_f32_16x16x32_bf16 v[72:75], v[216:219], a[76:79], v[72:75]\n"
    "v_max_u32 v9, v243, v9\n"
    "v_cmp_eq_u32 vcc, -1, v9\n"
    "v_mfma_f32_16x16x32_bf16 v[76:79], v[220:223], a[76:79], v[76:79]\n"
    "v_mfma_f32_16x16x32_bf16 v[80:83], v[224:227], a[76:79], v[80:83]\n"
    "s_cbranch_vccnz Lk_Hf_36\n"
    "Lk_Hr_37:\n"
    "v_mfma_f32_16x16x32_bf16 v[20:23], v[228:231], a[80:83], v[20:23]\n"
    "s_add_u32 s49, s42, 36864\n"
    "buffer_load_dwordx4 v[164:167], v2, s[12:15], s49 offen offset:0 sc1\n"
    "v_mfma_f32_16x16x32_bf16 v[24:27], v[232:235], a[80:83], v[24:27]\n"
    "s_waitcnt vmcnt(9)\n"
    "v_mov_b32 v212, v84\n"
    "v_mfma_f32_16x16x32_bf16 v[28:31], v[236:239], a[80:83], v[28:31]\n"
    "v_mov_b32 v213, v88\n"
    "v_mov_b32 v214, v92\n"
    "v_mfma_f32_16x16x32_bf16 v[32:35], v[240:243], a[80:83], v[32:35]\n"
    "buffer_load_dwordx4 v[168:171], v2, s[12:15], s49 offen offset:256 sc1\n"
    "v_mov_b32 v215, v96\n"
    "v_mov_b32 v216, v85\n"
    "v_mfma_f32_16x16x32_bf16 v[36:39], v[228:231], a[84:87], v[36:39]\n"
    "v_mov_b32 v217, v89\n"
    "v_mov_b32 v218, v93\n"
    "v_mfma_f32_16x16x32_bf16 v[40:43], v[232:235], a[84:87], v[40:43]\n"
    "v_mov_b32 v219, v97\n"
    "v_mov_b32 v220, v86\n"
    "v_mfma_f32_16x16x32_bf16 v[44:47], v[236:239], a[84:87], v[44:47]\n"
    "v_mov_b32 v221, v90\n"
    "v_mov_b32 v222, v94\n"
    "v_mfma_f32_16x16x32_bf16 v[48:51], v[240:243], a[84:87], v[48:51]\n"
    "buffer_load_dwordx4 v[172:175], v2, s[12:15], s49 offen offset:512 sc1\n"
    "v_mov_b32 v223, v98\n"
    "v_mov_b32 v224, v87\n"
    "v_mfma_f32_16x16x32_bf16 v[52:55], v[228:231], a[88:91], v[52:55]\n"
    "v_mov_b32 v225, v91\n"
    "v_mov_b32 v226, v95\n"
    "v_mfma_f32_16x16x32_bf16 v[56:59], v[232:235], a[88:91], v[56:59]\n"
    "v_mov_b32 v227, v99\n"
    "v_max3_u32 v9, v212, v213, v214\n"
    "v_mfma_f32_16x16x32_bf16 v[60:63], v[236:239], a[88:91], v[60:63]\n"
    "v_max3_u32 v9, v215, v216, v9\n"
    "v_max3_u32 v9, v217, v218, v9\n"
    "v_mfma_f32_16x16x32_bf16 v[64:67], v[240:243], a[88:91], v[64:67]\n"
    "buffer_load_dwordx4 v[176:179], v2, s[12:15], s49 offen offset:768 sc1\n"
    "v_max3_u32 v9, v219, v220, v9\n"
    "v_max3_u32 v9, v221, v222, v9\n"
    "v_mfma_f32_16x16x32_bf16 v[68:71], v[228:231], a[92:95], v[68:71]\n"
    "v_max3_u32 v9, v223, v224, v9\n"
    "v_max3_u32 v9, v225, v226, v9\n"
    "v_mfma_f32_16x16x32_bf16 v[72:75], v[232:235], a[92:95], v[72:75]\n"
    "v_max_u32 v9, v227, v9\n"
    "v_cmp_eq_u32 vcc, -1, v9\n"
    "v_mfma_f32_16x16x32_bf16 v[76:79], v[236:239], a[92:95], v[76:79]\n"
    "v_mfma_f32_16x16x32_bf16 v[80:83], v[240:243], a[92:95], v[80:83]\n"
    "s_cbranch_vccnz Lk_Hf_38\n"
    "Lk_Hr_39:\n"
    "v_mfma_f32_16x16x32_bf16 v[20:23], v[212:215], a[96:99], v[20:23]\n"
    "s_add_u32 s49, s42, 40960\n"
    "buffer_load_dwordx4 v[180:183], v2, s[12:15], s49 offen offset:0 sc1\n"
    "v_mfma_f32_16x16x32_bf16 v[24:27], v[216:219], a[96:99], v[24:27]\n"
    "s_waitcnt vmcnt(9)\n"
    "v_mov_b32 v228, v100\n"
    "v_mfma_f32_16x16x32_bf16 v[28:31], v[220:223], a[96:99], v[28:31]\n"
    "v_mov_b32 v229, v104\n"
    "v_mov_b32 v230, v108\n"
    "v_mfma_f32_16x16x32_bf16 v[32:35], v[224:227], a[96:99], v[32:35]\n"
    "buffer_load_dwordx4 v[184:187], v2, s[12:15], s49 offen offset:256 sc1\n"
    "v_mov_b32 v231, v112\n"
    "v_mov_b32 v232, v101\n"
    "v_mfma_f32_16x16x32_bf16 v[36:39], v[212:215], a[100:103], v[36:39]\n"
    "v_mov_b32 v233, v105\n"
    "v_mov_b32 v234, v109\n"
    "v_mfma_f32_16x16x32_bf16 v[40:43], v[216:219], a[100:103], v[40:43]\n"
    "v_mov_b32 v235, v113\n"
    "v_mov_b32 v236, v102\n"
    "v_mfma_f32_16x16x32_bf16 v[44:47], v[220:223], a[100:103], v[44:47]\n"
    "v_mov_b32 v237, v106\n"
    "v_mov_b32 v238, v110\n"
    "v_mfma_f32_16x16x32_bf16 v[48:51], v[224:227], a[100:103], v[48:51]\n"
    "buffer_load_dwordx4 v[188:191], v2, s[12:15], s49 offen offset:512 sc1\n"
    "v_mov_b32 v239, v114\n"
    "v_mov_b32 v240, v103\n"
    "v_mfma_f32_16x16x32_bf16 v[52:55], v[212:215], a[104:107], v[52:55]\n"
    "v_mov_b32 v241, v107\n"
    "v_mov_b32 v242, v111\n"
    "v_mfma_f32_16x16x32_bf16 v[56:59], v[216:219], a[104:107], v[56:59]\n"
    "v_mov_b32 v243, v115\n"
    "v_max3_u32 v9, v228, v229, v230\n"
    "v_mfma_f32_16x16x32_bf16 v[60:63], v[220:223], a[104:107], v[60:63]\n"
    "v_max3_u32 v9, v231, v232, v9\n"
    "v_max3_u32 v9, v233, v234, v9\n"
    "v_mfma_f32_16x16x32_bf16 v[64:67], v[224:227], a[104:107], v[64:67]\n"
    "buffer_load_dwordx4 v[192:195], v2, s[12:15], s49 offen offset:768 sc1\n"
    "v_max3_u32 v9, v235, v236, v9\n"
    "v_max3_u32 v9, v237, v238, v9\n"
    "v_mfma_f32_16x16x32_bf16 v[68:71], v[212:215], a[108:111], v[68:71]\n"
    "v_max3_u32 v9, v239, v240, v9\n"
    "v_max3_u32 v9, v241, v242, v9\n"
    "v_mfma_f32_16x16x32_bf16 v[72:75], v[216:219], a[108:111], v[72:75]\n"
    "v_max_u32 v9, v243, v9\n"
    "v_cmp_eq_u32 vcc, -1, v9\n"
    "v_mfma_f32_16x16x32_bf16 v[76:79], v[220:223], a[108:111], v[76:79]\n"
    "v_mfma_f32_16x16x32_bf16 v[80:83], v[224:227], a[108:111], v[80:83]\n"
    "s_cbranch_vccnz Lk_Hf_40\n"
    "Lk_Hr_41:\n"
    "v_mfma_f32_16x16x32_bf16 v[20:23], v[228:231], a[112:115], v[20:23]\n"
    "s_add_u32 s49, s42, 45056\n"
    "buffer_load_dwordx4 v[196:199], v2, s[12:15], s49 offen offset:0 sc1\n"
    "v_mfma_f32_16x16x32_bf16 v[24:27], v[232:235], a[112:115], v[24:27]\n"
    "s_waitcnt vmcnt(9)\n"
    "v_mov_b32 v212, v116\n"
    "v_mfma_f32_16x16x32_bf16 v[28:31], v[236:239], a[112:115], v[28:31]\n"
    "v_mov_b32 v213, v120\n"
    "v_mov_b32 v214, v124\n"
    "v_mfma_f32_16x16x32_bf16 v[32:35], v[240:243], a[112:115], v[32:35]\n"
    "buffer_load_dwordx4 v[200:203], v2, s[12:15], s49 offen offset:256 sc1\n"
    "v_mov_b32 v215, v128\n"
    "v_mov_b32 v216, v117\n"
    "v_mfma_f32_16x16x32_bf16 v[36:39], v[228:231], a[116:119], v[36:39]\n"
    "v_mov_b32 v217, v121\n"
    "v_mov_b32 v218, v125\n"
    "v_mfma_f32_16x16x32_bf16 v[40:43], v[232:235], a[116:119], v[40:43]\n"
    "v_mov_b32 v219, v129\n"
    "v_mov_b32 v220, v118\n"
    "v_mfma_f32_16x16x32_bf16 v[44:47], v[236:239], a[116:119], v[44:47]\n"
    "v_mov_b32 v221, v122\n"
    "v_mov_b32 v222, v126\n"
    "v_mfma_f32_16x16x32_bf16 v[48:51], v[240:243], a[116:119], v[48:51]\n"
    "buffer_load_dwordx4 v[204:207], v2, s[12:15], s49 offen offset:512 sc1\n"
    "v_mov_b32 v223, v130\n"
    "v_mov_b32 v224, v119\n"
    "v_mfma_f32_16x16x32_bf16 v[52:55], v[228:231], a[120:123], v[52:55]\n"
    "v_mov_b32 v225, v123\n"
    "v_mov_b32 v226, v127\n"
    "v_mfma_f32_16x16x32_bf16 v[56:59], v[232:235], a[120:123], v[56:59]\n"
    "v_mov_b32 v227, v131\n"
    "v_mfma_f32_16x16x32_bf16 v[60:63], v[236:239], a[120:123], v[60:63]\n"
    "v_mfma_f32_16x16x32_bf16 v[64:67], v[240:243], a[120:123], v[64:67]\n"
    "buffer_load_dwordx4 v[208:211], v2, s[12:15], s49 offen offset:768 sc1\n"
    "v_mfma_f32_16x16x32_bf16 v[68:71], v[228:231], a[124:127], v[68:71]\n"
    "v_mfma_f32_16x16x32_bf16 v[72:75], v[232:235], a[124:127], v[72:75]\n"
    "v_mfma_f32_16x16x32_bf16 v[76:79], v[236:239], a[124:127], v[76:79]\n"
    "v_mfma_f32_16x16x32_bf16 v[80:83], v[240:243], a[124:127], v[80:83]\n"
    "v_mfma_f32_16x16x32_bf16 v[20:23], v[212:215], a[128:131], v[20:23]\n"
    "s_add_u32 s49, s42, 49152\n"
    "buffer_load_dwordx4 a[224:227], v2, s[12:15], s49 offen offset:0 sc1\n"
    "v_mfma_f32_16x16x32_bf16 v[24:27], v[216:219], a[128:131], v[24:27]\n"
    "s_waitcnt vmcnt(9)\n"
    "v_mov_b32 v228, v164\n"
    "v_mfma_f32_16x16x32_bf16 v[28:31], v[220:223], a[128:131], v[28:31]\n"
    "v_mov_b32 v229, v168\n"
    "v_mov_b32 v230, v172\n"
    "v_mfma_f32_16x16x32_bf16 v[32:35], v[224:227], a[128:131], v[32:35]\n"
    "buffer_load_dwordx4 a[228:231], v2, s[12:15], s49 offen offset:256 sc1\n"
    "v_mov_b32 v231, v176\n"
    "v_mov_b32 v232, v165\n"
    "v_mfma_f32_16x16x32_bf16 v[36:39], v[212:215], a[132:135], v[36:39]\n"
    "v_mov_b32 v233, v169\n"
    "v_mov_b32 v234, v173\n"
    "v_mfma_f32_16x16x32_bf16 v[40:43], v[216:219], a[132:135], v[40:43]\n"
    "v_mov_b32 v235, v177\n"
    "v_mov_b32 v236, v166\n"
    "v_mfma_f32_16x16x32_bf16 v[44:47], v[220:223], a[132:135], v[44:47]\n"
    "v_mov_b32 v237, v170\n"
    "v_mov_b32 v238, v174\n"
    "v_mfma_f32_16x16x32_bf16 v[48:51], v[224:227], a[132:135], v[48:51]\n"
    "buffer_load_dwordx4 a[232:235], v2, s[12:15], s49 offen offset:512 sc1\n"
    "v_mov_b32 v239, v178\n"
    "v_mov_b32 v240, v167\n"
    "v_mfma_f32_16x16x32_bf16 v[52:55], v[212:215], a[136:139], v[52:55]\n"
    "v_mov_b32 v241, v171\n"
    "v_mov_b32 v242, v175\n"
    "v_mfma_f32_16x16x32_bf16 v[56:59], v[216:219], a[136:139], v[56:59]\n"
    "v_mov_b32 v243, v179\n"
    "v_mfma_f32_16x16x32_bf16 v[60:63], v[220:223], a[136:139], v[60:63]\n"
    "v_mfma_f32_16x16x32_bf16 v[64:67], v[224:227], a[136:139], v[64:67]\n"
    "buffer_load_dwordx4 a[236:239], v2, s[12:15], s49 offen offset:768 sc1\n"
    "v_mfma_f32_16x16x32_bf16 v[68:71], v[212:215], a[140:143], v[68:71]\n"
    "v_mfma_f32_16x16x32_bf16 v[72:75], v[216:219], a[140:143], v[72:75]\n"
    "v_mfma_f32_16x16x32_bf16 v[76:79], v[220:223], a[140:143], v[76:79]\n"
    "v_mfma_f32_16x16x32_bf16 v[80:83], v[224:227], a[140:143], v[80:83]\n"
    "v_mfma_f32_16x16x32_bf16 v[20:23], v[228:231], a[144:147], v[20:23]\n"
    "s_add_u32 s49, s42, 53248\n"
    "buffer_load_dwordx4 a[240:243], v2, s[12:15], s49 offen offset:0 sc1\n"
    "v_mfma_f32_16x16x32_bf16 v[24:27], v[232:235], a[144:147], v[24:27]\n"
    "s_waitcnt vmcnt(9)\n"
    "v_mov_b32 v212, v180\n"
    "v_mfma_f32_16x16x32_bf16 v[28:31], v[236:239], a[144:147], v[28:31]\n"
    "v_mov_b32 v213, v184\n"
    "v_mov_b32 v214, v188\n"
    "v_mfma_f32_16x16x32_bf16 v[32:35], v[240:243], a[144:147], v[32:35]\n"
    "buffer_load_dwordx4 a[244:247], v2, s[12:15], s49 offen offset:256 sc1\n"
    "v_mov_b32 v215, v192\n"
    "v_mov_b32 v216, v181\n"
    "v_mfma_f32_16x16x32_bf16 v[36:39], v[228:231], a[148:151], v[36:39]\n"
    "v_mov_b32 v217, v185\n"
    "v_mov_b32 v218, v189\n"
    "v_mfma_f32_16x16x32_bf16 v[40:43], v[232:235], a[148:151], v[40:43]\n"
    "v_mov_b32 v219, v193\n"
    "v_mov_b32 v220, v182\n"
    "v_mfma_f32_16x16x32_bf16 v[44:47], v[236:239], a[148:151], v[44:47]\n"
    "v_mov_b32 v221, v186\n"
    "v_mov_b32 v222, v190\n"
    "v_mfma_f32_16x16x32_bf16 v[48:51], v[240:243], a[148:151], v[48:51]\n"
    "buffer_load_dwordx4 a[248:251], v2, s[12:15], s49 offen offset:512 sc1\n"
    "v_mov_b32 v223, v194\n"
    "v_mov_b32 v224, v183\n"
    "v_mfma_f32_16x16x32_bf16 v[52:55], v[228:231], a[152:155], v[52:55]\n"
    "v_mov_b32 v225, v187\n"
    "v_mov_b32 v226, v191\n"
    "v_mfma_f32_16x16x32_bf16 v[56:59], v[232:235], a[152:155], v[56:59]\n"
    "v_mov_b32 v227, v195\n"
    "v_mfma_f32_16x16x32_bf16 v[60:63], v[236:239], a[152:155], v[60:63]\n"
    "v_mfma_f32_16x16x32_bf16 v[64:67], v[240:243], a[152:155], v[64:67]\n"
    "buffer_load_dwordx4 a[252:255], v2, s[12:15], s49 offen offset:768 sc1\n"
    "v_mfma_f32_16x16x32_bf16 v[68:71], v[228:231], a[156:159], v[68:71]\n"
    "v_mfma_f32_16x16x32_bf16 v[72:75], v[232:235], a[156:159], v[72:75]\n"
    "v_mfma_f32_16x16x32_bf16 v[76:79], v[236:239], a[156:159], v[76:79]\n"
    "v_mfma_f32_16x16x32_bf16 v[80:83], v[240:243], a[156:159], v[80:83]\n"
    "v_mfma_f32_16x16x32_bf16 v[20:23], v[212:215], a[160:163], v[20:23]\n"
    "v_mfma_f32_16x16x32_bf16 v[24:27], v[216:219], a[160:163], v[24:27]\n"
    "s_waitcnt vmcnt(8)\n"
    "v_mov_b32 v228, v196\n"
    "v_mov_b32 v229, v200\n"
    "v_mfma_f32_16x16x32_bf16 v[28:31], v[220:223], a[160:163], v[28:31]\n"
    "v_mov_b32 v230, v204\n"
    "v_mov_b32 v231, v208\n"
    "v_mov_b32 v232, v197\n"
    "v_mfma_f32_16x16x32_bf16 v[32:35], v[224:227], a[160:163], v[32:35]\n"
    "v_mov_b32 v233, v201\n"
    "v_mov_b32 v234, v205\n"
    "v_mov_b32 v235, v209\n"
    "v_mfma_f32_16x16x32_bf16 v[36:39], v[212:215], a[164:167], v[36:39]\n"
    "v_mov_b32 v236, v198\n"
    "v_mov_b32 v237, v202\n"
    "v_mov_b32 v238, v206\n"
    "v_mfma_f32_16x16x32_bf16 v[40:43], v[216:219], a[164:167], v[40:43]\n"
    "v_mov_b32 v239, v210\n"
    "v_mov_b32 v240, v199\n"
    "v_mov_b32 v241, v203\n"
    "v_mfma_f32_16x16x32_bf16 v[44:47], v[220:223], a[164:167], v[44:47]\n"
    "v_mov_b32 v242, v207\n"
    "v_mov_b32 v243, v211\n"
    "v_lshrrev_b32 v6, 4, v0\n"
    "v_mfma_f32_16x16x32_bf16 v[48:51], v[224:227], a[164:167], v[48:51]\n"
    "v_mul_u32_u24 v13, 272, v6\n"
    "s_movk_i32 s51, 4096\n"
    "v_add3_u32 v13, v13, v3, s51\n"
    "v_mfma_f32_16x16x32_bf16 v[52:55], v[212:215], a[168:171], v[52:55]\n"
    "v_and_b32 v6, 7, v0\n"
    "v_bfe_u32 v7, v0, 3, 4\n"
    "v_lshrrev_b32 v8, 7, v0\n"
    "v_mfma_f32_16x16x32_bf16 v[56:59], v[216:219], a[168:171], v[56:59]\n"
    "v_mul_u32_u24 v14, 1088, v6\n"
    "v_lshl_add_u32 v14, v7, 4, v14\n"
    "v_lshl_add_u32 v14, v8, 2, v14\n"
    "v_mfma_f32_16x16x32_bf16 v[60:63], v[220:223], a[168:171], v[60:63]\n"
    "v_add_u32 v14, s51, v14\n"
    "v_lshl_add_u32 v10, v7, 3, v8\n"
    "v_lshlrev_b32 v10, 10, v10\n"
    "v_mfma_f32_16x16x32_bf16 v[64:67], v[224:227], a[168:171], v[64:67]\n"
    "v_lshl_add_u32 v10, v6, 4, v10\n"
    "v_bfe_u32 v7, v0, 3, 2\n"
    "v_lshl_add_u32 v7, v8, 2, v7\n"
    "v_mfma_f32_16x16x32_bf16 v[68:71], v[212:215], a[172:175], v[68:71]\n"
    "v_bfe_u32 v8, v0, 1, 2\n"
    "v_lshl_add_u32 v7, v7, 2, v8\n"
    "v_mfma_f32_16x16x32_bf16 v[72:75], v[216:219], a[172:175], v[72:75]\n"
    "v_mfma_f32_16x16x32_bf16 v[76:79], v[220:223], a[172:175], v[76:79]\n"
    "v_mfma_f32_16x16x32_bf16 v[80:83], v[224:227], a[172:175], v[80:83]\n"
    "v_mfma_f32_16x16x32_bf16 v[20:23], v[228:231], a[176:179], v[20:23]\n"
    "v_mfma_f32_16x16x32_bf16 v[24:27], v[232:235], a[176:179], v[24:27]\n"
    "s_waitcnt vmcnt(4)\n"
    "v_accvgpr_read_b32 v212, a224\n"
    "v_mfma_f32_16x16x32_bf16 v[28:31], v[236:239], a[176:179], v[28:31]\n"
    "v_accvgpr_read_b32 v213, a228\n"
    "v_accvgpr_read_b32 v214, a232\n"
    "v_mfma_f32_16x16x32_bf16 v[32:35], v[240:243], a[176:179], v[32:35]\n"
    "v_accvgpr_read_b32 v215, a236\n"
    "v_accvgpr_read_b32 v216, a225\n"
    "v_mfma_f32_16x16x32_bf16 v[36:39], v[228:231], a[180:183], v[36:39]\n"
    "v_accvgpr_read_b32 v217, a229\n"
    "v_accvgpr_read_b32 v218, a233\n"
    "v_mfma_f32_16x16x32_bf16 v[40:43], v[232:235], a[180:183], v[40:43]\n"
    "v_accvgpr_read_b32 v219, a237\n"
    "v_accvgpr_read_b32 v220, a226\n"
    "v_mfma_f32_16x16x32_bf16 v[44:47], v[236:239], a[180:183], v[44:47]\n"
    "v_accvgpr_read_b32 v221, a230\n"
    "v_accvgpr_read_b32 v222, a234\n"
    "v_mfma_f32_16x16x32_bf16 v[48:51], v[240:243], a[180:183], v[48:51]\n"
    "v_accvgpr_read_b32 v223, a238\n"
    "v_accvgpr_read_b32 v224, a227\n"
    "v_mfma_f32_16x16x32_bf16 v[52:55], v[228:231], a[184:187], v[52:55]\n"
    "v_accvgpr_read_b32 v225, a231\n"
    "v_accvgpr_read_b32 v226, a235\n"
    "v_mfma_f32_16x16x32_bf16 v[56:59], v[232:235], a[184:187], v[56:59]\n"
    "v_accvgpr_read_b32 v227, a239\n"
    "v_mul_u32_u24 v11, 320, v7\n"
    "v_mfma_f32_16x16x32_bf16 v[60:63], v[236:239], a[184:187], v[60:63]\n"
    "v_bfe_u32 v7, v0, 5, 2\n"
    "v_lshl_add_u32 v11, v7, 6, v11\n"
    "v_mfma_f32_16x16x32_bf16 v[64:67], v[240:243], a[184:187], v[64:67]\n"
    "v_and_b32 v7, 1, v0\n"
    "v_lshl_add_u32 v11, v7, 5, v11\n"
    "v_mfma_f32_16x16x32_bf16 v[68:71], v[228:231], a[188:191], v[68:71]\n"
    "v_add_u32 v11, 28672, v11\n"
    "v_add_u32 v15, 41088, v11\n"
    "v_mfma_f32_16x16x32_bf16 v[72:75], v[232:235], a[188:191], v[72:75]\n"
    "v_mfma_f32_16x16x32_bf16 v[76:79], v[236:239], a[188:191], v[76:79]\n"
    "v_mfma_f32_16x16x32_bf16 v[80:83], v[240:243], a[188:191], v[80:83]\n"
    "v_mfma_f32_16x16x32_bf16 v[20:23], v[212:215], a[192:195], v[20:23]\n"
    "v_mfma_f32_16x16x32_bf16 v[24:27], v[216:219], a[192:195], v[24:27]\n"
    "s_waitcnt vmcnt(0)\n"
    "v_accvgpr_read_b32 v228, a240\n"
    "v_mfma_f32_16x16x32_bf16 v[28:31], v[220:223], a[192:195], v[28:31]\n"
    "v_accvgpr_read_b32 v229, a244\n"
    "v_accvgpr_read_b32 v230, a248\n"
    "v_mfma_f32_16x16x32_bf16 v[32:35], v[224:227], a[192:195], v[32:35]\n"
    "v_accvgpr_read_b32 v231, a252\n"
    "v_accvgpr_read_b32 v232, a241\n"
    "v_mfma_f32_16x16x32_bf16 v[36:39], v[212:215], a[196:199], v[36:39]\n"
    "v_accvgpr_read_b32 v233, a245\n"
    "v_accvgpr_read_b32 v234, a249\n"
    "v_mfma_f32_16x16x32_bf16 v[40:43], v[216:219], a[196:199], v[40:43]\n"
    "v_accvgpr_read_b32 v235, a253\n"
    "v_accvgpr_read_b32 v236, a242\n"
    "v_mfma_f32_16x16x32_bf16 v[44:47], v[220:223], a[196:199], v[44:47]\n"
    "v_accvgpr_read_b32 v237, a246\n"
    "v_accvgpr_read_b32 v238, a250\n"
    "v_mfma_f32_16x16x32_bf16 v[48:51], v[224:227], a[196:199], v[48:51]\n"
    "v_accvgpr_read_b32 v239, a254\n"
    "v_accvgpr_read_b32 v240, a243\n"
    "v_mfma_f32_16x16x32_bf16 v[52:55], v[212:215], a[200:203], v[52:55]\n"
    "v_accvgpr_read_b32 v241, a247\n"
    "v_accvgpr_read_b32 v242, a251\n"
    "v_mfma_f32_16x16x32_bf16 v[56:59], v[216:219], a[200:203], v[56:59]\n"
    "v_accvgpr_read_b32 v243, a255\n"
    "v_mfma_f32_16x16x32_bf16 v[60:63], v[220:223], a[200:203], v[60:63]\n"
    "v_mfma_f32_16x16x32_bf16 v[64:67], v[224:227], a[200:203], v[64:67]\n"
    "v_mfma_f32_16x16x32_bf16 v[68:71], v[212:215], a[204:207], v[68:71]\n"
    "v_mfma_f32_16x16x32_bf16 v[72:75], v[216:219], a[204:207], v[72:75]\n"
    "v_mfma_f32_16x16x32_bf16 v[76:79], v[220:223], a[204:207], v[76:79]\n"
    "v_mfma_f32_16x16x32_bf16 v[80:83], v[224:227], a[204:207], v[80:83]\n"
    "v_mfma_f32_16x16x32_bf16 v[20:23], v[228:231], a[208:211], v[20:23]\n"
    "v_max3_u32 v9, v244, v245, v246\n"
    "v_mfma_f32_16x16x32_bf16 v[24:27], v[232:235], a[208:211], v[24:27]\n"
    "v_max3_u32 v9, v247, v248, v9\n"
    "v_max3_u32 v9, v249, v250, v9\n"
    "v_mfma_f32_16x16x32_bf16 v[28:31], v[236:239], a[208:211], v[28:31]\n"
    "v_max_u32 v9, v251, v9\n"
    "v_cmp_eq_u32 vcc, -1, v9\n"
    "v_mfma_f32_16x16x32_bf16 v[32:35], v[240:243], a[208:211], v[32:35]\n"
    "v_mfma_f32_16x16x32_bf16 v[36:39], v[228:231], a[212:215], v[36:39]\n"
    "s_cbranch_vccnz Lk_resf_42\n"
    "Lk_resr_43:\n"
    "ds_write_b128 v13, v[244:247]\n"
    "v_mfma_f32_16x16x32_bf16 v[40:43], v[232:235], a[212:215], v[40:43]\n"
    "ds_write_b128 v13, v[248:251] offset:4352\n"
    "v_mfma_f32_16x16x32_bf16 v[44:47], v[236:239], a[212:215], v[44:47]\n"
    "v_mfma_f32_16x16x32_bf16 v[48:51], v[240:243], a[212:215], v[48:51]\n"
    "v_mfma_f32_16x16x32_bf16 v[52:55], v[228:231], a[216:219], v[52:55]\n"
    "v_cmp_u_f32 vcc, v20, v21\n"
    "v_mfma_f32_16x16x32_bf16 v[56:59], v[232:235], a[216:219], v[56:59]\n"
    "v_cmp_u_f32 s[26:27], v22, v23\n"
    "s_nop 0\n"
    "s_or_b64 vcc, vcc, s[26:27]\n"
    "v_mfma_f32_16x16x32_bf16 v[60:63], v[236:239], a[216:219], v[60:63]\n"
    "v_cmp_u_f32 s[26:27], v24, v25\n"
    "s_nop 0\n"
    "s_or_b64 vcc, vcc, s[26:27]\n"
    "v_mfma_f32_16x16x32_bf16 v[64:67], v[240:243], a[216:219], v[64:67]\n"
    "v_cmp_u_f32 s[26:27], v26, v27\n"
    "s_nop 0\n"
    "s_or_b64 vcc, vcc, s[26:27]\n"
    "v_mfma_f32_16x16x32_bf16 v[68:71], v[228:231], a[220:223], v[68:71]\n"
    "v_cmp_u_f32 s[26:27], v28, v29\n"
    "s_nop 0\n"
    "s_or_b64 vcc, vcc, s[26:27]\n"
    "v_mfma_f32_16x16x32_bf16 v[72:75], v[232:235], a[220:223], v[72:75]\n"
    "v_cmp_u_f32 s[26:27], v30, v31\n"
    "s_nop 0\n"
    "s_or_b64 vcc, vcc, s[26:27]\n"
    "v_mfma_f32_16x16x32_bf16 v[76:79], v[236:239], a[220:223], v[76:79]\n"
    "v_cmp_u_f32 s[26:27], v32, v33\n"
    "s_nop 0\n"
    "s_or_b64 vcc, vcc, s[26:27]\n"
    "v_mfma_f32_16x16x32_bf16 v[80:83], v[240:243], a[220:223], v[80:83]\n"
    "v_cmp_u_f32 s[26:27], v34, v35\n"
    "s_nop 0\n"
    "s_or_b64 vcc, vcc, s[26:27]\n"
    "s_cbranch_vccnz Lk_p4redo\n"
    "Lk_p4ok:\n"
    "s_mov_b64 s[28:29], s[38:39]\n"
    "s_mov_b32 s30, 2097152\n"
    "s_mov_b32 s31, 0x20000\n"
    "s_mul_i32 s49, s5, 20544\n"
    "s_add_u32 s49, s49, 28672\n"
    "s_sub_u32 m0, s49, 28672\n"
    "s_waitcnt lgkmcnt(0)\n"
    "s_barrier\n"
    "ds_read_b32 v212, v14 offset:0\n"
    "ds_read_b32 v213, v14 offset:272\n"
    "ds_read_b32 v214, v14 offset:544\n"
    "ds_read_b32 v215, v14 offset:816\n"
    "ds_read_b32 v216, v14 offset:8\n"
    "ds_read_b32 v217, v14 offset:280\n"
    "ds_read_b32 v218, v14 offset:552\n"
    "ds_read_b32 v219, v14 offset:824\n"
    "ds_write_addtid_b32 v20 offset:28672\n"
    "ds_write_addtid_b32 v21 offset:29952\n"
    "ds_write_addtid_b32 v22 offset:31232\n"
    "ds_write_addtid_b32 v23 offset:32512\n"
    "ds_write_addtid_b32 v24 offset:33792\n"
    "ds_write_addtid_b32 v25 offset:35072\n"
    "ds_write_addtid_b32 v26 offset:36352\n"
    "ds_write_addtid_b32 v27 offset:37632\n"
    "ds_write_addtid_b32 v28 offset:38912\n"
    "ds_write_addtid_b32 v29 offset:40192\n"
    "ds_write_addtid_b32 v30 offset:41472\n"
    "ds_write_addtid_b32 v31 offset:42752\n"
    "ds_write_addtid_b32 v32 offset:44032\n"
    "ds_write_addtid_b32 v33 offset:45312\n"
    "ds_write_addtid_b32 v34 offset:46592\n"
    "ds_write_addtid_b32 v35 offset:47872\n"
    "ds_write_addtid_b32 v36 offset:28992\n"
    "ds_write_addtid_b32 v37 offset:30272\n"
    "ds_write_addtid_b32 v38 offset:31552\n"
    "ds_write_addtid_b32 v39 offset:32832\n"
    "ds_write_addtid_b32 v40 offset:34112\n"
    "ds_write_addtid_b32 v41 offset:35392\n"
    "ds_write_addtid_b32 v42 offset:36672\n"
    "ds_write_addtid_b32 v43 offset:37952\n"
    "ds_write_addtid_b32 v44 offset:39232\n"
    "ds_write_addtid_b32 v45 offset:40512\n"
    "ds_write_addtid_b32 v46 offset:41792\n"
    "ds_write_addtid_b32 v47 offset:43072\n"
    "ds_write_addtid_b32 v48 offset:44352\n"
    "ds_write_addtid_b32 v49 offset:45632\n"
    "ds_write_addtid_b32 v50 offset:46912\n"
    "ds_write_addtid_b32 v51 offset:48192\n"
    "ds_write_addtid_b32 v52 offset:29312\n"
    "ds_write_addtid_b32 v53 offset:30592\n"
    "ds_write_addtid_b32 v54 offset:31872\n"
    "ds_write_addtid_b32 v55 offset:33152\n"
    "ds_write_addtid_b32 v56 offset:34432\n"
    "ds_write_addtid_b32 v57 offset:35712\n"
    "ds_write_addtid_b32 v58 offset:36992\n"
    "ds_write_addtid_b32 v59 offset:38272\n"
    "ds_write_addtid_b32 v60 offset:39552\n"
    "ds_write_addtid_b32 v61 offset:40832\n"
    "ds_write_addtid_b32 v62 offset:42112\n"
    "ds_write_addtid_b32 v63 offset:43392\n"
    "ds_write_addtid_b32 v64 offset:44672\n"
    "ds_write_addtid_b32 v65 offset:45952\n"
    "ds_write_addtid_b32 v66 offset:47232\n"
    "ds_write_addtid_b32 v67 offset:48512\n"
    "ds_write_addtid_b32 v68 offset:29632\n"
    "ds_write_addtid_b32 v69 offset:30912\n"
    "ds_write_addtid_b32 v70 offset:32192\n"
    "ds_write_addtid_b32 v71 offset:33472\n"
    "ds_write_addtid_b32 v72 offset:34752\n"
    "ds_write_addtid_b32 v73 offset:36032\n"
    "ds_write_addtid_b32 v74 offset:37312\n"
    "ds_write_addtid_b32 v75 offset:38592\n"
    "ds_write_addtid_b32 v76 offset:39872\n"
    "ds_write_addtid_b32 v77 offset:41152\n"
    "ds_write_addtid_b32 v78 offset:42432\n"
    "ds_write_addtid_b32 v79 offset:43712\n"
    "ds_write_addtid_b32 v80 offset:44992\n"
    "ds_write_addtid_b32 v81 offset:46272\n"
    "ds_write_addtid_b32 v82 offset:47552\n"
    "ds_write_addtid_b32 v83 offset:48832\n"
    "s_waitcnt lgkmcnt(15)\n"
    "v_lshlrev_b32 v220, 16, v212\n"
    "v_and_b32 v221, s48, v212\n"
    "v_lshlrev_b32 v222, 16, v213\n"
    "v_and_b32 v223, s48, v213\n"
    "v_lshlrev_b32 v224, 16, v214\n"
    "v_and_b32 v225, s48, v214\n"
    "v_lshlrev_b32 v226, 16, v215\n"
    "v_and_b32 v227, s48, v215\n"
    "v_lshlrev_b32 v228, 16, v216\n"
    "v_and_b32 v229, s48, v216\n"
    "v_lshlrev_b32 v230, 16, v217\n"
    "v_and_b32 v231, s48, v217\n"
    "v_lshlrev_b32 v232, 16, v218\n"
    "v_and_b32 v233, s48, v218\n"
    "v_lshlrev_b32 v234, 16, v219\n"
    "v_and_b32 v235, s48, v219\n"
    "s_waitcnt lgkmcnt(0)\n"
    "s_barrier\n"
    "ds_read_b128 v[20:23], v11 offset:0\n"
    "ds_read_b128 v[24:27], v11 offset:16\n"
    "ds_read_b128 v[28:31], v11 offset:20544\n"
    "ds_read_b128 v[32:35], v11 offset:20560\n"
    "ds_read_b128 v[36:39], v15 offset:0\n"
    "ds_read_b128 v[40:43], v15 offset:16\n"
    "ds_read_b128 v[44:47], v15 offset:20544\n"
    "ds_read_b128 v[48:51], v15 offset:20560\n"
    "ds_read_b128 v[52:55], v11 offset:10240\n"
    "ds_read_b128 v[56:59], v11 offset:10256\n"
    "ds_read_b128 v[60:63], v11 offset:30784\n"
    "ds_read_b128 v[64:67], v11 offset:30800\n"
    "ds_read_b128 v[68:71], v15 offset:10240\n"
    "ds_read_b128 v[72:75], v15 offset:10256\n"
    "ds_read_b128 v[76:79], v15 offset:30784\n"
    "ds_read_b128 v[80:83], v15 offset:30800\n"
    "s_waitcnt lgkmcnt(15)\n"
    "v_pk_add_f32 v[220:221], v[220:221], v[20:21]\n"
    "v_pk_add_f32 v[222:223], v[222:223], v[22:23]\n"
    "s_waitcnt lgkmcnt(14)\n"
    "v_pk_add_f32 v[224:225], v[224:225], v[24:25]\n"
    "v_pk_add_f32 v[226:227], v[226:227], v[26:27]\n"
    "s_waitcnt lgkmcnt(13)\n"
    "v_pk_add_f32 v[220:221], v[220:221], v[28:29]\n"
    "v_pk_add_f32 v[222:223], v[222:223], v[30:31]\n"
    "s_waitcnt lgkmcnt(12)\n"
    "v_pk_add_f32 v[224:225], v[224:225], v[32:33]\n"
    "v_pk_add_f32 v[226:227], v[226:227], v[34:35]\n"
    "s_waitcnt lgkmcnt(11)\n"
    "v_pk_add_f32 v[220:221], v[220:221], v[36:37]\n"
    "v_pk_add_f32 v[222:223], v[222:223], v[38:39]\n"
    "s_waitcnt lgkmcnt(10)\n"
    "v_pk_add_f32 v[224:225], v[224:225], v[40:41]\n"
    "v_pk_add_f32 v[226:227], v[226:227], v[42:43]\n"
    "s_waitcnt lgkmcnt(9)\n"
    "v_pk_add_f32 v[220:221], v[220:221], v[44:45]\n"
    "v_pk_add_f32 v[222:223], v[222:223], v[46:47]\n"
    "s_waitcnt lgkmcnt(8)\n"
    "v_pk_add_f32 v[224:225], v[224:225], v[48:49]\n"
    "v_pk_add_f32 v[226:227], v[226:227], v[50:51]\n"
    "v_cvt_pk_bf16_f32 v212, v220, v221\n"
    "v_cvt_pk_bf16_f32 v213, v222, v223\n"
    "v_cvt_pk_bf16_f32 v214, v224, v225\n"
    "v_cvt_pk_bf16_f32 v215, v226, v227\n"
    "buffer_store_dwordx4 v[212:215], v10, s[28:31], s43 offen offset:0 sc1\n"
    "s_waitcnt lgkmcnt(7)\n"
    "v_pk_add_f32 v[228:229], v[228:229], v[52:53]\n"
    "v_pk_add_f32 v[230:231], v[230:231], v[54:55]\n"
    "s_waitcnt lgkmcnt(6)\n"
    "v_pk_add_f32 v[232:233], v[232:233], v[56:57]\n"
    "v_pk_add_f32 v[234:235], v[234:235], v[58:59]\n"
    "s_waitcnt lgkmcnt(5)\n"
    "v_pk_add_f32 v[228:229], v[228:229], v[60:61]\n"
    "v_pk_add_f32 v[230:231], v[230:231], v[62:63]\n"
    "s_waitcnt lgkmcnt(4)\n"
    "v_pk_add_f32 v[232:233], v[232:233], v[64:65]\n"
    "v_pk_add_f32 v[234:235], v[234:235], v[66:67]\n"
    "s_waitcnt lgkmcnt(3)\n"
    "v_pk_add_f32 v[228:229], v[228:229], v[68:69]\n"
    "v_pk_add_f32 v[230:231], v[230:231], v[70:71]\n"
    "s_waitcnt lgkmcnt(2)\n"
    "v_pk_add_f32 v[232:233], v[232:233], v[72:73]\n"
    "v_pk_add_f32 v[234:235], v[234:235], v[74:75]\n"
    "s_waitcnt lgkmcnt(1)\n"
    "v_pk_add_f32 v[228:229], v[228:229], v[76:77]\n"
    "v_pk_add_f32 v[230:231], v[230:231], v[78:79]\n"
    "s_waitcnt lgkmcnt(0)\n"
    "v_pk_add_f32 v[232:233], v[232:233], v[80:81]\n"
    "v_pk_add_f32 v[234:235], v[234:235], v[82:83]\n"
    "v_cvt_pk_bf16_f32 v216, v228, v229\n"
    "v_cvt_pk_bf16_f32 v217, v230, v231\n"
    "v_cvt_pk_bf16_f32 v218, v232, v233\n"
    "v_cvt_pk_bf16_f32 v219, v234, v235\n"
    "buffer_store_dwordx4 v[216:219], v10, s[28:31], s43 offen offset:2048 sc1\n"
    "s_branch Lk_end\n"
    "Lk_helper:\n"
    "s_waitcnt vmcnt(0) lgkmcnt(0)\n"
    "v_cmp_ne_u32 vcc, s7, v244\n"
    "s_cbranch_vccnz Lk_hff_44\n"
    "Lk_hfr_45:\n"
    "s_add_u32 s8, s42, 0\n"
    "buffer_store_dwordx4 v[16:19], v1, s[12:15], s8 offen offset:0 sc1\n"
    "buffer_store_dwordx4 v[16:19], v1, s[12:15], s8 offen offset:1024 sc1\n"
    "buffer_store_dwordx4 v[16:19], v1, s[12:15], s8 offen offset:2048 sc1\n"
    "buffer_store_dwordx4 v[16:19], v1, s[12:15], s8 offen offset:3072 sc1\n"
    "s_add_u32 s8, s42, 4096\n"
    "buffer_store_dwordx4 v[16:19], v1, s[12:15], s8 offen offset:0 sc1\n"
    "buffer_store_dwordx4 v[16:19], v1, s[12:15], s8 offen offset:1024 sc1\n"
    "buffer_store_dwordx4 v[16:19], v1, s[12:15], s8 offen offset:2048 sc1\n"
    "buffer_store_dwordx4 v[16:19], v1, s[12:15], s8 offen offset:3072 sc1\n"
    "s_add_u32 s8, s42, 8192\n"
    "buffer_store_dwordx4 v[16:19], v1, s[12:15], s8 offen offset:0 sc1\n"
    "buffer_store_dwordx4 v[16:19], v1, s[12:15], s8 offen offset:1024 sc1\n"
    "buffer_store_dwordx4 v[16:19], v1, s[12:15], s8 offen offset:2048 sc1\n"
    "buffer_store_dwordx4 v[16:19], v1, s[12:15], s8 offen offset:3072 sc1\n"
    "s_lshl_b32 s41, s47, 16\n"
    "s_lshl_b32 s52, s5, 14\n"
    "s_add_u32 s41, s41, s52\n"
    "s_add_u32 s41, s41, 2621440\n"
    "s_lshl_b32 s45, s47, 10\n"
    "s_add_u32 s45, s45, 131072\n"
    "s_lshl_b32 s62, s47, 7\n"
    "s_add_u32 s62, s62, 40960\n"
    "s_mov_b32 s20, 0x22222222\n"
    "s_mov_b32 s21, s20\n"
    "s_mov_b32 s22, 0x44444444\n"
    "s_mov_b32 s23, s22\n"
    "s_mov_b32 s24, 0x88888888\n"
    "s_mov_b32 s25, s24\n"
    "s_add_u32 s50, s41, 0\n"
    "buffer_load_dwordx4 v[148:151], v2, s[12:15], s50 offen offset:0 sc1\n"
    "buffer_load_dwordx4 v[152:155], v2, s[12:15], s50 offen offset:256 sc1\n"
    "buffer_load_dwordx4 v[156:159], v2, s[12:15], s50 offen offset:512 sc1\n"
    "buffer_load_dwordx4 v[160:163], v2, s[12:15], s50 offen offset:768 sc1\n"
    "s_add_u32 s50, s41, 4096\n"
    "buffer_load_dwordx4 v[164:167], v2, s[12:15], s50 offen offset:0 sc1\n"
    "buffer_load_dwordx4 v[168:171], v2, s[12:15], s50 offen offset:256 sc1\n"
    "buffer_load_dwordx4 v[172:175], v2, s[12:15], s50 offen offset:512 sc1\n"
    "buffer_load_dwordx4 v[176:179], v2, s[12:15], s50 offen offset:768 sc1\n"
    "s_add_u32 s50, s41, 8192\n"
    "buffer_load_dwordx4 v[180:183], v2, s[12:15], s50 offen offset:0 sc1\n"
    "buffer_load_dwordx4 v[184:187], v2, s[12:15], s50 offen offset:256 sc1\n"
    "buffer_load_dwordx4 v[188:191], v2, s[12:15], s50 offen offset:512 sc1\n"
    "buffer_load_dwordx4 v[192:195], v2, s[12:15], s50 offen offset:768 sc1\n"
    "s_add_u32 s50, s41, 12288\n"
    "buffer_load_dwordx4 v[196:199], v2, s[12:15], s50 offen offset:0 sc1\n"
    "buffer_load_dwordx4 v[200:203], v2, s[12:15], s50 offen offset:256 sc1\n"
    "buffer_load_dwordx4 v[204:207], v2, s[12:15], s50 offen offset:512 sc1\n"
    "buffer_load_dwordx4 v[208:211], v2, s[12:15], s50 offen offset:768 sc1\n"
    "s_add_u32 s8, s42, 12288\n"
    "buffer_store_dwordx4 v[16:19], v1, s[12:15], s8 offen offset:0 sc1\n"
    "buffer_store_dwordx4 v[16:19], v1, s[12:15], s8 offen offset:1024 sc1\n"
    "buffer_store_dwordx4 v[16:19], v1, s[12:15], s8 offen offset:2048 sc1\n"
    "buffer_store_dwordx4 v[16:19], v1, s[12:15], s8 offen offset:3072 sc1\n"
    "s_add_u32 s8, s42, 16384\n"
    "buffer_store_dwordx4 v[16:19], v1, s[12:15], s8 offen offset:0 sc1\n"
    "buffer_store_dwordx4 v[16:19], v1, s[12:15], s8 offen offset:1024 sc1\n"
    "buffer_store_dwordx4 v[16:19], v1, s[12:15], s8 offen offset:2048 sc1\n"
    "buffer_store_dwordx4 v[16:19], v1, s[12:15], s8 offen offset:3072 sc1\n"
    "s_waitcnt vmcnt(20)\n"
    "v_mov_b32 v228, v148\n"
    "v_mov_b32 v229, v152\n"
    "v_mov_b32 v230, v156\n"
    "v_mov_b32 v231, v160\n"
    "v_mov_b32 v232, v149\n"
    "v_mov_b32 v233, v153\n"
    "v_mov_b32 v234, v157\n"
    "v_mov_b32 v235, v161\n"
    "v_mov_b32 v236, v150\n"
    "v_mov_b32 v237, v154\n"
    "v_mov_b32 v238, v158\n"
    "v_mov_b32 v239, v162\n"
    "v_mov_b32 v240, v151\n"
    "v_mov_b32 v241, v155\n"
    "v_mov_b32 v242, v159\n"
    "v_mov_b32 v243, v163\n"
    "v_max3_u32 v9, v228, v229, v230\n"
    "v_max3_u32 v9, v231, v232, v9\n"
    "v_max3_u32 v9, v233, v234, v9\n"
    "v_max3_u32 v9, v235, v236, v9\n"
    "v_max3_u32 v9, v237, v238, v9\n"
    "v_max3_u32 v9, v239, v240, v9\n"
    "v_max3_u32 v9, v241, v242, v9\n"
    "v_max_u32 v9, v243, v9\n"
    "v_cmp_eq_u32 vcc, -1, v9\n"
    "s_cbranch_vccnz Lk_hhf_46\n"
    "Lk_hhr_47:\n"
    "v_mfma_f32_16x16x32_bf16 v[20:23], v[212:215], v[228:231], 0\n"
    "v_mfma_f32_16x16x32_bf16 v[24:27], v[228:231], v[228:231], 0\n"
    "v_mfma_f32_16x16x32_bf16 v[28:31], v[212:215], v[232:235], 0\n"
    "v_mfma_f32_16x16x32_bf16 v[32:35], v[232:235], v[232:235], 0\n"
    "v_mfma_f32_16x16x32_bf16 v[36:39], v[212:215], v[236:239], 0\n"
    "v_mfma_f32_16x16x32_bf16 v[40:43], v[236:239], v[236:239], 0\n"
    "v_mfma_f32_16x16x32_bf16 v[44:47], v[212:215], v[240:243], 0\n"
    "v_mfma_f32_16x16x32_bf16 v[48:51], v[240:243], v[240:243], 0\n"
    "s_waitcnt vmcnt(16)\n"
    "v_mov_b32 v228, v164\n"
    "v_mov_b32 v229, v168\n"
    "v_mov_b32 v230, v172\n"
    "v_mov_b32 v231, v176\n"
    "v_mov_b32 v232, v165\n"
    "v_mov_b32 v233, v169\n"
    "v_mov_b32 v234, v173\n"
    "v_mov_b32 v235, v177\n"
    "v_mov_b32 v236, v166\n"
    "v_mov_b32 v237, v170\n"
    "v_mov_b32 v238, v174\n"
    "v_mov_b32 v239, v178\n"
    "v_mov_b32 v240, v167\n"
    "v_mov_b32 v241, v171\n"
    "v_mov_b32 v242, v175\n"
    "v_mov_b32 v243, v179\n"
    "v_max3_u32 v9, v228, v229, v230\n"
    "v_max3_u32 v9, v231, v232, v9\n"
    "v_max3_u32 v9, v233, v234, v9\n"
    "v_max3_u32 v9, v235, v236, v9\n"
    "v_max3_u32 v9, v237, v238, v9\n"
    "v_max3_u32 v9, v239, v240, v9\n"
    "v_max3_u32 v9, v241, v242, v9\n"
    "v_max_u32 v9, v243, v9\n"
    "v_cmp_eq_u32 vcc, -1, v9\n"
    "s_cbranch_vccnz Lk_hhf_48\n"
    "Lk_hhr_49:\n"
    "s_add_u32 s8, s42, 20480\n"
    "buffer_store_dwordx4 v[16:19], v1, s[12:15], s8 offen offset:0 sc1\n"
    "buffer_store_dwordx4 v[16:19], v1, s[12:15], s8 offen offset:1024 sc1\n"
    "buffer_store_dwordx4 v[16:19], v1, s[12:15], s8 offen offset:2048 sc1\n"
    "buffer_store_dwordx4 v[16:19], v1, s[12:15], s8 offen offset:3072 sc1\n"
    "v_mfma_f32_16x16x32_bf16 v[20:23], v[212:215], v[228:231], v[20:23]\n"
    "v_mfma_f32_16x16x32_bf16 v[24:27], v[228:231], v[228:231], v[24:27]\n"
    "v_mfma_f32_16x16x32_bf16 v[28:31], v[212:215], v[232:235], v[28:31]\n"
    "v_mfma_f32_16x16x32_bf16 v[32:35], v[232:235], v[232:235], v[32:35]\n"
    "v_mfma_f32_16x16x32_bf16 v[36:39], v[212:215], v[236:239], v[36:39]\n"
    "v_mfma_f32_16x16x32_bf16 v[40:43], v[236:239], v[236:239], v[40:43]\n"
    "v_mfma_f32_16x16x32_bf16 v[44:47], v[212:215], v[240:243], v[44:47]\n"
    "v_mfma_f32_16x16x32_bf16 v[48:51], v[240:243], v[240:243], v[48:51]\n"
    "s_waitcnt vmcnt(16)\n"
    "v_mov_b32 v228, v180\n"
    "v_mov_b32 v229, v184\n"
    "v_mov_b32 v230, v188\n"
    "v_mov_b32 v231, v192\n"
    "v_mov_b32 v232, v181\n"
    "v_mov_b32 v233, v185\n"
    "v_mov_b32 v234, v189\n"
    "v_mov_b32 v235, v193\n"
    "v_mov_b32 v236, v182\n"
    "v_mov_b32 v237, v186\n"
    "v_mov_b32 v238, v190\n"
    "v_mov_b32 v239, v194\n"
    "v_mov_b32 v240, v183\n"
    "v_mov_b32 v241, v187\n"
    "v_mov_b32 v242, v191\n"
    "v_mov_b32 v243, v195\n"
    "v_max3_u32 v9, v228, v229, v230\n"
    "v_max3_u32 v9, v231, v232, v9\n"
    "v_max3_u32 v9, v233, v234, v9\n"
    "v_max3_u32 v9, v235, v236, v9\n"
    "v_max3_u32 v9, v237, v238, v9\n"
    "v_max3_u32 v9, v239, v240, v9\n"
    "v_max3_u32 v9, v241, v242, v9\n"
    "v_max_u32 v9, v243, v9\n"
    "v_cmp_eq_u32 vcc, -1, v9\n"
    "s_cbranch_vccnz Lk_hhf_50\n"
    "Lk_hhr_51:\n"
    "v_mfma_f32_16x16x32_bf16 v[20:23], v[212:215], v[228:231], v[20:23]\n"
    "v_mfma_f32_16x16x32_bf16 v[24:27], v[228:231], v[228:231], v[24:27]\n"
    "v_mfma_f32_16x16x32_bf16 v[28:31], v[212:215], v[232:235], v[28:31]\n"
    "v_mfma_f32_16x16x32_bf16 v[32:35], v[232:235], v[232:235], v[32:35]\n"
    "v_mfma_f32_16x16x32_bf16 v[36:39], v[212:215], v[236:239], v[36:39]\n"
    "v_mfma_f32_16x16x32_bf16 v[40:43], v[236:239], v[236:239], v[40:43]\n"
    "v_mfma_f32_16x16x32_bf16 v[44:47], v[212:215], v[240:243], v[44:47]\n"
    "v_mfma_f32_16x16x32_bf16 v[48:51], v[240:243], v[240:243], v[48:51]\n"
    "s_waitcnt vmcnt(12)\n"
    "v_mov_b32 v228, v196\n"
    "v_mov_b32 v229, v200\n"
    "v_mov_b32 v230, v204\n"
    "v_mov_b32 v231, v208\n"
    "v_mov_b32 v232, v197\n"
    "v_mov_b32 v233, v201\n"
    "v_mov_b32 v234, v205\n"
    "v_mov_b32 v235, v209\n"
    "v_mov_b32 v236, v198\n"
    "v_mov_b32 v237, v202\n"
    "v_mov_b32 v238, v206\n"
    "v_mov_b32 v239, v210\n"
    "v_mov_b32 v240, v199\n"
    "v_mov_b32 v241, v203\n"
    "v_mov_b32 v242, v207\n"
    "v_mov_b32 v243, v211\n"
    "v_max3_u32 v9, v228, v229, v230\n"
    "v_max3_u32 v9, v231, v232, v9\n"
    "v_max3_u32 v9, v233, v234, v9\n"
    "v_max3_u32 v9, v235, v236, v9\n"
    "v_max3_u32 v9, v237, v238, v9\n"
    "v_max3_u32 v9, v239, v240, v9\n"
    "v_max3_u32 v9, v241, v242, v9\n"
    "v_max_u32 v9, v243, v9\n"
    "v_cmp_eq_u32 vcc, -1, v9\n"
    "s_cbranch_vccnz Lk_hhf_52\n"
    "Lk_hhr_53:\n"
    "v_mfma_f32_16x16x32_bf16 v[20:23], v[212:215], v[228:231], v[20:23]\n"
    "v_mfma_f32_16x16x32_bf16 v[24:27], v[228:231], v[228:231], v[24:27]\n"
    "v_mfma_f32_16x16x32_bf16 v[28:31], v[212:215], v[232:235], v[28:31]\n"
    "v_mfma_f32_16x16x32_bf16 v[32:35], v[232:235], v[232:235], v[32:35]\n"
    "v_mfma_f32_16x16x32_bf16 v[36:39], v[212:215], v[236:239], v[36:39]\n"
    "v_mfma_f32_16x16x32_bf16 v[40:43], v[236:239], v[236:239], v[40:43]\n"
    "v_mfma_f32_16x16x32_bf16 v[44:47], v[212:215], v[240:243], v[44:47]\n"
    "v_mfma_f32_16x16x32_bf16 v[48:51], v[240:243], v[240:243], v[48:51]\n"
    "s_nop 1\n"
    "v_cndmask_b32 v24, v24, v25, s[20:21]\n"
    "v_cndmask_b32 v24, v24, v26, s[22:23]\n"
    "v_cndmask_b32 v24, v24, v27, s[24:25]\n"
    "v_mov_b32 v52, v20\n"
    "v_mov_b32 v53, v24\n"
    "v_cndmask_b32 v32, v32, v33, s[20:21]\n"
    "v_cndmask_b32 v32, v32, v34, s[22:23]\n"
    "v_cndmask_b32 v32, v32, v35, s[24:25]\n"
    "v_mov_b32 v54, v28\n"
    "v_mov_b32 v55, v32\n"
    "v_cndmask_b32 v40, v40, v41, s[20:21]\n"
    "v_cndmask_b32 v40, v40, v42, s[22:23]\n"
    "v_cndmask_b32 v40, v40, v43, s[24:25]\n"
    "v_mov_b32 v56, v36\n"
    "v_mov_b32 v57, v40\n"
    "v_cndmask_b32 v48, v48, v49, s[20:21]\n"
    "v_cndmask_b32 v48, v48, v50, s[22:23]\n"
    "v_cndmask_b32 v48, v48, v51, s[24:25]\n"
    "v_mov_b32 v58, v44\n"
    "v_mov_b32 v59, v48\n"
    "v_lshl_add_u32 v6, v3, 1, s56\n"
    "v_lshrrev_b32 v7, 1, v1\n"
    "v_add_u32 v7, 0, v7\n"
    "s_mov_b32 exec_lo, 0x00f0000f\n"
    "s_mov_b32 exec_hi, 0xf0000f00\n"
    "ds_write_b128 v6, v[52:55]\n"
    "ds_write_b128 v6, v[56:59] offset:16\n"
    "s_mov_b64 exec, -1\n"
    "s_waitcnt lgkmcnt(0)\n"
    "s_barrier\n"
    "ds_read_b64 v[20:21], v7 offset:0\n"
    "ds_read_b64 v[22:23], v7 offset:512\n"
    "ds_read_b64 v[24:25], v7 offset:1024\n"
    "ds_read_b64 v[26:27], v7 offset:1536\n"
    "s_waitcnt lgkmcnt(0)\n"
    "v_pk_add_f32 v[20:21], v[20:21], v[22:23]\n"
    "v_pk_add_f32 v[20:21], v[20:21], v[24:25]\n"
    "v_pk_add_f32 v[20:21], v[20:21], v[26:27]\n"
    "v_mul_f32 v20, 0x3b000000, v20\n"
    "v_mul_f32 v21, 0x3b000000, v21\n"
    "v_fma_f32 v21, -v20, v20, v21\n"
    "v_max_f32 v21, 0, v21\n"
    "v_cvt_pk_bf16_f32 v22, v20, v21\n"
    "v_and_b32 v21, s48, v22\n"
    "v_lshlrev_b32 v20, 16, v22\n"
    "v_add_f32 v21, 0x3727c5ac, v21\n"
    "v_xor_b32 v24, 0x80000000, v20\n"
    "v_cvt_pk_bf16_f32 v22, v21, v21\n"
    "v_and_b32 v21, s48, v22\n"
    "v_sqrt_f32 v21, v21\n"
    "s_nop 0\n"
    "v_cvt_pk_bf16_f32 v22, v21, v21\n"
    "v_and_b32 v21, s48, v22\n"
    "v_rcp_f32 v22, v21\n"
    "v_mul_f32 v26, v21, v21\n"
    "v_mul_f32 v25, 0xbfb8aa3b, v22\n"
    "v_mov_b32 v27, 0\n"
    "v_mov_b32 v8, s7\n"
    "s_cmp_lg_u32 s5, 0\n"
    "s_cbranch_scc1 Lk_hw_54\n"
    "buffer_store_dwordx4 v[24:27], v1, s[12:15], s45 offen sc1\n"
    "s_waitcnt vmcnt(0)\n"
    "s_mov_b64 exec, 1\n"
    "buffer_store_dword v8, off, s[12:15], s62 sc1\n"
    "s_mov_b64 exec, -1\n"
    "Lk_hw_54:\n"
    "s_add_u32 s8, s42, 24576\n"
    "buffer_store_dwordx4 v[16:19], v1, s[12:15], s8 offen offset:0 sc1\n"
    "buffer_store_dwordx4 v[16:19], v1, s[12:15], s8 offen offset:1024 sc1\n"
    "buffer_store_dwordx4 v[16:19], v1, s[12:15], s8 offen offset:2048 sc1\n"
    "buffer_store_dwordx4 v[16:19], v1, s[12:15], s8 offen offset:3072 sc1\n"
    "s_waitcnt vmcnt(0)\n"
    "s_barrier\n"
    "s_cmp_lg_u32 s5, 0\n"
    "s_cbranch_scc1 Lk_hw_55\n"
    "s_mov_b64 exec, 1\n"
    "buffer_store_dword v8, off, s[12:15], s57 sc1\n"
    "s_mov_b64 exec, -1\n"
    "Lk_hw_55:\n"
    "s_add_u32 s11, s40, 0\n"
    "buffer_load_dwordx4 a[0:3], v13, s[12:15], s11 offen offset:0 sc1\n"
    "buffer_load_dwordx4 a[4:7], v13, s[12:15], s11 offen offset:256 sc1\n"
    "buffer_load_dwordx4 a[8:11], v13, s[12:15], s11 offen offset:512 sc1\n"
    "buffer_load_dwordx4 a[12:15], v13, s[12:15], s11 offen offset:768 sc1\n"
    "s_add_u32 s11, s40, 16384\n"
    "buffer_load_dwordx4 a[16:19], v13, s[12:15], s11 offen offset:0 sc1\n"
    "buffer_load_dwordx4 a[20:23], v13, s[12:15], s11 offen offset:256 sc1\n"
    "buffer_load_dwordx4 a[24:27], v13, s[12:15], s11 offen offset:512 sc1\n"
    "buffer_load_dwordx4 a[28:31], v13, s[12:15], s11 offen offset:768 sc1\n"
    "s_add_u32 s11, s40, 32768\n"
    "buffer_load_dwordx4 a[32:35], v13, s[12:15], s11 offen offset:0 sc1\n"
    "buffer_load_dwordx4 a[36:39], v13, s[12:15], s11 offen offset:256 sc1\n"
    "buffer_load_dwordx4 a[40:43], v13, s[12:15], s11 offen offset:512 sc1\n"
    "buffer_load_dwordx4 a[44:47], v13, s[12:15], s11 offen offset:768 sc1\n"
    "s_add_u32 s11, s40, 2048\n"
    "buffer_load_dwordx4 a[48:51], v14, s[12:15], s11 offen offset:0 sc1\n"
    "buffer_load_dwordx4 a[52:55], v14, s[12:15], s11 offen offset:256 sc1\n"
    "buffer_load_dwordx4 a[56:59], v14, s[12:15], s11 offen offset:512 sc1\n"
    "buffer_load_dwordx4 a[60:63], v14, s[12:15], s11 offen offset:768 sc1\n"
    "s_add_u32 s11, s40, 10240\n"
    "buffer_load_dwordx4 a[64:67], v13, s[12:15], s11 offen offset:0 sc1\n"
    "buffer_load_dwordx4 a[68:71], v13, s[12:15], s11 offen offset:256 sc1\n"
    "buffer_load_dwordx4 a[72:75], v13, s[12:15], s11 offen offset:512 sc1\n"
    "buffer_load_dwordx4 a[76:79], v13, s[12:15], s11 offen offset:768 sc1\n"
    "s_add_u32 s11, s40, 26624\n"
    "buffer_load_dwordx4 a[80:83], v13, s[12:15], s11 offen offset:0 sc1\n"
    "buffer_load_dwordx4 a[84:87], v13, s[12:15], s11 offen offset:256 sc1\n"
    "buffer_load_dwordx4 a[88:91], v13, s[12:15], s11 offen offset:512 sc1\n"
    "buffer_load_dwordx4 a[92:95], v13, s[12:15], s11 offen offset:768 sc1\n"
    "s_add_u32 s11, s40, 43008\n"
    "buffer_load_dwordx4 a[96:99], v13, s[12:15], s11 offen offset:0 sc1\n"
    "buffer_load_dwordx4 a[100:103], v13, s[12:15], s11 offen offset:256 sc1\n"
    "buffer_load_dwordx4 a[104:107], v13, s[12:15], s11 offen offset:512 sc1\n"
    "buffer_load_dwordx4 a[108:111], v13, s[12:15], s11 offen offset:768 sc1\n"
    "s_add_u32 s11, s40, 4096\n"
    "buffer_load_dwordx4 a[112:115], v13, s[12:15], s11 offen offset:0 sc1\n"
    "buffer_load_dwordx4 a[116:119], v13, s[12:15], s11 offen offset:256 sc1\n"
    "buffer_load_dwordx4 a[120:123], v13, s[12:15], s11 offen offset:512 sc1\n"
    "buffer_load_dwordx4 a[124:127], v13, s[12:15], s11 offen offset:768 sc1\n"
    "s_add_u32 s11, s40, 20480\n"
    "buffer_load_dwordx4 a[128:131], v13, s[12:15], s11 offen offset:0 sc1\n"
    "buffer_load_dwordx4 a[132:135], v13, s[12:15], s11 offen offset:256 sc1\n"
    "buffer_load_dwordx4 a[136:139], v13, s[12:15], s11 offen offset:512 sc1\n"
    "buffer_load_dwordx4 a[140:143], v13, s[12:15], s11 offen offset:768 sc1\n"
    "s_add_u32 s11, s40, 36864\n"
    "buffer_load_dwordx4 a[144:147], v13, s[12:15], s11 offen offset:0 sc1\n"
    "buffer_load_dwordx4 a[148:151], v13, s[12:15], s11 offen offset:256 sc1\n"
    "buffer_load_dwordx4 a[152:155], v13, s[12:15], s11 offen offset:512 sc1\n"
    "buffer_load_dwordx4 a[156:159], v13, s[12:15], s11 offen offset:768 sc1\n"
    "s_add_u32 s11, s40, 6144\n"
    "buffer_load_dwordx4 a[160:163], v14, s[12:15], s11 offen offset:0 sc1\n"
    "buffer_load_dwordx4 a[164:167], v14, s[12:15], s11 offen offset:256 sc1\n"
    "buffer_load_dwordx4 a[168:171], v14, s[12:15], s11 offen offset:512 sc1\n"
    "buffer_load_dwordx4 a[172:175], v14, s[12:15], s11 offen offset:768 sc1\n"
    "s_add_u32 s11, s40, 14336\n"
    "buffer_load_dwordx4 a[176:179], v13, s[12:15], s11 offen offset:0 sc1\n"
    "buffer_load_dwordx4 a[180:183], v13, s[12:15], s11 offen offset:256 sc1\n"
    "buffer_load_dwordx4 a[184:187], v13, s[12:15], s11 offen offset:512 sc1\n"
    "buffer_load_dwordx4 a[188:191], v13, s[12:15], s11 offen offset:768 sc1\n"
    "s_add_u32 s11, s40, 30720\n"
    "buffer_load_dwordx4 a[192:195], v13, s[12:15], s11 offen offset:0 sc1\n"
    "buffer_load_dwordx4 a[196:199], v13, s[12:15], s11 offen offset:256 sc1\n"
    "buffer_load_dwordx4 a[200:203], v13, s[12:15], s11 offen offset:512 sc1\n"
    "buffer_load_dwordx4 a[204:207], v13, s[12:15], s11 offen offset:768 sc1\n"
    "s_add_u32 s11, s40, 47104\n"
    "buffer_load_dwordx4 a[208:211], v13, s[12:15], s11 offen offset:0 sc1\n"
    "buffer_load_dwordx4 a[212:215], v13, s[12:15], s11 offen offset:256 sc1\n"
    "buffer_load_dwordx4 a[216:219], v13, s[12:15], s11 offen offset:512 sc1\n"
    "buffer_load_dwordx4 a[220:223], v13, s[12:15], s11 offen offset:768 sc1\n"
    "v_lshlrev_b32 v12, 4, v0\n"
    "buffer_load_dwordx4 v[244:247], v12, s[12:15], s36 offen sc1\n"
    "buffer_load_dwordx4 v[248:251], v12, s[12:15], s37 offen sc1\n"
    "v_min_u32 v253, 0x60, v1\n"
    "v_lshlrev_b32 v253, 7, v253\n"
    "buffer_load_dword v252, v253, s[12:15], s42 offen offset:256 sc1\n"
    "s_waitcnt vmcnt(0)\n"
    "v_cmp_eq_u32 vcc, -1, v252\n"
    "s_cbranch_vccnz Lk_hpf_56\n"
    "Lk_hpr_57:\n"
    "s_lshr_b32 s10, s4, 5\n"
    "s_lshl_b32 s10, s10, 7\n"
    "v_lshl_add_u32 v16, v1, 6, s10\n"
    "s_bfe_u32 s16, s4, 0x20003\n"
    "s_lshl_b32 s16, s16, 7\n"
    "v_lshl_add_u32 v18, v1, 5, s16\n"
    "s_branch Lk_join\n"
    "Lk_tail:\n"
    "Lk_xtf_1:\n"
    "s_nop 7\n"
    "s_mov_b32 s53, 0\n"
    "Lk_xtl_58:\n"
    "s_sleep 3\n"
    "buffer_load_dwordx4 v[100:103], v23, s[12:15], s54 offen offset:0 sc1\n"
    "buffer_load_dwordx4 v[104:107], v23, s[12:15], s54 offen offset:512 sc1\n"
    "buffer_load_dwordx4 v[108:111], v23, s[12:15], s54 offen offset:1024 sc1\n"
    "buffer_load_dwordx4 v[112:115], v23, s[12:15], s54 offen offset:1536 sc1\n"
    "s_add_u32 s53, s53, 1\n"
    "s_waitcnt vmcnt(0)\n"
    "v_lshlrev_b32 v48, 16, v100\n"
    "v_and_b32 v49, s48, v100\n"
    "v_lshlrev_b32 v50, 16, v102\n"
    "v_and_b32 v51, s48, v102\n"
    "v_lshlrev_b32 v52, 16, v104\n"
    "v_and_b32 v53, s48, v104\n"
    "v_lshlrev_b32 v54, 16, v106\n"
    "v_and_b32 v55, s48, v106\n"
    "v_lshlrev_b32 v56, 16, v108\n"
    "v_and_b32 v57, s48, v108\n"
    "v_lshlrev_b32 v58, 16, v110\n"
    "v_and_b32 v59, s48, v110\n"
    "v_lshlrev_b32 v60, 16, v112\n"
    "v_and_b32 v61, s48, v112\n"
    "v_lshlrev_b32 v62, 16, v114\n"
    "v_and_b32 v63, s48, v114\n"
    "v_bitop3_b32 v6, v101, v103, s7 bitop3:0x7e\n"
    "v_bitop3_b32 v7, v105, v107, s7 bitop3:0x7e\n"
    "v_bitop3_b32 v8, v109, v111, s7 bitop3:0x7e\n"
    "v_bitop3_b32 v9, v113, v115, s7 bitop3:0x7e\n"
    "v_or3_b32 v6, v6, v7, v8\n"
    "v_or_b32 v6, v6, v9\n"
    "v_cmp_ne_u32 vcc, 0, v6\n"
    "s_cmp_gt_u32 s53, 1024\n"
    "s_cbranch_scc1 Lk_xtr_2\n"
    "s_cbranch_vccnz Lk_xtl_58\n"
    "s_nop 1\n"
    "s_branch Lk_xtr_2\n"
    "Lk_stf_3:\n"
    "s_nop 7\n"
    "s_mov_b32 s53, 0\n"
    "Lk_stl_59:\n"
    "s_sleep 3\n"
    "buffer_load_dwordx4 v[132:135], v23, s[12:15], s47 offen offset:0 sc1\n"
    "buffer_load_dwordx4 v[136:139], v23, s[12:15], s47 offen offset:512 sc1\n"
    "buffer_load_dwordx4 v[140:143], v23, s[12:15], s47 offen offset:1024 sc1\n"
    "buffer_load_dwordx4 v[144:147], v23, s[12:15], s47 offen offset:1536 sc1\n"
    "s_add_u32 s53, s53, 1\n"
    "s_waitcnt vmcnt(0)\n"
    "v_and_b32 v101, s48, v132\n"
    "v_lshlrev_b32 v100, 16, v132\n"
    "v_and_b32 v103, s48, v134\n"
    "v_lshlrev_b32 v102, 16, v134\n"
    "v_and_b32 v105, s48, v136\n"
    "v_lshlrev_b32 v104, 16, v136\n"
    "v_and_b32 v107, s48, v138\n"
    "v_lshlrev_b32 v106, 16, v138\n"
    "v_and_b32 v109, s48, v140\n"
    "v_lshlrev_b32 v108, 16, v140\n"
    "v_and_b32 v111, s48, v142\n"
    "v_lshlrev_b32 v110, 16, v142\n"
    "v_and_b32 v113, s48, v144\n"
    "v_lshlrev_b32 v112, 16, v144\n"
    "v_and_b32 v115, s48, v146\n"
    "v_lshlrev_b32 v114, 16, v146\n"
    "v_bitop3_b32 v6, v133, v135, s7 bitop3:0x7e\n"
    "v_bitop3_b32 v7, v137, v139, s7 bitop3:0x7e\n"
    "v_bitop3_b32 v8, v141, v143, s7 bitop3:0x7e\n"
    "v_bitop3_b32 v9, v145, v147, s7 bitop3:0x7e\n"
    "v_or3_b32 v6, v6, v7, v8\n"
    "v_or_b32 v6, v6, v9\n"
    "v_cmp_ne_u32 vcc, 0, v6\n"
    "s_cmp_gt_u32 s53, 1024\n"
    "s_cbranch_scc1 Lk_str_4\n"
    "s_cbranch_vccnz Lk_stl_59\n"
    "s_nop 1\n"
    "s_branch Lk_str_4\n"
    "Lk_flf_7:\n"
    "s_nop 7\n"
    "s_mov_b32 s53, 0\n"
    "Lk_fll_60:\n"
    "s_sleep 3\n"
    "buffer_load_dword v244, v245, s[12:15], 0 offen sc1\n"
    "s_add_u32 s53, s53, 1\n"
    "s_waitcnt vmcnt(0)\n"
    "v_cmp_ne_u32 vcc, s7, v244\n"
    "s_cmp_gt_u32 s53, 1024\n"
    "s_cbranch_scc1 Lk_flr_8\n"
    "s_cbranch_vccnz Lk_fll_60\n"
    "s_nop 1\n"
    "s_branch Lk_flr_8\n"
    "Lk_hf_10:\n"
    "s_nop 7\n"
    "s_mov_b32 s53, 0\n"
    "s_add_u32 s50, s41, 8192\n"
    "Lk_hl_61:\n"
    "buffer_load_dwordx4 v[180:183], v2, s[12:15], s50 offen offset:0 sc1\n"
    "buffer_load_dwordx4 v[184:187], v2, s[12:15], s50 offen offset:256 sc1\n"
    "buffer_load_dwordx4 v[188:191], v2, s[12:15], s50 offen offset:512 sc1\n"
    "buffer_load_dwordx4 v[192:195], v2, s[12:15], s50 offen offset:768 sc1\n"
    "s_add_u32 s53, s53, 1\n"
    "s_waitcnt vmcnt(0)\n"
    "v_mov_b32 v212, v180\n"
    "v_mov_b32 v213, v184\n"
    "v_mov_b32 v214, v188\n"
    "v_mov_b32 v215, v192\n"
    "v_mov_b32 v216, v181\n"
    "v_mov_b32 v217, v185\n"
    "v_mov_b32 v218, v189\n"
    "v_mov_b32 v219, v193\n"
    "v_mov_b32 v220, v182\n"
    "v_mov_b32 v221, v186\n"
    "v_mov_b32 v222, v190\n"
    "v_mov_b32 v223, v194\n"
    "v_mov_b32 v224, v183\n"
    "v_mov_b32 v225, v187\n"
    "v_mov_b32 v226, v191\n"
    "v_mov_b32 v227, v195\n"
    "v_max3_u32 v9, v212, v213, v214\n"
    "v_max3_u32 v9, v215, v216, v9\n"
    "v_max3_u32 v9, v217, v218, v9\n"
    "v_max3_u32 v9, v219, v220, v9\n"
    "v_max3_u32 v9, v221, v222, v9\n"
    "v_max3_u32 v9, v223, v224, v9\n"
    "v_max3_u32 v9, v225, v226, v9\n"
    "v_max_u32 v9, v227, v9\n"
    "v_cmp_eq_u32 vcc, -1, v9\n"
    "s_cmp_gt_u32 s53, 1024\n"
    "s_cbranch_scc1 Lk_hr_11\n"
    "s_cbranch_vccnz Lk_hl_61\n"
    "s_add_u32 s50, s41, 12288\n"
    "buffer_load_dwordx4 v[196:199], v2, s[12:15], s50 offen offset:0 sc1\n"
    "buffer_load_dwordx4 v[200:203], v2, s[12:15], s50 offen offset:256 sc1\n"
    "buffer_load_dwordx4 v[204:207], v2, s[12:15], s50 offen offset:512 sc1\n"
    "buffer_load_dwordx4 v[208:211], v2, s[12:15], s50 offen offset:768 sc1\n"
    "s_add_u32 s50, s41, 16384\n"
    "buffer_load_dwordx4 a[224:227], v2, s[12:15], s50 offen offset:0 sc1\n"
    "buffer_load_dwordx4 a[228:231], v2, s[12:15], s50 offen offset:256 sc1\n"
    "buffer_load_dwordx4 a[232:235], v2, s[12:15], s50 offen offset:512 sc1\n"
    "buffer_load_dwordx4 a[236:239], v2, s[12:15], s50 offen offset:768 sc1\n"
    "s_add_u32 s50, s41, 20480\n"
    "buffer_load_dwordx4 a[240:243], v2, s[12:15], s50 offen offset:0 sc1\n"
    "buffer_load_dwordx4 a[244:247], v2, s[12:15], s50 offen offset:256 sc1\n"
    "buffer_load_dwordx4 a[248:251], v2, s[12:15], s50 offen offset:512 sc1\n"
    "buffer_load_dwordx4 a[252:255], v2, s[12:15], s50 offen offset:768 sc1\n"
    "buffer_load_dword v19, off, s[12:15], 64 sc1\n"
    "buffer_load_dword v19, off, s[12:15], 64 sc1\n"
    "buffer_load_dword v19, off, s[12:15], 64 sc1\n"
    "buffer_load_dword v19, off, s[12:15], 64 sc1\n"
    "s_nop 1\n"
    "s_branch Lk_hr_11\n"
    "Lk_hf_12:\n"
    "s_nop 7\n"
    "s_mov_b32 s53, 0\n"
    "s_add_u32 s50, s41, 12288\n"
    "Lk_hl_62:\n"
    "buffer_load_dwordx4 v[196:199], v2, s[12:15], s50 offen offset:0 sc1\n"
    "buffer_load_dwordx4 v[200:203], v2, s[12:15], s50 offen offset:256 sc1\n"
    "buffer_load_dwordx4 v[204:207], v2, s[12:15], s50 offen offset:512 sc1\n"
    "buffer_load_dwordx4 v[208:211], v2, s[12:15], s50 offen offset:768 sc1\n"
    "s_add_u32 s53, s53, 1\n"
    "s_waitcnt vmcnt(0)\n"
    "v_mov_b32 v228, v196\n"
    "v_mov_b32 v229, v200\n"
    "v_mov_b32 v230, v204\n"
    "v_mov_b32 v231, v208\n"
    "v_mov_b32 v232, v197\n"
    "v_mov_b32 v233, v201\n"
    "v_mov_b32 v234, v205\n"
    "v_mov_b32 v235, v209\n"
    "v_mov_b32 v236, v198\n"
    "v_mov_b32 v237, v202\n"
    "v_mov_b32 v238, v206\n"
    "v_mov_b32 v239, v210\n"
    "v_mov_b32 v240, v199\n"
    "v_mov_b32 v241, v203\n"
    "v_mov_b32 v242, v207\n"
    "v_mov_b32 v243, v211\n"
    "v_max3_u32 v9, v228, v229, v230\n"
    "v_max3_u32 v9, v231, v232, v9\n"
    "v_max3_u32 v9, v233, v234, v9\n"
    "v_max3_u32 v9, v235, v236, v9\n"
    "v_max3_u32 v9, v237, v238, v9\n"
    "v_max3_u32 v9, v239, v240, v9\n"
    "v_max3_u32 v9, v241, v242, v9\n"
    "v_max_u32 v9, v243, v9\n"
    "v_cmp_eq_u32 vcc, -1, v9\n"
    "s_cmp_gt_u32 s53, 1024\n"
    "s_cbranch_scc1 Lk_hr_13\n"
    "s_cbranch_vccnz Lk_hl_62\n"
    "s_add_u32 s50, s41, 16384\n"
    "buffer_load_dwordx4 a[224:227], v2, s[12:15], s50 offen offset:0 sc1\n"
    "buffer_load_dwordx4 a[228:231], v2, s[12:15], s50 offen offset:256 sc1\n"
    "buffer_load_dwordx4 a[232:235], v2, s[12:15], s50 offen offset:512 sc1\n"
    "buffer_load_dwordx4 a[236:239], v2, s[12:15], s50 offen offset:768 sc1\n"
    "s_add_u32 s50, s41, 20480\n"
    "buffer_load_dwordx4 a[240:243], v2, s[12:15], s50 offen offset:0 sc1\n"
    "buffer_load_dwordx4 a[244:247], v2, s[12:15], s50 offen offset:256 sc1\n"
    "buffer_load_dwordx4 a[248:251], v2, s[12:15], s50 offen offset:512 sc1\n"
    "buffer_load_dwordx4 a[252:255], v2, s[12:15], s50 offen offset:768 sc1\n"
    "buffer_load_dword v19, off, s[12:15], 64 sc1\n"
    "buffer_load_dword v19, off, s[12:15], 64 sc1\n"
    "buffer_load_dword v19, off, s[12:15], 64 sc1\n"
    "buffer_load_dword v19, off, s[12:15], 64 sc1\n"
    "s_add_u32 s50, s41, 24576\n"
    "buffer_load_dwordx4 v[148:151], v2, s[12:15], s50 offen offset:0 sc1\n"
    "buffer_load_dwordx4 v[152:155], v2, s[12:15], s50 offen offset:256 sc1\n"
    "buffer_load_dwordx4 v[156:159], v2, s[12:15], s50 offen offset:512 sc1\n"
    "buffer_load_dwordx4 v[160:163], v2, s[12:15], s50 offen offset:768 sc1\n"
    "s_add_u32 s50, s41, 28672\n"
    "buffer_load_dwordx4 v[164:167], v2, s[12:15], s50 offen offset:0 sc1\n"
    "buffer_load_dwordx4 v[168:171], v2, s[12:15], s50 offen offset:256 sc1\n"
    "buffer_load_dwordx4 v[172:175], v2, s[12:15], s50 offen offset:512 sc1\n"
    "buffer_load_dwordx4 v[176:179], v2, s[12:15], s50 offen offset:768 sc1\n"
    "s_nop 1\n"
    "s_branch Lk_hr_13\n"
    "Lk_hf_14:\n"
    "s_nop 7\n"
    "s_mov_b32 s53, 0\n"
    "s_add_u32 s50, s41, 16384\n"
    "Lk_hl_63:\n"
    "buffer_load_dwordx4 a[224:227], v2, s[12:15], s50 offen offset:0 sc1\n"
    "buffer_load_dwordx4 a[228:231], v2, s[12:15], s50 offen offset:256 sc1\n"
    "buffer_load_dwordx4 a[232:235], v2, s[12:15], s50 offen offset:512 sc1\n"
    "buffer_load_dwordx4 a[236:239], v2, s[12:15], s50 offen offset:768 sc1\n"
    "s_add_u32 s53, s53, 1\n"
    "s_waitcnt vmcnt(0)\n"
    "v_accvgpr_read_b32 v212, a224\n"
    "v_accvgpr_read_b32 v213, a228\n"
    "v_accvgpr_read_b32 v214, a232\n"
    "v_accvgpr_read_b32 v215, a236\n"
    "v_accvgpr_read_b32 v216, a225\n"
    "v_accvgpr_read_b32 v217, a229\n"
    "v_accvgpr_read_b32 v218, a233\n"
    "v_accvgpr_read_b32 v219, a237\n"
    "v_accvgpr_read_b32 v220, a226\n"
    "v_accvgpr_read_b32 v221, a230\n"
    "v_accvgpr_read_b32 v222, a234\n"
    "v_accvgpr_read_b32 v223, a238\n"
    "v_accvgpr_read_b32 v224, a227\n"
    "v_accvgpr_read_b32 v225, a231\n"
    "v_accvgpr_read_b32 v226, a235\n"
    "v_accvgpr_read_b32 v227, a239\n"
    "v_max3_u32 v9, v212, v213, v214\n"
    "v_max3_u32 v9, v215, v216, v9\n"
    "v_max3_u32 v9, v217, v218, v9\n"
    "v_max3_u32 v9, v219, v220, v9\n"
    "v_max3_u32 v9, v221, v222, v9\n"
    "v_max3_u32 v9, v223, v224, v9\n"
    "v_max3_u32 v9, v225, v226, v9\n"
    "v_max_u32 v9, v227, v9\n"
    "v_cmp_eq_u32 vcc, -1, v9\n"
    "s_cmp_gt_u32 s53, 1024\n"
    "s_cbranch_scc1 Lk_hr_15\n"
    "s_cbranch_vccnz Lk_hl_63\n"
    "s_add_u32 s50, s41, 20480\n"
    "buffer_load_dwordx4 a[240:243], v2, s[12:15], s50 offen offset:0 sc1\n"
    "buffer_load_dwordx4 a[244:247], v2, s[12:15], s50 offen offset:256 sc1\n"
    "buffer_load_dwordx4 a[248:251], v2, s[12:15], s50 offen offset:512 sc1\n"
    "buffer_load_dwordx4 a[252:255], v2, s[12:15], s50 offen offset:768 sc1\n"
    "buffer_load_dword v19, off, s[12:15], 64 sc1\n"
    "buffer_load_dword v19, off, s[12:15], 64 sc1\n"
    "buffer_load_dword v19, off, s[12:15], 64 sc1\n"
    "buffer_load_dword v19, off, s[12:15], 64 sc1\n"
    "s_add_u32 s50, s41, 24576\n"
    "buffer_load_dwordx4 v[148:151], v2, s[12:15], s50 offen offset:0 sc1\n"
    "buffer_load_dwordx4 v[152:155], v2, s[12:15], s50 offen offset:256 sc1\n"
    "buffer_load_dwordx4 v[156:159], v2, s[12:15], s50 offen offset:512 sc1\n"
    "buffer_load_dwordx4 v[160:163], v2, s[12:15], s50 offen offset:768 sc1\n"
    "s_add_u32 s50, s41, 28672\n"
    "buffer_load_dwordx4 v[164:167], v2, s[12:15], s50 offen offset:0 sc1\n"
    "buffer_load_dwordx4 v[168:171], v2, s[12:15], s50 offen offset:256 sc1\n"
    "buffer_load_dwordx4 v[172:175], v2, s[12:15], s50 offen offset:512 sc1\n"
    "buffer_load_dwordx4 v[176:179], v2, s[12:15], s50 offen offset:768 sc1\n"
    "s_add_u32 s50, s41, 32768\n"
    "buffer_load_dwordx4 v[180:183], v2, s[12:15], s50 offen offset:0 sc1\n"
    "buffer_load_dwordx4 v[184:187], v2, s[12:15], s50 offen offset:256 sc1\n"
    "buffer_load_dwordx4 v[188:191], v2, s[12:15], s50 offen offset:512 sc1\n"
    "buffer_load_dwordx4 v[192:195], v2, s[12:15], s50 offen offset:768 sc1\n"
    "s_add_u32 s50, s41, 36864\n"
    "buffer_load_dwordx4 v[196:199], v2, s[12:15], s50 offen offset:0 sc1\n"
    "buffer_load_dwordx4 v[200:203], v2, s[12:15], s50 offen offset:256 sc1\n"
    "buffer_load_dwordx4 v[204:207], v2, s[12:15], s50 offen offset:512 sc1\n"
    "buffer_load_dwordx4 v[208:211], v2, s[12:15], s50 offen offset:768 sc1\n"
    "s_nop 1\n"
    "s_branch Lk_hr_15\n"
    "Lk_hf_16:\n"
    "s_nop 7\n"
    "s_mov_b32 s53, 0\n"
    "s_add_u32 s50, s41, 20480\n"
    "Lk_hl_64:\n"
    "buffer_load_dwordx4 a[240:243], v2, s[12:15], s50 offen offset:0 sc1\n"
    "buffer_load_dwordx4 a[244:247], v2, s[12:15], s50 offen offset:256 sc1\n"
    "buffer_load_dwordx4 a[248:251], v2, s[12:15], s50 offen offset:512 sc1\n"
    "buffer_load_dwordx4 a[252:255], v2, s[12:15], s50 offen offset:768 sc1\n"
    "s_add_u32 s53, s53, 1\n"
    "s_waitcnt vmcnt(0)\n"
    "v_accvgpr_read_b32 v228, a240\n"
    "v_accvgpr_read_b32 v229, a244\n"
    "v_accvgpr_read_b32 v230, a248\n"
    "v_accvgpr_read_b32 v231, a252\n"
    "v_accvgpr_read_b32 v232, a241\n"
    "v_accvgpr_read_b32 v233, a245\n"
    "v_accvgpr_read_b32 v234, a249\n"
    "v_accvgpr_read_b32 v235, a253\n"
    "v_accvgpr_read_b32 v236, a242\n"
    "v_accvgpr_read_b32 v237, a246\n"
    "v_accvgpr_read_b32 v238, a250\n"
    "v_accvgpr_read_b32 v239, a254\n"
    "v_accvgpr_read_b32 v240, a243\n"
    "v_accvgpr_read_b32 v241, a247\n"
    "v_accvgpr_read_b32 v242, a251\n"
    "v_accvgpr_read_b32 v243, a255\n"
    "v_max3_u32 v9, v228, v229, v230\n"
    "v_max3_u32 v9, v231, v232, v9\n"
    "v_max3_u32 v9, v233, v234, v9\n"
    "v_max3_u32 v9, v235, v236, v9\n"
    "v_max3_u32 v9, v237, v238, v9\n"
    "v_max3_u32 v9, v239, v240, v9\n"
    "v_max3_u32 v9, v241, v242, v9\n"
    "v_max_u32 v9, v243, v9\n"
    "v_cmp_eq_u32 vcc, -1, v9\n"
    "s_cmp_gt_u32 s53, 1024\n"
    "s_cbranch_scc1 Lk_hr_17\n"
    "s_cbranch_vccnz Lk_hl_64\n"
    "buffer_load_dword v19, off, s[12:15], 64 sc1\n"
    "buffer_load_dword v19, off, s[12:15], 64 sc1\n"
    "buffer_load_dword v19, off, s[12:15], 64 sc1\n"
    "buffer_load_dword v19, off, s[12:15], 64 sc1\n"
    "s_add_u32 s50, s41, 24576\n"
    "buffer_load_dwordx4 v[148:151], v2, s[12:15], s50 offen offset:0 sc1\n"
    "buffer_load_dwordx4 v[152:155], v2, s[12:15], s50 offen offset:256 sc1\n"
    "buffer_load_dwordx4 v[156:159], v2, s[12:15], s50 offen offset:512 sc1\n"
    "buffer_load_dwordx4 v[160:163], v2, s[12:15], s50 offen offset:768 sc1\n"
    "s_add_u32 s50, s41, 28672\n"
    "buffer_load_dwordx4 v[164:167], v2, s[12:15], s50 offen offset:0 sc1\n"
    "buffer_load_dwordx4 v[168:171], v2, s[12:15], s50 offen offset:256 sc1\n"
    "buffer_load_dwordx4 v[172:175], v2, s[12:15], s50 offen offset:512 sc1\n"
    "buffer_load_dwordx4 v[176:179], v2, s[12:15], s50 offen offset:768 sc1\n"
    "s_add_u32 s50, s41, 32768\n"
    "buffer_load_dwordx4 v[180:183], v2, s[12:15], s50 offen offset:0 sc1\n"
    "buffer_load_dwordx4 v[184:187], v2, s[12:15], s50 offen offset:256 sc1\n"
    "buffer_load_dwordx4 v[188:191], v2, s[12:15], s50 offen offset:512 sc1\n"
    "buffer_load_dwordx4 v[192:195], v2, s[12:15], s50 offen offset:768 sc1\n"
    "s_add_u32 s50, s41, 36864\n"
    "buffer_load_dwordx4 v[196:199], v2, s[12:15], s50 offen offset:0 sc1\n"
    "buffer_load_dwordx4 v[200:203], v2, s[12:15], s50 offen offset:256 sc1\n"
    "buffer_load_dwordx4 v[204:207], v2, s[12:15], s50 offen offset:512 sc1\n"
    "buffer_load_dwordx4 v[208:211], v2, s[12:15], s50 offen offset:768 sc1\n"
    "s_add_u32 s50, s41, 0\n"
    "buffer_load_dwordx4 a[224:227], v2, s[12:15], s50 offen offset:0 sc1\n"
    "buffer_load_dwordx4 a[228:231], v2, s[12:15], s50 offen offset:256 sc1\n"
    "buffer_load_dwordx4 a[232:235], v2, s[12:15], s50 offen offset:512 sc1\n"
    "buffer_load_dwordx4 a[236:239], v2, s[12:15], s50 offen offset:768 sc1\n"
    "buffer_load_dword v19, off, s[12:15], 64 sc1\n"
    "buffer_load_dword v19, off, s[12:15], 64 sc1\n"
    "buffer_load_dword v19, off, s[12:15], 64 sc1\n"
    "s_nop 1\n"
    "s_branch Lk_hr_17\n"
    "Lk_hf_18:\n"
    "s_nop 7\n"
    "s_mov_b32 s53, 0\n"
    "s_add_u32 s50, s41, 24576\n"
    "Lk_hl_65:\n"
    "buffer_load_dwordx4 v[148:151], v2, s[12:15], s50 offen offset:0 sc1\n"
    "buffer_load_dwordx4 v[152:155], v2, s[12:15], s50 offen offset:256 sc1\n"
    "buffer_load_dwordx4 v[156:159], v2, s[12:15], s50 offen offset:512 sc1\n"
    "buffer_load_dwordx4 v[160:163], v2, s[12:15], s50 offen offset:768 sc1\n"
    "s_add_u32 s53, s53, 1\n"
    "s_waitcnt vmcnt(0)\n"
    "v_mov_b32 v212, v148\n"
    "v_mov_b32 v213, v152\n"
    "v_mov_b32 v214, v156\n"
    "v_mov_b32 v215, v160\n"
    "v_mov_b32 v216, v149\n"
    "v_mov_b32 v217, v153\n"
    "v_mov_b32 v218, v157\n"
    "v_mov_b32 v219, v161\n"
    "v_mov_b32 v220, v150\n"
    "v_mov_b32 v221, v154\n"
    "v_mov_b32 v222, v158\n"
    "v_mov_b32 v223, v162\n"
    "v_mov_b32 v224, v151\n"
    "v_mov_b32 v225, v155\n"
    "v_mov_b32 v226, v159\n"
    "v_mov_b32 v227, v163\n"
    "v_max3_u32 v9, v212, v213, v214\n"
    "v_max3_u32 v9, v215, v216, v9\n"
    "v_max3_u32 v9, v217, v218, v9\n"
    "v_max3_u32 v9, v219, v220, v9\n"
    "v_max3_u32 v9, v221, v222, v9\n"
    "v_max3_u32 v9, v223, v224, v9\n"
    "v_max3_u32 v9, v225, v226, v9\n"
    "v_max_u32 v9, v227, v9\n"
    "v_cmp_eq_u32 vcc, -1, v9\n"
    "s_cmp_gt_u32 s53, 1024\n"
    "s_cbranch_scc1 Lk_hr_19\n"
    "s_cbranch_vccnz Lk_hl_65\n"
    "s_add_u32 s50, s41, 28672\n"
    "buffer_load_dwordx4 v[164:167], v2, s[12:15], s50 offen offset:0 sc1\n"
    "buffer_load_dwordx4 v[168:171], v2, s[12:15], s50 offen offset:256 sc1\n"
    "buffer_load_dwordx4 v[172:175], v2, s[12:15], s50 offen offset:512 sc1\n"
    "buffer_load_dwordx4 v[176:179], v2, s[12:15], s50 offen offset:768 sc1\n"
    "s_add_u32 s50, s41, 32768\n"
    "buffer_load_dwordx4 v[180:183], v2, s[12:15], s50 offen offset:0 sc1\n"
    "buffer_load_dwordx4 v[184:187], v2, s[12:15], s50 offen offset:256 sc1\n"
    "buffer_load_dwordx4 v[188:191], v2, s[12:15], s50 offen offset:512 sc1\n"
    "buffer_load_dwordx4 v[192:195], v2, s[12:15], s50 offen offset:768 sc1\n"
    "s_add_u32 s50, s41, 36864\n"
    "buffer_load_dwordx4 v[196:199], v2, s[12:15], s50 offen offset:0 sc1\n"
    "buffer_load_dwordx4 v[200:203], v2, s[12:15], s50 offen offset:256 sc1\n"
    "buffer_load_dwordx4 v[204:207], v2, s[12:15], s50 offen offset:512 sc1\n"
    "buffer_load_dwordx4 v[208:211], v2, s[12:15], s50 offen offset:768 sc1\n"
    "s_add_u32 s50, s41, 0\n"
    "buffer_load_dwordx4 a[224:227], v2, s[12:15], s50 offen offset:0 sc1\n"
    "buffer_load_dwordx4 a[228:231], v2, s[12:15], s50 offen offset:256 sc1\n"
    "buffer_load_dwordx4 a[232:235], v2, s[12:15], s50 offen offset:512 sc1\n"
    "buffer_load_dwordx4 a[236:239], v2, s[12:15], s50 offen offset:768 sc1\n"
    "buffer_load_dword v19, off, s[12:15], 64 sc1\n"
    "buffer_load_dword v19, off, s[12:15], 64 sc1\n"
    "buffer_load_dword v19, off, s[12:15], 64 sc1\n"
    "s_add_u32 s50, s41, 4096\n"
    "buffer_load_dwordx4 a[240:243], v2, s[12:15], s50 offen offset:0 sc1\n"
    "buffer_load_dwordx4 a[244:247], v2, s[12:15], s50 offen offset:256 sc1\n"
    "buffer_load_dwordx4 a[248:251], v2, s[12:15], s50 offen offset:512 sc1\n"
    "buffer_load_dwordx4 a[252:255], v2, s[12:15], s50 offen offset:768 sc1\n"
    "buffer_load_dword v19, off, s[12:15], 64 sc1\n"
    "buffer_load_dword v19, off, s[12:15], 64 sc1\n"
    "buffer_load_dword v19, off, s[12:15], 64 sc1\n"
    "s_nop 1\n"
    "s_branch Lk_hr_19\n"
    "Lk_hf_20:\n"
    "s_nop 7\n"
    "s_mov_b32 s53, 0\n"
    "s_add_u32 s50, s41, 28672\n"
    "Lk_hl_66:\n"
    "buffer_load_dwordx4 v[164:167], v2, s[12:15], s50 offen offset:0 sc1\n"
    "buffer_load_dwordx4 v[168:171], v2, s[12:15], s50 offen offset:256 sc1\n"
    "buffer_load_dwordx4 v[172:175], v2, s[12:15], s50 offen offset:512 sc1\n"
    "buffer_load_dwordx4 v[176:179], v2, s[12:15], s50 offen offset:768 sc1\n"
    "s_add_u32 s53, s53, 1\n"
    "s_waitcnt vmcnt(0)\n"
    "v_mov_b32 v228, v164\n"
    "v_mov_b32 v229, v168\n"
    "v_mov_b32 v230, v172\n"
    "v_mov_b32 v231, v176\n"
    "v_mov_b32 v232, v165\n"
    "v_mov_b32 v233, v169\n"
    "v_mov_b32 v234, v173\n"
    "v_mov_b32 v235, v177\n"
    "v_mov_b32 v236, v166\n"
    "v_mov_b32 v237, v170\n"
    "v_mov_b32 v238, v174\n"
    "v_mov_b32 v239, v178\n"
    "v_mov_b32 v240, v167\n"
    "v_mov_b32 v241, v171\n"
    "v_mov_b32 v242, v175\n"
    "v_mov_b32 v243, v179\n"
    "v_max3_u32 v9, v228, v229, v230\n"
    "v_max3_u32 v9, v231, v232, v9\n"
    "v_max3_u32 v9, v233, v234, v9\n"
    "v_max3_u32 v9, v235, v236, v9\n"
    "v_max3_u32 v9, v237, v238, v9\n"
    "v_max3_u32 v9, v239, v240, v9\n"
    "v_max3_u32 v9, v241, v242, v9\n"
    "v_max_u32 v9, v243, v9\n"
    "v_cmp_eq_u32 vcc, -1, v9\n"
    "s_cmp_gt_u32 s53, 1024\n"
    "s_cbranch_scc1 Lk_hr_21\n"
    "s_cbranch_vccnz Lk_hl_66\n"
    "s_add_u32 s50, s41, 32768\n"
    "buffer_load_dwordx4 v[180:183], v2, s[12:15], s50 offen offset:0 sc1\n"
    "buffer_load_dwordx4 v[184:187], v2, s[12:15], s50 offen offset:256 sc1\n"
    "buffer_load_dwordx4 v[188:191], v2, s[12:15], s50 offen offset:512 sc1\n"
    "buffer_load_dwordx4 v[192:195], v2, s[12:15], s50 offen offset:768 sc1\n"
    "s_add_u32 s50, s41, 36864\n"
    "buffer_load_dwordx4 v[196:199], v2, s[12:15], s50 offen offset:0 sc1\n"
    "buffer_load_dwordx4 v[200:203], v2, s[12:15], s50 offen offset:256 sc1\n"
    "buffer_load_dwordx4 v[204:207], v2, s[12:15], s50 offen offset:512 sc1\n"
    "buffer_load_dwordx4 v[208:211], v2, s[12:15], s50 offen offset:768 sc1\n"
    "s_add_u32 s50, s41, 0\n"
    "buffer_load_dwordx4 a[224:227], v2, s[12:15], s50 offen offset:0 sc1\n"
    "buffer_load_dwordx4 a[228:231], v2, s[12:15], s50 offen offset:256 sc1\n"
    "buffer_load_dwordx4 a[232:235], v2, s[12:15], s50 offen offset:512 sc1\n"
    "buffer_load_dwordx4 a[236:239], v2, s[12:15], s50 offen offset:768 sc1\n"
    "buffer_load_dword v19, off, s[12:15], 64 sc1\n"
    "buffer_load_dword v19, off, s[12:15], 64 sc1\n"
    "buffer_load_dword v19, off, s[12:15], 64 sc1\n"
    "s_add_u32 s50, s41, 4096\n"
    "buffer_load_dwordx4 a[240:243], v2, s[12:15], s50 offen offset:0 sc1\n"
    "buffer_load_dwordx4 a[244:247], v2, s[12:15], s50 offen offset:256 sc1\n"
    "buffer_load_dwordx4 a[248:251], v2, s[12:15], s50 offen offset:512 sc1\n"
    "buffer_load_dwordx4 a[252:255], v2, s[12:15], s50 offen offset:768 sc1\n"
    "buffer_load_dword v19, off, s[12:15], 64 sc1\n"
    "buffer_load_dword v19, off, s[12:15], 64 sc1\n"
    "buffer_load_dword v19, off, s[12:15], 64 sc1\n"
    "s_add_u32 s50, s41, 57344\n"
    "buffer_load_dwordx4 v[148:151], v2, s[12:15], s50 offen offset:0 sc1\n"
    "buffer_load_dwordx4 v[152:155], v2, s[12:15], s50 offen offset:256 sc1\n"
    "buffer_load_dwordx4 v[156:159], v2, s[12:15], s50 offen offset:512 sc1\n"
    "buffer_load_dwordx4 v[160:163], v2, s[12:15], s50 offen offset:768 sc1\n"
    "buffer_load_dword v19, off, s[12:15], 64 sc1\n"
    "buffer_load_dword v19, off, s[12:15], 64 sc1\n"
    "buffer_load_dword v19, off, s[12:15], 64 sc1\n"
    "s_nop 1\n"
    "s_branch Lk_hr_21\n"
    "Lk_f1f_22:\n"
    "s_nop 7\n"
    "s_mov_b32 s53, 0\n"
    "Lk_f1l_67:\n"
    "s_sleep 3\n"
    "buffer_load_dword v6, off, s[12:15], s62 sc1\n"
    "s_add_u32 s53, s53, 1\n"
    "s_waitcnt vmcnt(0)\n"
    "v_cmp_ne_u32 vcc, s7, v6\n"
    "s_cmp_gt_u32 s53, 1024\n"
    "s_cbranch_scc1 Lk_f1r_23\n"
    "s_cbranch_vccnz Lk_f1l_67\n"
    "s_nop 1\n"
    "s_branch Lk_f1r_23\n"
    "Lk_f0f_24:\n"
    "s_nop 7\n"
    "s_mov_b32 s53, 0\n"
    "Lk_f0l_68:\n"
    "s_sleep 3\n"
    "buffer_load_dword v7, off, s[12:15], s57 sc1\n"
    "s_add_u32 s53, s53, 1\n"
    "s_waitcnt vmcnt(0)\n"
    "v_cmp_ne_u32 vcc, s7, v7\n"
    "s_cmp_gt_u32 s53, 1024\n"
    "s_cbranch_scc1 Lk_f0r_25\n"
    "s_cbranch_vccnz Lk_f0l_68\n"
    "s_nop 1\n"
    "s_branch Lk_f0r_25\n"
    "Lk_p3redo:\n"
    "s_waitcnt vmcnt(0) lgkmcnt(0)\n"
    "s_add_u32 s9, s9, 1\n"
    "s_cmp_gt_u32 s9, 8\n"
    "s_cbranch_scc1 Lk_p3ok\n"
    "s_sleep 32\n"
    "s_branch Lk_p3\n"
    "Lk_Hf_26:\n"
    "s_nop 7\n"
    "s_mov_b32 s53, 0\n"
    "s_add_u32 s50, s42, 0\n"
    "Lk_Hl_69:\n"
    "buffer_load_dwordx4 v[164:167], v2, s[12:15], s50 offen offset:0 sc1\n"
    "buffer_load_dwordx4 v[168:171], v2, s[12:15], s50 offen offset:256 sc1\n"
    "buffer_load_dwordx4 v[172:175], v2, s[12:15], s50 offen offset:512 sc1\n"
    "buffer_load_dwordx4 v[176:179], v2, s[12:15], s50 offen offset:768 sc1\n"
    "s_add_u32 s53, s53, 1\n"
    "s_waitcnt vmcnt(0)\n"
    "v_mov_b32 v212, v164\n"
    "v_mov_b32 v213, v168\n"
    "v_mov_b32 v214, v172\n"
    "v_mov_b32 v215, v176\n"
    "v_mov_b32 v216, v165\n"
    "v_mov_b32 v217, v169\n"
    "v_mov_b32 v218, v173\n"
    "v_mov_b32 v219, v177\n"
    "v_mov_b32 v220, v166\n"
    "v_mov_b32 v221, v170\n"
    "v_mov_b32 v222, v174\n"
    "v_mov_b32 v223, v178\n"
    "v_mov_b32 v224, v167\n"
    "v_mov_b32 v225, v171\n"
    "v_mov_b32 v226, v175\n"
    "v_mov_b32 v227, v179\n"
    "v_max3_u32 v9, v212, v213, v214\n"
    "v_max3_u32 v9, v215, v216, v9\n"
    "v_max3_u32 v9, v217, v218, v9\n"
    "v_max3_u32 v9, v219, v220, v9\n"
    "v_max3_u32 v9, v221, v222, v9\n"
    "v_max3_u32 v9, v223, v224, v9\n"
    "v_max3_u32 v9, v225, v226, v9\n"
    "v_max_u32 v9, v227, v9\n"
    "v_cmp_eq_u32 vcc, -1, v9\n"
    "s_cmp_gt_u32 s53, 1024\n"
    "s_cbranch_scc1 Lk_Hr_27\n"
    "s_cbranch_vccnz Lk_Hl_69\n"
    "s_add_u32 s50, s42, 4096\n"
    "buffer_load_dwordx4 v[180:183], v2, s[12:15], s50 offen offset:0 sc1\n"
    "buffer_load_dwordx4 v[184:187], v2, s[12:15], s50 offen offset:256 sc1\n"
    "buffer_load_dwordx4 v[188:191], v2, s[12:15], s50 offen offset:512 sc1\n"
    "buffer_load_dwordx4 v[192:195], v2, s[12:15], s50 offen offset:768 sc1\n"
    "s_add_u32 s50, s42, 8192\n"
    "buffer_load_dwordx4 v[196:199], v2, s[12:15], s50 offen offset:0 sc1\n"
    "buffer_load_dwordx4 v[200:203], v2, s[12:15], s50 offen offset:256 sc1\n"
    "buffer_load_dwordx4 v[204:207], v2, s[12:15], s50 offen offset:512 sc1\n"
    "buffer_load_dwordx4 v[208:211], v2, s[12:15], s50 offen offset:768 sc1\n"
    "s_add_u32 s50, s42, 12288\n"
    "buffer_load_dwordx4 a[224:227], v2, s[12:15], s50 offen offset:0 sc1\n"
    "buffer_load_dwordx4 a[228:231], v2, s[12:15], s50 offen offset:256 sc1\n"
    "buffer_load_dwordx4 a[232:235], v2, s[12:15], s50 offen offset:512 sc1\n"
    "buffer_load_dwordx4 a[236:239], v2, s[12:15], s50 offen offset:768 sc1\n"
    "s_nop 1\n"
    "s_branch Lk_Hr_27\n"
    "Lk_Hf_28:\n"
    "s_nop 7\n"
    "s_mov_b32 s53, 0\n"
    "s_add_u32 s50, s42, 4096\n"
    "Lk_Hl_70:\n"
    "buffer_load_dwordx4 v[180:183], v2, s[12:15], s50 offen offset:0 sc1\n"
    "buffer_load_dwordx4 v[184:187], v2, s[12:15], s50 offen offset:256 sc1\n"
    "buffer_load_dwordx4 v[188:191], v2, s[12:15], s50 offen offset:512 sc1\n"
    "buffer_load_dwordx4 v[192:195], v2, s[12:15], s50 offen offset:768 sc1\n"
    "s_add_u32 s53, s53, 1\n"
    "s_waitcnt vmcnt(0)\n"
    "v_mov_b32 v228, v180\n"
    "v_mov_b32 v229, v184\n"
    "v_mov_b32 v230, v188\n"
    "v_mov_b32 v231, v192\n"
    "v_mov_b32 v232, v181\n"
    "v_mov_b32 v233, v185\n"
    "v_mov_b32 v234, v189\n"
    "v_mov_b32 v235, v193\n"
    "v_mov_b32 v236, v182\n"
    "v_mov_b32 v237, v186\n"
    "v_mov_b32 v238, v190\n"
    "v_mov_b32 v239, v194\n"
    "v_mov_b32 v240, v183\n"
    "v_mov_b32 v241, v187\n"
    "v_mov_b32 v242, v191\n"
    "v_mov_b32 v243, v195\n"
    "v_max3_u32 v9, v228, v229, v230\n"
    "v_max3_u32 v9, v231, v232, v9\n"
    "v_max3_u32 v9, v233, v234, v9\n"
    "v_max3_u32 v9, v235, v236, v9\n"
    "v_max3_u32 v9, v237, v238, v9\n"
    "v_max3_u32 v9, v239, v240, v9\n"
    "v_max3_u32 v9, v241, v242, v9\n"
    "v_max_u32 v9, v243, v9\n"
    "v_cmp_eq_u32 vcc, -1, v9\n"
    "s_cmp_gt_u32 s53, 1024\n"
    "s_cbranch_scc1 Lk_Hr_29\n"
    "s_cbranch_vccnz Lk_Hl_70\n"
    "s_add_u32 s50, s42, 8192\n"
    "buffer_load_dwordx4 v[196:199], v2, s[12:15], s50 offen offset:0 sc1\n"
    "buffer_load_dwordx4 v[200:203], v2, s[12:15], s50 offen offset:256 sc1\n"
    "buffer_load_dwordx4 v[204:207], v2, s[12:15], s50 offen offset:512 sc1\n"
    "buffer_load_dwordx4 v[208:211], v2, s[12:15], s50 offen offset:768 sc1\n"
    "s_add_u32 s50, s42, 12288\n"
    "buffer_load_dwordx4 a[224:227], v2, s[12:15], s50 offen offset:0 sc1\n"
    "buffer_load_dwordx4 a[228:231], v2, s[12:15], s50 offen offset:256 sc1\n"
    "buffer_load_dwordx4 a[232:235], v2, s[12:15], s50 offen offset:512 sc1\n"
    "buffer_load_dwordx4 a[236:239], v2, s[12:15], s50 offen offset:768 sc1\n"
    "s_add_u32 s50, s42, 16384\n"
    "buffer_load_dwordx4 a[240:243], v2, s[12:15], s50 offen offset:0 sc1\n"
    "buffer_load_dwordx4 a[244:247], v2, s[12:15], s50 offen offset:256 sc1\n"
    "buffer_load_dwordx4 a[248:251], v2, s[12:15], s50 offen offset:512 sc1\n"
    "buffer_load_dwordx4 a[252:255], v2, s[12:15], s50 offen offset:768 sc1\n"
    "s_nop 1\n"
    "s_branch Lk_Hr_29\n"
    "Lk_Hf_30:\n"
    "s_nop 7\n"
    "s_mov_b32 s53, 0\n"
    "s_add_u32 s50, s42, 8192\n"
    "Lk_Hl_71:\n"
    "buffer_load_dwordx4 v[196:199], v2, s[12:15], s50 offen offset:0 sc1\n"
    "buffer_load_dwordx4 v[200:203], v2, s[12:15], s50 offen offset:256 sc1\n"
    "buffer_load_dwordx4 v[204:207], v2, s[12:15], s50 offen offset:512 sc1\n"
    "buffer_load_dwordx4 v[208:211], v2, s[12:15], s50 offen offset:768 sc1\n"
    "s_add_u32 s53, s53, 1\n"
    "s_waitcnt vmcnt(0)\n"
    "v_mov_b32 v212, v196\n"
    "v_mov_b32 v213, v200\n"
    "v_mov_b32 v214, v204\n"
    "v_mov_b32 v215, v208\n"
    "v_mov_b32 v216, v197\n"
    "v_mov_b32 v217, v201\n"
    "v_mov_b32 v218, v205\n"
    "v_mov_b32 v219, v209\n"
    "v_mov_b32 v220, v198\n"
    "v_mov_b32 v221, v202\n"
    "v_mov_b32 v222, v206\n"
    "v_mov_b32 v223, v210\n"
    "v_mov_b32 v224, v199\n"
    "v_mov_b32 v225, v203\n"
    "v_mov_b32 v226, v207\n"
    "v_mov_b32 v227, v211\n"
    "v_max3_u32 v9, v212, v213, v214\n"
    "v_max3_u32 v9, v215, v216, v9\n"
    "v_max3_u32 v9, v217, v218, v9\n"
    "v_max3_u32 v9, v219, v220, v9\n"
    "v_max3_u32 v9, v221, v222, v9\n"
    "v_max3_u32 v9, v223, v224, v9\n"
    "v_max3_u32 v9, v225, v226, v9\n"
    "v_max_u32 v9, v227, v9\n"
    "v_cmp_eq_u32 vcc, -1, v9\n"
    "s_cmp_gt_u32 s53, 1024\n"
    "s_cbranch_scc1 Lk_Hr_31\n"
    "s_cbranch_vccnz Lk_Hl_71\n"
    "s_add_u32 s50, s42, 12288\n"
    "buffer_load_dwordx4 a[224:227], v2, s[12:15], s50 offen offset:0 sc1\n"
    "buffer_load_dwordx4 a[228:231], v2, s[12:15], s50 offen offset:256 sc1\n"
    "buffer_load_dwordx4 a[232:235], v2, s[12:15], s50 offen offset:512 sc1\n"
    "buffer_load_dwordx4 a[236:239], v2, s[12:15], s50 offen offset:768 sc1\n"
    "s_add_u32 s50, s42, 16384\n"
    "buffer_load_dwordx4 a[240:243], v2, s[12:15], s50 offen offset:0 sc1\n"
    "buffer_load_dwordx4 a[244:247], v2, s[12:15], s50 offen offset:256 sc1\n"
    "buffer_load_dwordx4 a[248:251], v2, s[12:15], s50 offen offset:512 sc1\n"
    "buffer_load_dwordx4 a[252:255], v2, s[12:15], s50 offen offset:768 sc1\n"
    "s_add_u32 s50, s42, 20480\n"
    "buffer_load_dwordx4 v[148:151], v2, s[12:15], s50 offen offset:0 sc1\n"
    "buffer_load_dwordx4 v[152:155], v2, s[12:15], s50 offen offset:256 sc1\n"
    "buffer_load_dwordx4 v[156:159], v2, s[12:15], s50 offen offset:512 sc1\n"
    "buffer_load_dwordx4 v[160:163], v2, s[12:15], s50 offen offset:768 sc1\n"
    "s_nop 1\n"
    "s_branch Lk_Hr_31\n"
    "Lk_Hf_32:\n"
    "s_nop 7\n"
    "s_mov_b32 s53, 0\n"
    "s_add_u32 s50, s42, 12288\n"
    "Lk_Hl_72:\n"
    "buffer_load_dwordx4 a[224:227], v2, s[12:15], s50 offen offset:0 sc1\n"
    "buffer_load_dwordx4 a[228:231], v2, s[12:15], s50 offen offset:256 sc1\n"
    "buffer_load_dwordx4 a[232:235], v2, s[12:15], s50 offen offset:512 sc1\n"
    "buffer_load_dwordx4 a[236:239], v2, s[12:15], s50 offen offset:768 sc1\n"
    "s_add_u32 s53, s53, 1\n"
    "s_waitcnt vmcnt(0)\n"
    "v_accvgpr_read_b32 v228, a224\n"
    "v_accvgpr_read_b32 v229, a228\n"
    "v_accvgpr_read_b32 v230, a232\n"
    "v_accvgpr_read_b32 v231, a236\n"
    "v_accvgpr_read_b32 v232, a225\n"
    "v_accvgpr_read_b32 v233, a229\n"
    "v_accvgpr_read_b32 v234, a233\n"
    "v_accvgpr_read_b32 v235, a237\n"
    "v_accvgpr_read_b32 v236, a226\n"
    "v_accvgpr_read_b32 v237, a230\n"
    "v_accvgpr_read_b32 v238, a234\n"
    "v_accvgpr_read_b32 v239, a238\n"
    "v_accvgpr_read_b32 v240, a227\n"
    "v_accvgpr_read_b32 v241, a231\n"
    "v_accvgpr_read_b32 v242, a235\n"
    "v_accvgpr_read_b32 v243, a239\n"
    "v_max3_u32 v9, v228, v229, v230\n"
    "v_max3_u32 v9, v231, v232, v9\n"
    "v_max3_u32 v9, v233, v234, v9\n"
    "v_max3_u32 v9, v235, v236, v9\n"
    "v_max3_u32 v9, v237, v238, v9\n"
    "v_max3_u32 v9, v239, v240, v9\n"
    "v_max3_u32 v9, v241, v242, v9\n"
    "v_max_u32 v9, v243, v9\n"
    "v_cmp_eq_u32 vcc, -1, v9\n"
    "s_cmp_gt_u32 s53, 1024\n"
    "s_cbranch_scc1 Lk_Hr_33\n"
    "s_cbranch_vccnz Lk_Hl_72\n"
    "s_add_u32 s50, s42, 16384\n"
    "buffer_load_dwordx4 a[240:243], v2, s[12:15], s50 offen offset:0 sc1\n"
    "buffer_load_dwordx4 a[244:247], v2, s[12:15], s50 offen offset:256 sc1\n"
    "buffer_load_dwordx4 a[248:251], v2, s[12:15], s50 offen offset:512 sc1\n"
    "buffer_load_dwordx4 a[252:255], v2, s[12:15], s50 offen offset:768 sc1\n"
    "s_add_u32 s50, s42, 20480\n"
    "buffer_load_dwordx4 v[148:151], v2, s[12:15], s50 offen offset:0 sc1\n"
    "buffer_load_dwordx4 v[152:155], v2, s[12:15], s50 offen offset:256 sc1\n"
    "buffer_load_dwordx4 v[156:159], v2, s[12:15], s50 offen offset:512 sc1\n"
    "buffer_load_dwordx4 v[160:163], v2, s[12:15], s50 offen offset:768 sc1\n"
    "s_add_u32 s50, s42, 24576\n"
    "buffer_load_dwordx4 v[84:87], v2, s[12:15], s50 offen offset:0 sc1\n"
    "buffer_load_dwordx4 v[88:91], v2, s[12:15], s50 offen offset:256 sc1\n"
    "buffer_load_dwordx4 v[92:95], v2, s[12:15], s50 offen offset:512 sc1\n"
    "buffer_load_dwordx4 v[96:99], v2, s[12:15], s50 offen offset:768 sc1\n"
    "s_nop 1\n"
    "s_branch Lk_Hr_33\n"
    "Lk_Hf_34:\n"
    "s_nop 7\n"
    "s_mov_b32 s53, 0\n"
    "s_add_u32 s50, s42, 16384\n"
    "Lk_Hl_73:\n"
    "buffer_load_dwordx4 a[240:243], v2, s[12:15], s50 offen offset:0 sc1\n"
    "buffer_load_dwordx4 a[244:247], v2, s[12:15], s50 offen offset:256 sc1\n"
    "buffer_load_dwordx4 a[248:251], v2, s[12:15], s50 offen offset:512 sc1\n"
    "buffer_load_dwordx4 a[252:255], v2, s[12:15], s50 offen offset:768 sc1\n"
    "s_add_u32 s53, s53, 1\n"
    "s_waitcnt vmcnt(0)\n"
    "v_accvgpr_read_b32 v212, a240\n"
    "v_accvgpr_read_b32 v213, a244\n"
    "v_accvgpr_read_b32 v214, a248\n"
    "v_accvgpr_read_b32 v215, a252\n"
    "v_accvgpr_read_b32 v216, a241\n"
    "v_accvgpr_read_b32 v217, a245\n"
    "v_accvgpr_read_b32 v218, a249\n"
    "v_accvgpr_read_b32 v219, a253\n"
    "v_accvgpr_read_b32 v220, a242\n"
    "v_accvgpr_read_b32 v221, a246\n"
    "v_accvgpr_read_b32 v222, a250\n"
    "v_accvgpr_read_b32 v223, a254\n"
    "v_accvgpr_read_b32 v224, a243\n"
    "v_accvgpr_read_b32 v225, a247\n"
    "v_accvgpr_read_b32 v226, a251\n"
    "v_accvgpr_read_b32 v227, a255\n"
    "v_max3_u32 v9, v212, v213, v214\n"
    "v_max3_u32 v9, v215, v216, v9\n"
    "v_max3_u32 v9, v217, v218, v9\n"
    "v_max3_u32 v9, v219, v220, v9\n"
    "v_max3_u32 v9, v221, v222, v9\n"
    "v_max3_u32 v9, v223, v224, v9\n"
    "v_max3_u32 v9, v225, v226, v9\n"
    "v_max_u32 v9, v227, v9\n"
    "v_cmp_eq_u32 vcc, -1, v9\n"
    "s_cmp_gt_u32 s53, 1024\n"
    "s_cbranch_scc1 Lk_Hr_35\n"
    "s_cbranch_vccnz Lk_Hl_73\n"
    "s_add_u32 s50, s42, 20480\n"
    "buffer_load_dwordx4 v[148:151], v2, s[12:15], s50 offen offset:0 sc1\n"
    "buffer_load_dwordx4 v[152:155], v2, s[12:15], s50 offen offset:256 sc1\n"
    "buffer_load_dwordx4 v[156:159], v2, s[12:15], s50 offen offset:512 sc1\n"
    "buffer_load_dwordx4 v[160:163], v2, s[12:15], s50 offen offset:768 sc1\n"
    "s_add_u32 s50, s42, 24576\n"
    "buffer_load_dwordx4 v[84:87], v2, s[12:15], s50 offen offset:0 sc1\n"
    "buffer_load_dwordx4 v[88:91], v2, s[12:15], s50 offen offset:256 sc1\n"
    "buffer_load_dwordx4 v[92:95], v2, s[12:15], s50 offen offset:512 sc1\n"
    "buffer_load_dwordx4 v[96:99], v2, s[12:15], s50 offen offset:768 sc1\n"
    "s_add_u32 s50, s42, 28672\n"
    "buffer_load_dwordx4 v[100:103], v2, s[12:15], s50 offen offset:0 sc1\n"
    "buffer_load_dwordx4 v[104:107], v2, s[12:15], s50 offen offset:256 sc1\n"
    "buffer_load_dwordx4 v[108:111], v2, s[12:15], s50 offen offset:512 sc1\n"
    "buffer_load_dwordx4 v[112:115], v2, s[12:15], s50 offen offset:768 sc1\n"
    "s_nop 1\n"
    "s_branch Lk_Hr_35\n"
    "Lk_Hf_36:\n"
    "s_nop 7\n"
    "s_mov_b32 s53, 0\n"
    "s_add_u32 s50, s42, 20480\n"
    "Lk_Hl_74:\n"
    "buffer_load_dwordx4 v[148:151], v2, s[12:15], s50 offen offset:0 sc1\n"
    "buffer_load_dwordx4 v[152:155], v2, s[12:15], s50 offen offset:256 sc1\n"
    "buffer_load_dwordx4 v[156:159], v2, s[12:15], s50 offen offset:512 sc1\n"
    "buffer_load_dwordx4 v[160:163], v2, s[12:15], s50 offen offset:768 sc1\n"
    "s_add_u32 s53, s53, 1\n"
    "s_waitcnt vmcnt(0)\n"
    "v_mov_b32 v228, v148\n"
    "v_mov_b32 v229, v152\n"
    "v_mov_b32 v230, v156\n"
    "v_mov_b32 v231, v160\n"
    "v_mov_b32 v232, v149\n"
    "v_mov_b32 v233, v153\n"
    "v_mov_b32 v234, v157\n"
    "v_mov_b32 v235, v161\n"
    "v_mov_b32 v236, v150\n"
    "v_mov_b32 v237, v154\n"
    "v_mov_b32 v238, v158\n"
    "v_mov_b32 v239, v162\n"
    "v_mov_b32 v240, v151\n"
    "v_mov_b32 v241, v155\n"
    "v_mov_b32 v242, v159\n"
    "v_mov_b32 v243, v163\n"
    "v_max3_u32 v9, v228, v229, v230\n"
    "v_max3_u32 v9, v231, v232, v9\n"
    "v_max3_u32 v9, v233, v234, v9\n"
    "v_max3_u32 v9, v235, v236, v9\n"
    "v_max3_u32 v9, v237, v238, v9\n"
    "v_max3_u32 v9, v239, v240, v9\n"
    "v_max3_u32 v9, v241, v242, v9\n"
    "v_max_u32 v9, v243, v9\n"
    "v_cmp_eq_u32 vcc, -1, v9\n"
    "s_cmp_gt_u32 s53, 1024\n"
    "s_cbranch_scc1 Lk_Hr_37\n"
    "s_cbranch_vccnz Lk_Hl_74\n"
    "s_add_u32 s50, s42, 24576\n"
    "buffer_load_dwordx4 v[84:87], v2, s[12:15], s50 offen offset:0 sc1\n"
    "buffer_load_dwordx4 v[88:91], v2, s[12:15], s50 offen offset:256 sc1\n"
    "buffer_load_dwordx4 v[92:95], v2, s[12:15], s50 offen offset:512 sc1\n"
    "buffer_load_dwordx4 v[96:99], v2, s[12:15], s50 offen offset:768 sc1\n"
    "s_add_u32 s50, s42, 28672\n"
    "buffer_load_dwordx4 v[100:103], v2, s[12:15], s50 offen offset:0 sc1\n"
    "buffer_load_dwordx4 v[104:107], v2, s[12:15], s50 offen offset:256 sc1\n"
    "buffer_load_dwordx4 v[108:111], v2, s[12:15], s50 offen offset:512 sc1\n"
    "buffer_load_dwordx4 v[112:115], v2, s[12:15], s50 offen offset:768 sc1\n"
    "s_add_u32 s50, s42, 32768\n"
    "buffer_load_dwordx4 v[116:119], v2, s[12:15], s50 offen offset:0 sc1\n"
    "buffer_load_dwordx4 v[120:123], v2, s[12:15], s50 offen offset:256 sc1\n"
    "buffer_load_dwordx4 v[124:127], v2, s[12:15], s50 offen offset:512 sc1\n"
    "buffer_load_dwordx4 v[128:131], v2, s[12:15], s50 offen offset:768 sc1\n"
    "s_nop 1\n"
    "s_branch Lk_Hr_37\n"
    "Lk_Hf_38:\n"
    "s_nop 7\n"
    "s_mov_b32 s53, 0\n"
    "s_add_u32 s50, s42, 24576\n"
    "Lk_Hl_75:\n"
    "buffer_load_dwordx4 v[84:87], v2, s[12:15], s50 offen offset:0 sc1\n"
    "buffer_load_dwordx4 v[88:91], v2, s[12:15], s50 offen offset:256 sc1\n"
    "buffer_load_dwordx4 v[92:95], v2, s[12:15], s50 offen offset:512 sc1\n"
    "buffer_load_dwordx4 v[96:99], v2, s[12:15], s50 offen offset:768 sc1\n"
    "s_add_u32 s53, s53, 1\n"
    "s_waitcnt vmcnt(0)\n"
    "v_mov_b32 v212, v84\n"
    "v_mov_b32 v213, v88\n"
    "v_mov_b32 v214, v92\n"
    "v_mov_b32 v215, v96\n"
    "v_mov_b32 v216, v85\n"
    "v_mov_b32 v217, v89\n"
    "v_mov_b32 v218, v93\n"
    "v_mov_b32 v219, v97\n"
    "v_mov_b32 v220, v86\n"
    "v_mov_b32 v221, v90\n"
    "v_mov_b32 v222, v94\n"
    "v_mov_b32 v223, v98\n"
    "v_mov_b32 v224, v87\n"
    "v_mov_b32 v225, v91\n"
    "v_mov_b32 v226, v95\n"
    "v_mov_b32 v227, v99\n"
    "v_max3_u32 v9, v212, v213, v214\n"
    "v_max3_u32 v9, v215, v216, v9\n"
    "v_max3_u32 v9, v217, v218, v9\n"
    "v_max3_u32 v9, v219, v220, v9\n"
    "v_max3_u32 v9, v221, v222, v9\n"
    "v_max3_u32 v9, v223, v224, v9\n"
    "v_max3_u32 v9, v225, v226, v9\n"
    "v_max_u32 v9, v227, v9\n"
    "v_cmp_eq_u32 vcc, -1, v9\n"
    "s_cmp_gt_u32 s53, 1024\n"
    "s_cbranch_scc1 Lk_Hr_39\n"
    "s_cbranch_vccnz Lk_Hl_75\n"
    "s_add_u32 s50, s42, 28672\n"
    "buffer_load_dwordx4 v[100:103], v2, s[12:15], s50 offen offset:0 sc1\n"
    "buffer_load_dwordx4 v[104:107], v2, s[12:15], s50 offen offset:256 sc1\n"
    "buffer_load_dwordx4 v[108:111], v2, s[12:15], s50 offen offset:512 sc1\n"
    "buffer_load_dwordx4 v[112:115], v2, s[12:15], s50 offen offset:768 sc1\n"
    "s_add_u32 s50, s42, 32768\n"
    "buffer_load_dwordx4 v[116:119], v2, s[12:15], s50 offen offset:0 sc1\n"
    "buffer_load_dwordx4 v[120:123], v2, s[12:15], s50 offen offset:256 sc1\n"
    "buffer_load_dwordx4 v[124:127], v2, s[12:15], s50 offen offset:512 sc1\n"
    "buffer_load_dwordx4 v[128:131], v2, s[12:15], s50 offen offset:768 sc1\n"
    "s_add_u32 s50, s42, 36864\n"
    "buffer_load_dwordx4 v[164:167], v2, s[12:15], s50 offen offset:0 sc1\n"
    "buffer_load_dwordx4 v[168:171], v2, s[12:15], s50 offen offset:256 sc1\n"
    "buffer_load_dwordx4 v[172:175], v2, s[12:15], s50 offen offset:512 sc1\n"
    "buffer_load_dwordx4 v[176:179], v2, s[12:15], s50 offen offset:768 sc1\n"
    "s_nop 1\n"
    "s_branch Lk_Hr_39\n"
    "Lk_Hf_40:\n"
    "s_nop 7\n"
    "s_mov_b32 s53, 0\n"
    "s_add_u32 s50, s42, 28672\n"
    "Lk_Hl_76:\n"
    "buffer_load_dwordx4 v[100:103], v2, s[12:15], s50 offen offset:0 sc1\n"
    "buffer_load_dwordx4 v[104:107], v2, s[12:15], s50 offen offset:256 sc1\n"
    "buffer_load_dwordx4 v[108:111], v2, s[12:15], s50 offen offset:512 sc1\n"
    "buffer_load_dwordx4 v[112:115], v2, s[12:15], s50 offen offset:768 sc1\n"
    "s_add_u32 s53, s53, 1\n"
    "s_waitcnt vmcnt(0)\n"
    "v_mov_b32 v228, v100\n"
    "v_mov_b32 v229, v104\n"
    "v_mov_b32 v230, v108\n"
    "v_mov_b32 v231, v112\n"
    "v_mov_b32 v232, v101\n"
    "v_mov_b32 v233, v105\n"
    "v_mov_b32 v234, v109\n"
    "v_mov_b32 v235, v113\n"
    "v_mov_b32 v236, v102\n"
    "v_mov_b32 v237, v106\n"
    "v_mov_b32 v238, v110\n"
    "v_mov_b32 v239, v114\n"
    "v_mov_b32 v240, v103\n"
    "v_mov_b32 v241, v107\n"
    "v_mov_b32 v242, v111\n"
    "v_mov_b32 v243, v115\n"
    "v_max3_u32 v9, v228, v229, v230\n"
    "v_max3_u32 v9, v231, v232, v9\n"
    "v_max3_u32 v9, v233, v234, v9\n"
    "v_max3_u32 v9, v235, v236, v9\n"
    "v_max3_u32 v9, v237, v238, v9\n"
    "v_max3_u32 v9, v239, v240, v9\n"
    "v_max3_u32 v9, v241, v242, v9\n"
    "v_max_u32 v9, v243, v9\n"
    "v_cmp_eq_u32 vcc, -1, v9\n"
    "s_cmp_gt_u32 s53, 1024\n"
    "s_cbranch_scc1 Lk_Hr_41\n"
    "s_cbranch_vccnz Lk_Hl_76\n"
    "s_add_u32 s50, s42, 32768\n"
    "buffer_load_dwordx4 v[116:119], v2, s[12:15], s50 offen offset:0 sc1\n"
    "buffer_load_dwordx4 v[120:123], v2, s[12:15], s50 offen offset:256 sc1\n"
    "buffer_load_dwordx4 v[124:127], v2, s[12:15], s50 offen offset:512 sc1\n"
    "buffer_load_dwordx4 v[128:131], v2, s[12:15], s50 offen offset:768 sc1\n"
    "s_add_u32 s50, s42, 36864\n"
    "buffer_load_dwordx4 v[164:167], v2, s[12:15], s50 offen offset:0 sc1\n"
    "buffer_load_dwordx4 v[168:171], v2, s[12:15], s50 offen offset:256 sc1\n"
    "buffer_load_dwordx4 v[172:175], v2, s[12:15], s50 offen offset:512 sc1\n"
    "buffer_load_dwordx4 v[176:179], v2, s[12:15], s50 offen offset:768 sc1\n"
    "s_add_u32 s50, s42, 40960\n"
    "buffer_load_dwordx4 v[180:183], v2, s[12:15], s50 offen offset:0 sc1\n"
    "buffer_load_dwordx4 v[184:187], v2, s[12:15], s50 offen offset:256 sc1\n"
    "buffer_load_dwordx4 v[188:191], v2, s[12:15], s50 offen offset:512 sc1\n"
    "buffer_load_dwordx4 v[192:195], v2, s[12:15], s50 offen offset:768 sc1\n"
    "s_nop 1\n"
    "s_branch Lk_Hr_41\n"
    "Lk_resf_42:\n"
    "s_nop 7\n"
    "s_mov_b32 s53, 0\n"
    "Lk_resl_77:\n"
    "s_sleep 3\n"
    "buffer_load_dwordx4 v[244:247], v12, s[12:15], s36 offen sc1\n"
    "buffer_load_dwordx4 v[248:251], v12, s[12:15], s37 offen sc1\n"
    "s_add_u32 s53, s53, 1\n"
    "s_waitcnt vmcnt(0)\n"
    "v_max3_u32 v9, v244, v245, v246\n"
    "v_max3_u32 v9, v247, v248, v9\n"
    "v_max3_u32 v9, v249, v250, v9\n"
    "v_max_u32 v9, v251, v9\n"
    "v_cmp_eq_u32 vcc, -1, v9\n"
    "s_cmp_gt_u32 s53, 1024\n"
    "s_cbranch_scc1 Lk_resr_43\n"
    "s_cbranch_vccnz Lk_resl_77\n"
    "s_nop 1\n"
    "s_branch Lk_resr_43\n"
    "Lk_p4redo:\n"
    "s_waitcnt vmcnt(0) lgkmcnt(0)\n"
    "s_add_u32 s9, s9, 1\n"
    "s_cmp_gt_u32 s9, 8\n"
    "s_cbranch_scc1 Lk_p4ok\n"
    "s_sleep 32\n"
    "s_add_u32 s49, s42, 0\n"
    "buffer_load_dwordx4 v[164:167], v2, s[12:15], s49 offen offset:0 sc1\n"
    "buffer_load_dwordx4 v[168:171], v2, s[12:15], s49 offen offset:256 sc1\n"
    "buffer_load_dwordx4 v[172:175], v2, s[12:15], s49 offen offset:512 sc1\n"
    "buffer_load_dwordx4 v[176:179], v2, s[12:15], s49 offen offset:768 sc1\n"
    "s_add_u32 s49, s42, 4096\n"
    "buffer_load_dwordx4 v[180:183], v2, s[12:15], s49 offen offset:0 sc1\n"
    "buffer_load_dwordx4 v[184:187], v2, s[12:15], s49 offen offset:256 sc1\n"
    "buffer_load_dwordx4 v[188:191], v2, s[12:15], s49 offen offset:512 sc1\n"
    "buffer_load_dwordx4 v[192:195], v2, s[12:15], s49 offen offset:768 sc1\n"
    "s_add_u32 s49, s42, 8192\n"
    "buffer_load_dwordx4 v[196:199], v2, s[12:15], s49 offen offset:0 sc1\n"
    "buffer_load_dwordx4 v[200:203], v2, s[12:15], s49 offen offset:256 sc1\n"
    "buffer_load_dwordx4 v[204:207], v2, s[12:15], s49 offen offset:512 sc1\n"
    "buffer_load_dwordx4 v[208:211], v2, s[12:15], s49 offen offset:768 sc1\n"
    "s_add_u32 s49, s42, 12288\n"
    "buffer_load_dwordx4 a[224:227], v2, s[12:15], s49 offen offset:0 sc1\n"
    "buffer_load_dwordx4 a[228:231], v2, s[12:15], s49 offen offset:256 sc1\n"
    "buffer_load_dwordx4 a[232:235], v2, s[12:15], s49 offen offset:512 sc1\n"
    "buffer_load_dwordx4 a[236:239], v2, s[12:15], s49 offen offset:768 sc1\n"
    "s_waitcnt vmcnt(0)\n"
    "s_branch Lk_p4\n"
    "Lk_hff_44:\n"
    "s_nop 7\n"
    "s_mov_b32 s53, 0\n"
    "Lk_hfl_78:\n"
    "s_sleep 3\n"
    "buffer_load_dword v244, v245, s[12:15], 0 offen sc1\n"
    "s_add_u32 s53, s53, 1\n"
    "s_waitcnt vmcnt(0)\n"
    "v_cmp_ne_u32 vcc, s7, v244\n"
    "s_cmp_gt_u32 s53, 1024\n"
    "s_cbranch_scc1 Lk_hfr_45\n"
    "s_cbranch_vccnz Lk_hfl_78\n"
    "s_nop 1\n"
    "s_branch Lk_hfr_45\n"
    "Lk_hhf_46:\n"
    "s_nop 7\n"
    "s_mov_b32 s53, 0\n"
    "Lk_hhl_79:\n"
    "s_sleep 3\n"
    "s_add_u32 s50, s41, 0\n"
    "buffer_load_dwordx4 v[148:151], v2, s[12:15], s50 offen offset:0 sc1\n"
    "buffer_load_dwordx4 v[152:155], v2, s[12:15], s50 offen offset:256 sc1\n"
    "buffer_load_dwordx4 v[156:159], v2, s[12:15], s50 offen offset:512 sc1\n"
    "buffer_load_dwordx4 v[160:163], v2, s[12:15], s50 offen offset:768 sc1\n"
    "s_add_u32 s53, s53, 1\n"
    "s_waitcnt vmcnt(0)\n"
    "v_mov_b32 v228, v148\n"
    "v_mov_b32 v229, v152\n"
    "v_mov_b32 v230, v156\n"
    "v_mov_b32 v231, v160\n"
    "v_mov_b32 v232, v149\n"
    "v_mov_b32 v233, v153\n"
    "v_mov_b32 v234, v157\n"
    "v_mov_b32 v235, v161\n"
    "v_mov_b32 v236, v150\n"
    "v_mov_b32 v237, v154\n"
    "v_mov_b32 v238, v158\n"
    "v_mov_b32 v239, v162\n"
    "v_mov_b32 v240, v151\n"
    "v_mov_b32 v241, v155\n"
    "v_mov_b32 v242, v159\n"
    "v_mov_b32 v243, v163\n"
    "v_max3_u32 v9, v228, v229, v230\n"
    "v_max3_u32 v9, v231, v232, v9\n"
    "v_max3_u32 v9, v233, v234, v9\n"
    "v_max3_u32 v9, v235, v236, v9\n"
    "v_max3_u32 v9, v237, v238, v9\n"
    "v_max3_u32 v9, v239, v240, v9\n"
    "v_max3_u32 v9, v241, v242, v9\n"
    "v_max_u32 v9, v243, v9\n"
    "v_cmp_eq_u32 vcc, -1, v9\n"
    "s_cmp_gt_u32 s53, 1024\n"
    "s_cbranch_scc1 Lk_hhr_47\n"
    "s_cbranch_vccnz Lk_hhl_79\n"
    "s_nop 1\n"
    "s_branch Lk_hhr_47\n"
    "Lk_hhf_48:\n"
    "s_nop 7\n"
    "s_mov_b32 s53, 0\n"
    "Lk_hhl_80:\n"
    "s_sleep 3\n"
    "s_add_u32 s50, s41, 4096\n"
    "buffer_load_dwordx4 v[164:167], v2, s[12:15], s50 offen offset:0 sc1\n"
    "buffer_load_dwordx4 v[168:171], v2, s[12:15], s50 offen offset:256 sc1\n"
    "buffer_load_dwordx4 v[172:175], v2, s[12:15], s50 offen offset:512 sc1\n"
    "buffer_load_dwordx4 v[176:179], v2, s[12:15], s50 offen offset:768 sc1\n"
    "s_add_u32 s53, s53, 1\n"
    "s_waitcnt vmcnt(0)\n"
    "v_mov_b32 v228, v164\n"
    "v_mov_b32 v229, v168\n"
    "v_mov_b32 v230, v172\n"
    "v_mov_b32 v231, v176\n"
    "v_mov_b32 v232, v165\n"
    "v_mov_b32 v233, v169\n"
    "v_mov_b32 v234, v173\n"
    "v_mov_b32 v235, v177\n"
    "v_mov_b32 v236, v166\n"
    "v_mov_b32 v237, v170\n"
    "v_mov_b32 v238, v174\n"
    "v_mov_b32 v239, v178\n"
    "v_mov_b32 v240, v167\n"
    "v_mov_b32 v241, v171\n"
    "v_mov_b32 v242, v175\n"
    "v_mov_b32 v243, v179\n"
    "v_max3_u32 v9, v228, v229, v230\n"
    "v_max3_u32 v9, v231, v232, v9\n"
    "v_max3_u32 v9, v233, v234, v9\n"
    "v_max3_u32 v9, v235, v236, v9\n"
    "v_max3_u32 v9, v237, v238, v9\n"
    "v_max3_u32 v9, v239, v240, v9\n"
    "v_max3_u32 v9, v241, v242, v9\n"
    "v_max_u32 v9, v243, v9\n"
    "v_cmp_eq_u32 vcc, -1, v9\n"
    "s_cmp_gt_u32 s53, 1024\n"
    "s_cbranch_scc1 Lk_hhr_49\n"
    "s_cbranch_vccnz Lk_hhl_80\n"
    "s_nop 1\n"
    "s_branch Lk_hhr_49\n"
    "Lk_hhf_50:\n"
    "s_nop 7\n"
    "s_mov_b32 s53, 0\n"
    "Lk_hhl_81:\n"
    "s_sleep 3\n"
    "s_add_u32 s50, s41, 8192\n"
    "buffer_load_dwordx4 v[180:183], v2, s[12:15], s50 offen offset:0 sc1\n"
    "buffer_load_dwordx4 v[184:187], v2, s[12:15], s50 offen offset:256 sc1\n"
    "buffer_load_dwordx4 v[188:191], v2, s[12:15], s50 offen offset:512 sc1\n"
    "buffer_load_dwordx4 v[192:195], v2, s[12:15], s50 offen offset:768 sc1\n"
    "s_add_u32 s53, s53, 1\n"
    "s_waitcnt vmcnt(0)\n"
    "v_mov_b32 v228, v180\n"
    "v_mov_b32 v229, v184\n"
    "v_mov_b32 v230, v188\n"
    "v_mov_b32 v231, v192\n"
    "v_mov_b32 v232, v181\n"
    "v_mov_b32 v233, v185\n"
    "v_mov_b32 v234, v189\n"
    "v_mov_b32 v235, v193\n"
    "v_mov_b32 v236, v182\n"
    "v_mov_b32 v237, v186\n"
    "v_mov_b32 v238, v190\n"
    "v_mov_b32 v239, v194\n"
    "v_mov_b32 v240, v183\n"
    "v_mov_b32 v241, v187\n"
    "v_mov_b32 v242, v191\n"
    "v_mov_b32 v243, v195\n"
    "v_max3_u32 v9, v228, v229, v230\n"
    "v_max3_u32 v9, v231, v232, v9\n"
    "v_max3_u32 v9, v233, v234, v9\n"
    "v_max3_u32 v9, v235, v236, v9\n"
    "v_max3_u32 v9, v237, v238, v9\n"
    "v_max3_u32 v9, v239, v240, v9\n"
    "v_max3_u32 v9, v241, v242, v9\n"
    "v_max_u32 v9, v243, v9\n"
    "v_cmp_eq_u32 vcc, -1, v9\n"
    "s_cmp_gt_u32 s53, 1024\n"
    "s_cbranch_scc1 Lk_hhr_51\n"
    "s_cbranch_vccnz Lk_hhl_81\n"
    "s_nop 1\n"
    "s_branch Lk_hhr_51\n"
    "Lk_hhf_52:\n"
    "s_nop 7\n"
    "s_mov_b32 s53, 0\n"
    "Lk_hhl_82:\n"
    "s_sleep 3\n"
    "s_add_u32 s50, s41, 12288\n"
    "buffer_load_dwordx4 v[196:199], v2, s[12:15], s50 offen offset:0 sc1\n"
    "buffer_load_dwordx4 v[200:203], v2, s[12:15], s50 offen offset:256 sc1\n"
    "buffer_load_dwordx4 v[204:207], v2, s[12:15], s50 offen offset:512 sc1\n"
    "buffer_load_dwordx4 v[208:211], v2, s[12:15], s50 offen offset:768 sc1\n"
    "s_add_u32 s53, s53, 1\n"
    "s_waitcnt vmcnt(0)\n"
    "v_mov_b32 v228, v196\n"
    "v_mov_b32 v229, v200\n"
    "v_mov_b32 v230, v204\n"
    "v_mov_b32 v231, v208\n"
    "v_mov_b32 v232, v197\n"
    "v_mov_b32 v233, v201\n"
    "v_mov_b32 v234, v205\n"
    "v_mov_b32 v235, v209\n"
    "v_mov_b32 v236, v198\n"
    "v_mov_b32 v237, v202\n"
    "v_mov_b32 v238, v206\n"
    "v_mov_b32 v239, v210\n"
    "v_mov_b32 v240, v199\n"
    "v_mov_b32 v241, v203\n"
    "v_mov_b32 v242, v207\n"
    "v_mov_b32 v243, v211\n"
    "v_max3_u32 v9, v228, v229, v230\n"
    "v_max3_u32 v9, v231, v232, v9\n"
    "v_max3_u32 v9, v233, v234, v9\n"
    "v_max3_u32 v9, v235, v236, v9\n"
    "v_max3_u32 v9, v237, v238, v9\n"
    "v_max3_u32 v9, v239, v240, v9\n"
    "v_max3_u32 v9, v241, v242, v9\n"
    "v_max_u32 v9, v243, v9\n"
    "v_cmp_eq_u32 vcc, -1, v9\n"
    "s_cmp_gt_u32 s53, 1024\n"
    "s_cbranch_scc1 Lk_hhr_53\n"
    "s_cbranch_vccnz Lk_hhl_82\n"
    "s_nop 1\n"
    "s_branch Lk_hhr_53\n"
    "Lk_hpf_56:\n"
    "s_nop 7\n"
    "s_mov_b32 s53, 0\n"
    "Lk_hpl_83:\n"
    "s_sleep 3\n"
    "s_sleep 8\n"
    "buffer_load_dword v252, v253, s[12:15], s42 offen offset:256 sc1\n"
    "s_add_u32 s53, s53, 1\n"
    "s_waitcnt vmcnt(0)\n"
    "v_cmp_eq_u32 vcc, -1, v252\n"
    "s_cmp_gt_u32 s53, 1024\n"
    "s_cbranch_scc1 Lk_hpr_57\n"
    "s_cbranch_vccnz Lk_hpl_83\n"
    "s_nop 1\n"
    "s_branch Lk_hpr_57\n"
    "Lk_end:\n"

    :: "s"(__builtin_amdgcn_kernarg_segment_ptr()), "s"(blockIdx.x), "v"(threadIdx.x), "s"((unsigned)(size_t)lds)
    :
      "v1", "v2", "v3", "v4", "v5", "v6", "v7", "v8", "v9", "v10", "v11", "v12", "v13", "v14", "v15", "v16", "v17", "v18", "v19", "v20", "v21", "v22", "v23", "v24",
      "v25", "v26", "v27", "v28", "v29", "v30", "v31", "v32", "v33", "v34", "v35", "v36", "v37", "v38", "v39", "v40", "v41", "v42", "v43", "v44", "v45", "v46", "v47", "v48",
      "v49", "v50", "v51", "v52", "v53", "v54", "v55", "v56", "v57", "v58", "v59", "v60", "v61", "v62", "v63", "v64", "v65", "v66", "v67", "v68", "v69", "v70", "v71", "v72",
      "v73", "v74", "v75", "v76", "v77", "v78", "v79", "v80", "v81", "v82", "v83", "v84", "v85", "v86", "v87", "v88", "v89", "v90", "v91", "v92", "v93", "v94", "v95", "v96",
      "v97", "v98", "v99", "v100", "v101", "v102", "v103", "v104", "v105", "v106", "v107", "v108", "v109", "v110", "v111", "v112", "v113", "v114", "v115", "v116", "v117", "v118", "v119", "v120",
      "v121", "v122", "v123", "v124", "v125", "v126", "v127", "v128", "v129", "v130", "v131", "v132", "v133", "v134", "v135", "v136", "v137", "v138", "v139", "v140", "v141", "v142", "v143", "v144",
      "v145", "v146", "v147", "v148", "v149", "v150", "v151", "v152", "v153", "v154", "v155", "v156", "v157", "v158", "v159", "v160", "v161", "v162", "v163", "v164", "v165", "v166", "v167", "v168",
      "v169", "v170", "v171", "v172", "v173", "v174", "v175", "v176", "v177", "v178", "v179", "v180", "v181", "v182", "v183", "v184", "v185", "v186", "v187", "v188", "v189", "v190", "v191", "v192",
      "v193", "v194", "v195", "v196", "v197", "v198", "v199", "v200", "v201", "v202", "v203", "v204", "v205", "v206", "v207", "v208", "v209", "v210", "v211", "v212", "v213", "v214", "v215", "v216",
      "v217", "v218", "v219", "v220", "v221", "v222", "v223", "v224", "v225", "v226", "v227", "v228", "v229", "v230", "v231", "v232", "v233", "v234", "v235", "v236", "v237", "v238", "v239", "v240",
      "v241", "v242", "v243", "v244", "v245", "v246", "v247", "v248", "v249", "v250", "v251", "v252", "v253", "v254", "v255", "a0", "a1", "a2", "a3", "a4", "a5", "a6", "a7", "a8",
      "a9", "a10", "a11", "a12", "a13", "a14", "a15", "a16", "a17", "a18", "a19", "a20", "a21", "a22", "a23", "a24", "a25", "a26", "a27", "a28", "a29", "a30", "a31", "a32",
      "a33", "a34", "a35", "a36", "a37", "a38", "a39", "a40", "a41", "a42", "a43", "a44", "a45", "a46", "a47", "a48", "a49", "a50", "a51", "a52", "a53", "a54", "a55", "a56",
      "a57", "a58", "a59", "a60", "a61", "a62", "a63", "a64", "a65", "a66", "a67", "a68", "a69", "a70", "a71", "a72", "a73", "a74", "a75", "a76", "a77", "a78", "a79", "a80",
      "a81", "a82", "a83", "a84", "a85", "a86", "a87", "a88", "a89", "a90", "a91", "a92", "a93", "a94", "a95", "a96", "a97", "a98", "a99", "a100", "a101", "a102", "a103", "a104",
      "a105", "a106", "a107", "a108", "a109", "a110", "a111", "a112", "a113", "a114", "a115", "a116", "a117", "a118", "a119", "a120", "a121", "a122", "a123", "a124", "a125", "a126", "a127", "a128",
      "a129", "a130", "a131", "a132", "a133", "a134", "a135", "a136", "a137", "a138", "a139", "a140", "a141", "a142", "a143", "a144", "a145", "a146", "a147", "a148", "a149", "a150", "a151", "a152",
      "a153", "a154", "a155", "a156", "a157", "a158", "a159", "a160", "a161", "a162", "a163", "a164", "a165", "a166", "a167", "a168", "a169", "a170", "a171", "a172", "a173", "a174", "a175", "a176",
      "a177", "a178", "a179", "a180", "a181", "a182", "a183", "a184", "a185", "a186", "a187", "a188", "a189", "a190", "a191", "a192", "a193", "a194", "a195", "a196", "a197", "a198", "a199", "a200",
      "a201", "a202", "a203", "a204", "a205", "a206", "a207", "a208", "a209", "a210", "a211", "a212", "a213", "a214", "a215", "a216", "a217", "a218", "a219", "a220", "a221", "a222", "a223", "a224",
      "a225", "a226", "a227", "a228", "a229", "a230", "a231", "a232", "a233", "a234", "a235", "a236", "a237", "a238", "a239", "a240", "a241", "a242", "a243", "a244", "a245", "a246", "a247", "a248",
      "a249", "a250", "a251", "a252", "a253", "a254", "a255", "s4", "s5", "s6", "s7", "s8", "s9", "s10", "s11", "s12", "s13", "s14", "s15", "s16", "s17", "s18", "s19", "s20",
      "s21", "s22", "s23", "s24", "s25", "s26", "s27", "s28", "s29", "s30", "s31", "s33", "s34", "s35", "s36", "s37", "s38", "s39", "s40", "s41", "s42", "s43", "s44", "s45",
      "s46", "s47", "s48", "s49", "s50", "s51", "s52", "s53", "s54", "s55", "s56", "s57", "s58", "s59", "s60", "s61", "s62", "s63", "s64", "s65", "s66", "s67", "s68", "s69",
      "s70", "s71", "s72", "s73", "s74", "s75", "s76", "s77", "s78", "s79", "s80", "s81", "s82", "s83", "s84", "s85", "s86", "s87", "s88", "s89", "s90", "s91", "s92", "s93",
      "s94", "s95", "s96", "s97", "s98", "s99", "vcc", "scc", "memory"
  );
}

extern "C" void kernel_launch(void* const* d_in, const int* in_sizes, int n_in, void* d_out, int out_size, void* d_ws,
                              size_t ws_size, hipStream_t stream) {
  const unsigned F = 0x00020000u;
  KArgs k;
  k.x = {d_in[0], 2048u * 512u * 2u, F};
  k.ws = {d_ws, 33554432u, F};
  k.lar = {d_in[1], 512u * 128u * 2u, F};
  k.aim = {d_in[2], 512u * 128u * 2u, F};
  k.cr = {d_in[3], 512u * 128u * 2u, F};
  k.ci = {d_in[4], 512u * 128u * 2u, F};
  k.w1 = {d_in[8], 512u * 1792u * 2u, F};
  k.w3 = d_in[9];
  k.w2 = {d_in[10], 512u * 1792u * 2u, F};
  k.out = d_out;
  k.ldt = d_in[5];
  k.dsk = d_in[6];
  s4d_swiglu_fused<<<256, 256, 0, stream>>>(k);
}
```
